```python
import torch

def _softmax(x, axis=-1):
    return torch.softmax(x, dim=axis)
def _gelu(x):
    return torch.nn.functional.gelu(x, approximate="tanh")
def _silu(x):
    return torch.nn.functional.silu(x)
def _sigmoid(x):
    return torch.sigmoid(x)
def _layernorm(x, axis=-1, eps=1e-5):
    m = x.mean(dim=axis, keepdim=True)
    v = x.var(dim=axis, keepdim=True, unbiased=False)
    return (x - m) / torch.sqrt(v + eps)
def _scatter_add(tgt, idx, src, axis=0):
    return tgt.clone().index_add_(axis, idx, src)
def _segment_sum(src, seg, n):
    out = torch.zeros((n,) + src.shape[1:], dtype=src.dtype)
    return out.index_add_(0, seg, src)
def _pad(x, pw):
    flat = tuple(v for pair in reversed(pw) for v in pair)
    return torch.nn.functional.pad(x, flat)

kernel_name = 'gated_mix__mha_block__linear_attn_block__prompt'

def setup_inputs():

    _w = (lambda a: (lambda t: t.to(torch.bfloat16) if t.is_floating_point() else t)(torch.as_tensor(a)))
    def _A_setup():
        B, S, D, H = 1, 2048, 576, 32
        return {
            "x": _w((lambda *s: torch.randn(*s, dtype=torch.bfloat16) * 0.05)(B, S, D)),
            "wqkv": _w((lambda *s: torch.randn(*s, dtype=torch.bfloat16) * 0.8)(D, 3 * D)),
            "wo": _w((lambda *s: torch.randn(*s, dtype=torch.bfloat16) * 0.05)(D, D)),
        }
    _A_in = _A_setup()

    def _B_setup():
        B, S, D = 1, 2048, 576
        return {
            "x": _w((lambda *s: torch.randn(*s, dtype=torch.bfloat16) * 0.05)(B, S, D)),
            "wqkv": _w((lambda *s: torch.randn(*s, dtype=torch.bfloat16) * 0.8)(D, 3 * D)),
            "wo": _w((lambda *s: torch.randn(*s, dtype=torch.bfloat16) * 0.1)(D, D)),
        }
    _B_in = _B_setup()

    return {
        "A_x": _A_in["x"],
        "A_wqkv": _A_in["wqkv"],
        "A_wo": _A_in["wo"],
        "B_x": _B_in["x"],
        "B_wqkv": _B_in["wqkv"],
        "B_wo": _B_in["wo"],
        "gate_w": _w((lambda *s: torch.randn(*s, dtype=torch.bfloat16) * 0.05)(576) * 16.0),
    }

def reference(A_x, A_wqkv, A_wo, B_x, B_wqkv, B_wo, gate_w):
    def _A_ref(x, wqkv, wo):
        B, S, D = x.shape
        H = 32
        Dh = D // H
        qkv = (lambda x, *d: x.permute(*d))((x @ wqkv).reshape(B, S, 3, H, Dh), 2, 0, 3, 1, 4)
        q, k, v = qkv[0], qkv[1], qkv[2]
        scores = torch.einsum("bhqd,bhkd->bhqk", q, k) * (Dh ** -0.5)
        p = _softmax(scores, axis=-1)
        o = torch.einsum("bhqk,bhkd->bhqd", p, v)
        return (lambda x, *d: x.permute(*d))(o, 0, 2, 1, 3).reshape(B, S, D) @ wo

    def _B_ref(x, wqkv, wo):
        B, S, D = x.shape
        H = 32; Dh = D // H
        qkv = (lambda x, *d: x.permute(*d))((x @ wqkv).reshape(B, S, 3, H, Dh), 2, 0, 3, 1, 4)
        q, k, v = qkv[0], qkv[1], qkv[2]
        phi = lambda t: torch.where(t > 0, t + 1, torch.exp(t)) + 1e-6
        pq, pk = phi(q), phi(k)
        kv = torch.einsum("bhsd,bhse->bhde", pk, v)
        z = torch.einsum("bhsd->bhd", pk)
        num = torch.einsum("bhqd,bhde->bhqe", pq, kv)
        den = torch.einsum("bhqd,bhd->bhq", pq, z)[..., None] + 1e-6
        o = num / den
        return (lambda x, *d: x.permute(*d))(o, 0, 2, 1, 3).reshape(B, S, D) @ wo

    x = A_x
    g = _sigmoid((x * gate_w).sum(-1, keepdims=True))
    a = _A_ref(x, A_wqkv, A_wo)
    b = _B_ref(x, B_wqkv, B_wo)
    return g * a + (1 - g) * b
```

```cpp
#include <hip/hip_runtime.h>
#include <stdint.h>

typedef __attribute__((ext_vector_type(8))) __bf16 bf16x8;
typedef __attribute__((ext_vector_type(2))) __bf16 bf16x2;
typedef __attribute__((ext_vector_type(4))) short s16x4;
typedef __attribute__((ext_vector_type(2))) float f32x2;
typedef __attribute__((ext_vector_type(4))) float f32x4;
typedef __attribute__((ext_vector_type(16))) float f32x16;
typedef __attribute__((ext_vector_type(4))) unsigned u32x4;
typedef __attribute__((ext_vector_type(2))) unsigned u32x2;
#define LAS __attribute__((address_space(3)))
#define DI __device__ __forceinline__

constexpr unsigned CHUNK = 37888;
constexpr unsigned KV_HEAD = 4 * CHUNK;
constexpr size_t OFF_KV = 0;
constexpr size_t OFF_Q1 = OFF_KV + 32 * (size_t)KV_HEAD;
constexpr size_t OFF_Q2 = OFF_Q1 + 2 * 32 * 65536;
constexpr size_t OFF_KMAX = OFF_Q2 + 2 * 32 * 8192;
constexpr size_t OFF_GATE = OFF_KMAX + 32 * 64 * 4;
constexpr size_t OFF_KVP = OFF_GATE + 2048 * 4;
constexpr size_t OFF_O = OFF_KVP + 32 * 16 * 384 * 4;

constexpr float QSCALE = 0.23570226039551584f * 1.4426950408889634f;

DI unsigned pk(float a, float b) { bf16x2 v = __builtin_convertvector((f32x2){a, b}, bf16x2); return __builtin_bit_cast(unsigned, v); }
DI float bflo(unsigned u) { return __uint_as_float(u << 16); }
DI float bfhi(unsigned u) { return __uint_as_float(u & 0xffff0000u); }
DI float phi(float t) { return (t > 0.f ? t + 1.f : __expf(t)) + 1e-6f; }
DI void glds16(unsigned voff, const void* sbase, unsigned lds_dst) {
    unsigned keep;
    asm volatile("s_mov_b32 %0, m0\n\ts_mov_b32 m0, %3\n\ts_nop 0\n\tglobal_load_lds_dwordx4 %1, %2\n\ts_mov_b32 m0, %0"
                 : "=&s"(keep) : "v"(voff), "s"(sbase), "s"(lds_dst) : "memory");
}
#define WAITV_BAR(n) asm volatile("s_waitcnt vmcnt(" #n ") lgkmcnt(0)\n\ts_barrier" ::: "memory")
DI bf16x8 ldsr128(unsigned a) { return *(const LAS bf16x8*)(size_t)a; }
DI s16x4 trr(unsigned a) { return __builtin_amdgcn_ds_read_tr16_b64_v4i16((LAS s16x4*)(size_t)a); }
DI bf16x8 trfrag(unsigned a, unsigned rowb) {
    s16x4 lo = trr(a), hi = trr(a + rowb);
    return __builtin_bit_cast(bf16x8, __builtin_shufflevector(lo, hi, 0, 1, 2, 3, 4, 5, 6, 7));
}
DI f32x4 mfma16(bf16x8 a, bf16x8 b, f32x4 c) { return __builtin_amdgcn_mfma_f32_16x16x32_bf16(a, b, c, 0, 0, 0); }
DI float dpp_xor1(float v) { return __int_as_float(__builtin_amdgcn_mov_dpp(__float_as_int(v), 0xB1, 0xf, 0xf, true)); }

constexpr unsigned ST1 = 44032;

DI u32x4 fm_pack(f32x4 t0, f32x4 t1) {
    unsigned a0 = pk(t0[0], t0[1]), a1 = pk(t0[2], t0[3]), b0 = pk(t1[0], t1[1]), b1 = pk(t1[2], t1[3]);
    auto r0 = __builtin_amdgcn_permlane16_swap(a0, b0, false, false);
    auto r1 = __builtin_amdgcn_permlane16_swap(a1, b1, false, false);
    return (u32x4){r0[0], r1[0], r0[1], r1[1]};
}
DI u32x4 tm_pack(f32x4 t0, f32x4 t1) { return (u32x4){pk(t0[0], t0[1]), pk(t0[2], t0[3]), pk(t1[0], t1[1]), pk(t1[2], t1[3])}; }

template <bool ISB>
DI void k1_body(const char* __restrict__ X, const char* __restrict__ W, char* __restrict__ ws, char* smem, int quad, int r) {
    const int tid = threadIdx.x, lane = tid & 63, w = __builtin_amdgcn_readfirstlane(tid >> 6);
    const unsigned lds0 = (unsigned)(size_t)smem;
    const int rg = w & 3, pr = w >> 2, g = lane >> 4, m = lane & 15;
    unsigned xo[2], wo[4];
#pragma unroll
    for (int i = 0; i < 2; i++) { int row = (w + 8 * i) * 8 + (lane >> 3); xo[i] = (unsigned)(r * 128 + row) * 1152u + (((lane & 7) ^ (row & 7)) << 4); }
#pragma unroll
    for (int i = 0; i < 4; i++) {
        unsigned o = (w + 8 * i) * 1024u + lane * 16u; if (o >= 27648u) o = 0;
        unsigned lr = o / 432u, pc = (o % 432u) >> 4;
        unsigned k = (lr & ~7u) | ((lr & 1u) << 2) | ((lr >> 1) & 3u);
        wo[i] = k * 3456u + (pc / 9u) * 1152u + quad * 144u + (pc % 9u) * 16u;
    }
    auto stage = [&](int s) {
        const unsigned dst = lds0 + (s % 3) * ST1 + w * 1024;
        const char* xb = X + s * 128; const char* wb = W + (size_t)s * 64 * 3456;
        glds16(xo[0], xb, dst); glds16(xo[1], xb, dst + 8192);
        glds16(wo[0], wb, dst + 16384); glds16(wo[1], wb, dst + 16384 + 8192); glds16(wo[2], wb, dst + 16384 + 16384);
        if (w < 3) glds16(wo[3], wb, dst + 16384 + 24576);
    };
    stage(0); stage(1);
    const int q4 = m >> 2, p = m & 3;
    const unsigned aW = lds0 + 16384 + (g * 8 + q4 * 2) * 432 + pr * 72 + 8 * p;
    const unsigned aR = lds0 + 16384 + (g * 8 + q4 * 2) * 432 + pr * 72 + (p < 2 ? p : 2) * 144 + 32;
    const unsigned aX = lds0 + (32 * rg + m) * 128 + ((g ^ (m & 7)) << 4);
    f32x4 acc[7][2];
#pragma unroll
    for (int t = 0; t < 7; t++) { acc[t][0] = (f32x4){0, 0, 0, 0}; acc[t][1] = (f32x4){0, 0, 0, 0}; }
#pragma unroll
    for (int s = 0; s < 9; s++) {
        if (s < 8) WAITV_BAR(5); else WAITV_BAR(0);
        if (s + 2 < 9) stage(s + 2);
        const unsigned sb = (s % 3) * ST1;
#pragma unroll
        for (int ks = 0; ks < 2; ks++) {
            bf16x8 x0 = ldsr128((aX ^ (ks * 64)) + sb), x1 = ldsr128((aX ^ (ks * 64)) + sb + 2048);
#pragma unroll
            for (int t = 0; t < 7; t++) {
                const unsigned a = (t < 6 ? aW + (t >> 1) * 144 + (t & 1) * 40 : aR) + sb + ks * 13824;
                bf16x8 wf = trfrag(a, 432);
                const bool fm = ISB ? (t < 2) : (t < 4);
                if (fm) { acc[t][0] = mfma16(wf, x0, acc[t][0]); acc[t][1] = mfma16(wf, x1, acc[t][1]); }
                else    { acc[t][0] = mfma16(x0, wf, acc[t][0]); acc[t][1] = mfma16(x1, wf, acc[t][1]); }
            }
        }
    }
    const int h0 = 4 * quad + 2 * pr, T = 4 * r + rg;
    const int rho = ((m >> 2) << 3) + ((g & 1) << 2) + (m & 3);
    const unsigned fmoff = (g >> 1) * 512 + rho * 16;
    const bool odd = lane & 1;
    f32x4 r0 = acc[6][0], r1 = acc[6][1];
    if (!ISB) {
        char* kvb = ws + OFF_KV + (size_t)h0 * KV_HEAD + (T >> 4) * CHUNK;
        const int u = T & 15;
#pragma unroll
        for (int j = 0; j < 2; j++) {
            *(u32x4*)(ws + OFF_Q1 + (size_t)(h0 + j) * 65536 + T * 1024 + fmoff) = fm_pack(acc[j][0] * QSCALE, acc[j][1] * QSCALE);
            *(u32x4*)(kvb + (size_t)j * KV_HEAD + u * 1024 + fmoff) = fm_pack(acc[2 + j][0], acc[2 + j][1]);
            *(u32x4*)(kvb + (size_t)j * KV_HEAD + 18432 + u * 1216 + g * 304 + m * 16) = tm_pack(acc[4 + j][0], acc[4 + j][1]);
        }
        if (m >= 8 && m < 14) {
            u32x4 v = tm_pack(r0, r1);
            int j = (m - 8) >> 1, row = 16 + (m & 1);
            if (m >= 12) { v = (u32x4){0x3f803f80u, 0x3f803f80u, 0x3f803f80u, 0x3f803f80u}; j = m - 12; row = 18; }
            *(u32x4*)(kvb + (size_t)j * KV_HEAD + 18432 + u * 1216 + g * 304 + row * 16) = v;
        }
        float km[2];
#pragma unroll
        for (int j = 0; j < 2; j++) {
            f32x4 a = acc[2 + j][0], b = acc[2 + j][1];
            float sa = a[0] * a[0] + a[1] * a[1] + a[2] * a[2] + a[3] * a[3], sb2 = b[0] * b[0] + b[1] * b[1] + b[2] * b[2] + b[3] * b[3];
            sa += __shfl_xor(sa, 16); sb2 += __shfl_xor(sb2, 16); sa += __shfl_xor(sa, 32); sb2 += __shfl_xor(sb2, 32);
            float x = fmaxf(sa, sb2);
            x = fmaxf(x, __shfl_xor(x, 1)); x = fmaxf(x, __shfl_xor(x, 2)); x = fmaxf(x, __shfl_xor(x, 4)); x = fmaxf(x, __shfl_xor(x, 8));
            km[j] = x;
        }
        float rm = 0.f;
#pragma unroll
        for (int i = 0; i < 4; i++) {
            float a = r0[i] * r0[i], b = r1[i] * r1[i];
            a += dpp_xor1(a); b += dpp_xor1(b); rm = fmaxf(rm, fmaxf(a, b));
        }
        rm = fmaxf(rm, __shfl_xor(rm, 16)); rm = fmaxf(rm, __shfl_xor(rm, 32));
        if (lane == 4 || lane == 6) { int j = (lane - 4) >> 1; ((float*)(ws + OFF_KMAX))[(h0 + j) * 64 + T] = (j ? km[1] : km[0]) + rm; }
        if (m < 4) { r0 *= QSCALE; r1 *= QSCALE; }
        u32x4 v;
#pragma unroll
        for (int i = 0; i < 4; i++) { float o = dpp_xor1(odd ? r0[i] : r1[i]); v[i] = odd ? pk(o, r1[i]) : pk(r0[i], o); }
        if (m < 8) {
            const int j = (m >> 1) & 1; const unsigned off = (8 * g + 4 * (m & 1)) * 4;
            char* dst = m < 4 ? ws + OFF_Q2 + (size_t)(h0 + j) * 8192 + T * 128 + off : kvb + (size_t)j * KV_HEAD + 16384 + u * 128 + off;
            *(u32x4*)dst = v;
        }
    } else {
        auto phi4 = [](f32x4 a) { return (f32x4){phi(a[0]), phi(a[1]), phi(a[2]), phi(a[3])}; };
#pragma unroll
        for (int j = 0; j < 2; j++)
            *(u32x4*)(ws + OFF_Q1 + (size_t)(32 + h0 + j) * 65536 + T * 1024 + fmoff) = fm_pack(phi4(acc[j][0]), phi4(acc[j][1]));
        f32x4 p0 = phi4(r0), p1 = phi4(r1);
        u32x4 v;
#pragma unroll
        for (int i = 0; i < 4; i++) { float o = dpp_xor1(odd ? p0[i] : p1[i]); v[i] = odd ? pk(o, p1[i]) : pk(p0[i], o); }
        if (m < 4) *(u32x4*)(ws + OFF_Q2 + (size_t)(32 + h0 + (m >> 1)) * 8192 + T * 128 + (8 * g + 4 * (m & 1)) * 4) = v;
        bf16x8 k0 = __builtin_bit_cast(bf16x8, tm_pack(phi4(acc[2][0]), phi4(acc[2][1])));
        bf16x8 k1 = __builtin_bit_cast(bf16x8, tm_pack(phi4(acc[3][0]), phi4(acc[3][1])));
        bf16x8 kr = __builtin_bit_cast(bf16x8, tm_pack(p0, p1));
        bf16x8 v0 = __builtin_bit_cast(bf16x8, tm_pack(acc[4][0], acc[4][1]));
        bf16x8 v1 = __builtin_bit_cast(bf16x8, tm_pack(acc[5][0], acc[5][1]));
        u32x4 vru = tm_pack(r0, r1);
        if (m >= 12) vru = (u32x4){0x3f803f80u, 0x3f803f80u, 0x3f803f80u, 0x3f803f80u};
        bf16x8 vr = __builtin_bit_cast(bf16x8, vru);
        const f32x4 z4 = {0, 0, 0, 0};
        f32x4 M0 = mfma16(k0, v0, z4), M1 = mfma16(k0, vr, z4), M2 = mfma16(kr, v0, z4), M3 = mfma16(kr, vr, z4);
        f32x4 M4 = mfma16(k1, v1, z4), M5 = mfma16(k1, vr, z4), M6 = mfma16(kr, v1, z4);
        __syncthreads();
        float* red = (float*)smem + w * 768;
        const int e0 = m == 12 ? 18 : 8 + m, e1 = m == 12 ? 18 : 6 + m;
        const bool c0 = m == 8 || m == 9 || m == 12, c1 = m == 10 || m == 11 || m == 12;
#pragma unroll
        for (int i = 0; i < 4; i++) {
            red[m * 18 + 4 * g + i] = M0[i]; red[384 + m * 18 + 4 * g + i] = M4[i];
            if (c0) red[e0 * 18 + 4 * g + i] = M1[i];
            if (c1) red[384 + e1 * 18 + 4 * g + i] = M5[i];
        }
        if (g == 1) {
            red[m * 18 + 16] = M2[0]; red[m * 18 + 17] = M2[1]; red[384 + m * 18 + 16] = M6[2]; red[384 + m * 18 + 17] = M6[3];
            if (c0) { red[e0 * 18 + 16] = M3[0]; red[e0 * 18 + 17] = M3[1]; }
            if (c1) { red[384 + e1 * 18 + 16] = M3[2]; red[384 + e1 * 18 + 17] = M3[3]; }
        }
        __syncthreads();
        const float* rd = (const float*)smem;
#pragma unroll
        for (int it = 0; it < 3; it++) {
            const int n = tid + 512 * it, hh = n / 384, e = n % 384;
            if (e < 342) {
                const float* s4 = rd + (hh >> 1) * 4 * 768 + (hh & 1) * 384 + e;
                ((float*)(ws + OFF_KVP))[((4 * quad + hh) * 16 + r) * 384 + e] = (s4[0] + s4[768]) + (s4[1536] + s4[2304]);
            }
        }
    }
}

__global__ void __launch_bounds__(512, 2) k1_qkv(const char* __restrict__ X, const char* __restrict__ WA, const char* __restrict__ WB,
                                                  const unsigned* __restrict__ gw, char* __restrict__ ws) {
    extern __shared__ __attribute__((aligned(16))) char smem[];
    const int b = blockIdx.x, xcd = b & 7, idx = b >> 3;
    const int br = xcd & 1, quad = ((xcd >> 1) & 1) * 4 + (idx & 3), r = (xcd >> 2) * 8 + (idx >> 2);
    {
        const int lane = threadIdx.x & 63, row = 128 * r + 8 * (br * 8 + quad) + (threadIdx.x >> 6);
        const unsigned* xr = (const unsigned*)(X + (size_t)row * 1152);
        float s = 0.f;
#pragma unroll
        for (int i = 0; i < 5; i++) {
            const int c = lane + 64 * i;
            if (c < 288) { unsigned a = xr[c], q = gw[c]; s += bflo(a) * bflo(q) + bfhi(a) * bfhi(q); }
        }
#pragma unroll
        for (int o = 32; o; o >>= 1) s += __shfl_xor(s, o);
        if (lane == 0) ((float*)(ws + OFF_GATE))[row] = 1.f / (1.f + __expf(-s));
    }
    if (br) k1_body<true>(X, WB, ws, smem, quad, r); else k1_body<false>(X, WA, ws, smem, quad, r);
}

#define EXP(r) "v_exp_f32 v[" r "], v[" r "]\n"
#define ATT_MACROS \
    ".macro PHA t, par\n"                                \
    EXP("64") EXP("65") \
    "v_smfmac_f32_32x32x32_bf16 v[112:127], v[186:189], v[88:95], v190\n" \
    "ds_read_b128 v[186:189], v193 offset:(\\t)*1216+608\n" \
    EXP("66") EXP("67") EXP("68") \
    "v_mfma_f32_32x32x8_bf16 v[80:95], v[180:181], v[172+2*\\par:173+2*\\par], v[144:159]\n" \
    EXP("69") EXP("70") EXP("71") EXP("72") \
    "v_mfma_f32_32x32x16_bf16 v[80:95], v[176:179], v[164:167], v[80:95]\n" \
    ".if \\t == 7\n" \
    "v_add_u32 v191, 37888, v191\n v_add_u32 v192, 37888, v192\n" \
    "ds_read_b128 v[176:179], v191\n" \
    "ds_read2_b32 v[180:181], v192 offset0:0 offset1:32\n" \
    ".else\n" \
    "ds_read_b128 v[176:179], v191 offset:(\\t+1)*1024\n" \
    ".if \\par == 1\n" \
    "ds_read2_b32 v[180:181], v192 offset0:(\\t+1)*32 offset1:(\\t+2)*32\n" \
    ".endif\n" \
    ".endif\n" \
    EXP("73") EXP("74") EXP("75") EXP("76") \
    ".if \\par == 1\n s_waitcnt lgkmcnt(3)\n .else\n s_waitcnt lgkmcnt(2)\n .endif\n" \
    "v_smfmac_f32_32x32x32_bf16 v[96:111], v[182:185], v[64:71], v190\n" \
    EXP("77") EXP("78") EXP("79") \
    "s_nop 2\n" \
    ".endm\n" \
    ".macro PHB t, par\n"                                \
    EXP("80") EXP("81") \
    ".if \\par == 1\n s_waitcnt lgkmcnt(2)\n .else\n s_waitcnt lgkmcnt(1)\n .endif\n" \
    "v_smfmac_f32_32x32x32_bf16 v[96:111], v[186:189], v[72:79], v190\n" \
    EXP("82") EXP("83") EXP("84") \
    "s_waitcnt lgkmcnt(0)\n" \
    "v_mfma_f32_32x32x8_bf16 v[64:79], v[180:181], v[170-2*\\par:171-2*\\par], v[128:143]\n" \
    EXP("85") EXP("86") EXP("87") EXP("88") \
    "v_mfma_f32_32x32x16_bf16 v[64:79], v[176:179], v[160:163], v[64:79]\n" \
    EXP("89") EXP("90") EXP("91") EXP("92") \
    "v_smfmac_f32_32x32x32_bf16 v[112:127], v[182:185], v[80:87], v190\n" \
    ".if \\t == 7\n" \
    "v_add_u32 v193, 37888, v193\n" \
    "ds_read_b128 v[182:185], v193\n" \
    ".else\n" \
    "ds_read_b128 v[182:185], v193 offset:(\\t+1)*1216\n" \
    ".endif\n" \
    EXP("93") EXP("94") EXP("95") \
    "s_nop 2\n" \
    ".endm\n"

DI void attn_loop(f32x16& Oa, f32x16& Ob, float ma, float mb, u32x4 q1a, u32x4 q1b, unsigned q2a, unsigned q2b,
                  unsigned aK, unsigned aK2, unsigned aV) {
    unsigned cnt;
    asm volatile(
        ATT_MACROS
        "ds_read_b128 v[176:179], v191\n"
        "ds_read2_b32 v[180:181], v192 offset0:0 offset1:32\n"
        "ds_read_b128 v[182:185], v193\n"
        "v_mov_b32 v169, 0\n v_mov_b32 v170, 0\n v_mov_b32 v171, v168\n"
        "v_mov_b32 v173, 0\n v_mov_b32 v174, 0\n v_mov_b32 v175, v172\n"
        "v_mov_b32 v190, 0xdddddddd\n"
        ".irp r,129,130,131,132,133,134,135,136,137,138,139,140,141,142,143\n v_mov_b32 v[\\r], v128\n .endr\n"
        ".irp r,145,146,147,148,149,150,151,152,153,154,155,156,157,158,159\n v_mov_b32 v[\\r], v144\n .endr\n"
        ".irp r,88,89,90,91,92,93,94,95,96,97,98,99,100,101,102,103,104,105,106,107,108,109,110,111,112,113,114,115,116,117,118,119,120,121,122,123,124,125,126,127,186,187,188,189\n v_mov_b32 v[\\r], 0\n .endr\n"
        "s_mov_b32 %2, 4\n"
        "s_waitcnt lgkmcnt(1)\n"
        "v_mfma_f32_32x32x8_bf16 v[64:79], v[180:181], v[168:169], v[128:143]\n"
        "v_mfma_f32_32x32x16_bf16 v[64:79], v[176:179], v[160:163], v[64:79]\n"
        "s_nop 15\n"
        "1:\n"
        "PHA 0, 0\n PHB 0, 0\n PHA 1, 1\n PHB 1, 1\n PHA 2, 0\n PHB 2, 0\n PHA 3, 1\n PHB 3, 1\n"
        "PHA 4, 0\n PHB 4, 0\n PHA 5, 1\n PHB 5, 1\n PHA 6, 0\n PHB 6, 0\n PHA 7, 1\n PHB 7, 1\n"
        "s_sub_u32 %2, %2, 1\n"
        "s_cmp_lg_u32 %2, 0\n"
        "s_cbranch_scc1 1b\n"
        "v_smfmac_f32_32x32x32_bf16 v[112:127], v[186:189], v[88:95], v190\n"
        "s_waitcnt lgkmcnt(0)\n"
        "s_nop 15\n"
        ".purgem PHA\n .purgem PHB\n"
        : "={v[96:111]}"(Oa), "={v[112:127]}"(Ob), "=&s"(cnt), "+{v191}"(aK), "+{v192}"(aK2), "+{v193}"(aV)
        : "{v128}"(ma), "{v144}"(mb), "{v[160:163]}"(q1a), "{v[164:167]}"(q1b), "{v168}"(q2a), "{v172}"(q2b)
        : "memory", "scc",
          "v64", "v65", "v66", "v67", "v68", "v69", "v70", "v71", "v72", "v73", "v74", "v75", "v76", "v77", "v78", "v79",
          "v80", "v81", "v82", "v83", "v84", "v85", "v86", "v87", "v88", "v89", "v90", "v91", "v92", "v93", "v94", "v95",
          "v129", "v130", "v131", "v132", "v133", "v134", "v135", "v136", "v137", "v138", "v139", "v140", "v141", "v142", "v143",
          "v145", "v146", "v147", "v148", "v149", "v150", "v151", "v152", "v153", "v154", "v155", "v156", "v157", "v158", "v159",
          "v169", "v170", "v171", "v173", "v174", "v175", "v176", "v177", "v178", "v179", "v180", "v181", "v182", "v183", "v184",
          "v185", "v186", "v187", "v188", "v189", "v190");
}

DI float sq8(u32x4 q) {
    float s = 0.f;
#pragma unroll
    for (int i = 0; i < 4; i++) { float a = bflo(q[i]), b = bfhi(q[i]); s += a * a + b * b; }
    return s;
}
DI void store_o(char* ob, const f32x16& o, float scale_num, float eps, int h, int qt, int lane) {
    const int hh = lane >> 5, rho = lane & 31;
    float den = __shfl(o[10], rho) + eps;
    const float sc = scale_num / den;
    const int tok = (((rho >> 2) & 1) << 4) + ((rho >> 3) << 2) + (rho & 3);
    char* base = ob + (size_t)(2 * qt + (tok >> 4)) * 18432 + (tok & 15) * 16;
    u32x4 v = {pk(o[0] * sc, o[1] * sc), pk(o[2] * sc, o[3] * sc), pk(o[4] * sc, o[5] * sc), pk(o[6] * sc, o[7] * sc)};
    *(u32x4*)(base + (h >> 1) * 1024 + (2 * (h & 1) + hh) * 256) = v;
    if (hh == 0) *(unsigned*)(base + (16 + (h >> 4)) * 1024 + ((h >> 2) & 3) * 256 + (h & 3) * 4) = pk(o[8] * sc, o[9] * sc);
}

constexpr unsigned L2_KVT = KV_HEAD;
constexpr unsigned L2_FLAG = KV_HEAD + 1024;
constexpr unsigned L2_MAX = KV_HEAD + 1280;
constexpr unsigned LDS2 = KV_HEAD + 4096;

__global__ void __launch_bounds__(512, 2) k2_attn(char* __restrict__ ws) {
    extern __shared__ __attribute__((aligned(16))) char smem[];
    const int tid = threadIdx.x, lane = tid & 63, w = __builtin_amdgcn_readfirstlane(tid >> 6);
    const unsigned lds0 = (unsigned)(size_t)smem;
    const int b = blockIdx.x, h = (b & 7) * 4 + ((b >> 3) & 3), qb = b >> 5;
    const int qg = w & 3, kh = w >> 2, hh = lane >> 5, rho = lane & 31;
    const char* kvsrc = ws + OFF_KV + (size_t)h * KV_HEAD;
    for (int i = w; i < 148; i += 8) glds16(lane * 16, kvsrc + i * 1024, lds0 + i * 1024);
    const int qt0 = 8 * qb + 2 * qg, qtf = qt0 + kh;
    const u32x4 q1a = *(const u32x4*)(ws + OFF_Q1 + (size_t)h * 65536 + qt0 * 1024 + lane * 16);
    const u32x4 q1b = *(const u32x4*)(ws + OFF_Q1 + (size_t)h * 65536 + qt0 * 1024 + 1024 + lane * 16);
    unsigned q2a = *(const unsigned*)(ws + OFF_Q2 + (size_t)h * 8192 + qt0 * 128 + rho * 4);
    unsigned q2b = *(const unsigned*)(ws + OFF_Q2 + (size_t)h * 8192 + qt0 * 128 + 128 + rho * 4);
    const u32x4 p1 = *(const u32x4*)(ws + OFF_Q1 + (size_t)(32 + h) * 65536 + qtf * 1024 + lane * 16);
    unsigned p2 = *(const unsigned*)(ws + OFF_Q2 + (size_t)(32 + h) * 8192 + qtf * 128 + rho * 4);
    const int tok = (((rho >> 2) & 1) << 4) + ((rho >> 3) << 2) + (rho & 3);
    const float gate = ((const float*)(ws + OFF_GATE))[qtf * 32 + tok];
    float kmax = ((const float*)(ws + OFF_KMAX))[h * 64 + lane];
    if (tid < 342) {
        const float* src = (const float*)(ws + OFF_KVP) + h * 16 * 384 + tid;
        float s = 0.f;
#pragma unroll
        for (int i = 0; i < 16; i++) s += src[i * 384];
        const int e = tid / 18, d = tid % 18;
        __bf16 v = (__bf16)s;
        *(__bf16*)(smem + L2_KVT + (d < 16 ? e * 32 + d * 2 : 640 + e * 4 + (d - 16) * 2)) = v;
    }
    if (tid == 0) *(unsigned*)(smem + L2_FLAG) = 0;
    if (hh) { q2a = 0; q2b = 0; p2 = 0; }
#pragma unroll
    for (int o = 32; o; o >>= 1) kmax = fmaxf(kmax, __shfl_xor(kmax, o));
    float na = sq8(q1a) + bflo(q2a) * bflo(q2a) + bfhi(q2a) * bfhi(q2a), nb = sq8(q1b) + bflo(q2b) * bflo(q2b) + bfhi(q2b) * bfhi(q2b);
    na += __shfl_xor(na, 32); nb += __shfl_xor(nb, 32);
    float ma = -1.01f * __builtin_sqrtf(na * kmax), mb = -1.01f * __builtin_sqrtf(nb * kmax);
    WAITV_BAR(0);
    {
        const int e = rho < 19 ? rho : 18;
        bf16x8 a1 = *(const bf16x8*)(smem + L2_KVT + e * 32 + hh * 16);
        u32x2 a2 = {*(const unsigned*)(smem + L2_KVT + 640 + e * 4), 0u};
        u32x2 b2 = {p2, 0u};
        f32x16 o = {};
        o = __builtin_amdgcn_mfma_f32_32x32x8bf16_1k(__builtin_bit_cast(s16x4, a2), __builtin_bit_cast(s16x4, b2), o, 0, 0, 0);
        o = __builtin_amdgcn_mfma_f32_32x32x16_bf16(a1, __builtin_bit_cast(bf16x8, p1), o, 0, 0, 0);
        store_o(ws + OFF_O + (size_t)128 * 18432, o, 1.f - gate, 1e-6f, h, qtf, lane);
    }
    const int vrow = rho < 19 ? rho : ((rho >= 20 && rho < 28) ? 0 : 4);
    const unsigned aK = lds0 + kh * 8192 + lane * 16, aK2 = lds0 + 16384 + kh * 1024 + rho * 4, aV = lds0 + 18432 + kh * 9728 + hh * 304 + vrow * 16;
    f32x16 Oa, Ob;
    for (int attempt = 0;; attempt++) {
        attn_loop(Oa, Ob, ma, mb, q1a, q1b, q2a, q2b, aK, aK2, aV);
        if (attempt) { __syncthreads(); break; }
        const float thr = 9.094947e-13f;
        const bool bad = hh == 0 && !(Oa[10] >= thr && Oa[10] < 1e30f && Ob[10] >= thr && Ob[10] < 1e30f);
        if (__any(bad) && lane == 0) *(volatile unsigned*)(smem + L2_FLAG) = 1;
        __syncthreads();
        if (*(volatile unsigned*)(smem + L2_FLAG) == 0) break;
        float xa = -INFINITY, xb = -INFINITY;
        const u32x2 b2a = {q2a, 0u}, b2b = {q2b, 0u};
        for (int t = 0; t < 32; t++) {
            const unsigned cb = (t >> 3) * CHUNK, tt = t & 7;
            bf16x8 kf = ldsr128(aK + cb + tt * 1024);
            u32x2 k2 = {*(const LAS unsigned*)(size_t)(aK2 + cb + tt * 128), 0u};
            f32x16 s = {};
            s = __builtin_amdgcn_mfma_f32_32x32x8bf16_1k(__builtin_bit_cast(s16x4, k2), __builtin_bit_cast(s16x4, b2a), s, 0, 0, 0);
            s = __builtin_amdgcn_mfma_f32_32x32x16_bf16(kf, __builtin_bit_cast(bf16x8, q1a), s, 0, 0, 0);
#pragma unroll
            for (int i = 0; i < 16; i++) xa = fmaxf(xa, s[i]);
            f32x16 s2 = {};
            s2 = __builtin_amdgcn_mfma_f32_32x32x8bf16_1k(__builtin_bit_cast(s16x4, k2), __builtin_bit_cast(s16x4, b2b), s2, 0, 0, 0);
            s2 = __builtin_amdgcn_mfma_f32_32x32x16_bf16(kf, __builtin_bit_cast(bf16x8, q1b), s2, 0, 0, 0);
#pragma unroll
            for (int i = 0; i < 16; i++) xb = fmaxf(xb, s2[i]);
        }
        xa = fmaxf(xa, __shfl_xor(xa, 32)); xb = fmaxf(xb, __shfl_xor(xb, 32));
        float* mx = (float*)(smem + L2_MAX);
        mx[w * 64 + lane] = hh ? xb : xa;
        __syncthreads();
        ma = -fmaxf(xa, mx[(w ^ 4) * 64 + rho]); mb = -fmaxf(xb, mx[(w ^ 4) * 64 + 32 + rho]);
    }
    float* ex = (float*)smem;
    const f32x16 give = kh ? Oa : Ob;
    f32x16 mine = kh ? Ob : Oa;
#pragma unroll
    for (int i = 0; i < 11; i++) ex[(w * 11 + i) * 64 + lane] = give[i];
    __syncthreads();
#pragma unroll
    for (int i = 0; i < 11; i++) mine[i] += ex[((w ^ 4) * 11 + i) * 64 + lane];
    store_o(ws + OFF_O, mine, gate, 0.f, h, qtf, lane);
}

constexpr unsigned ST3 = 27648;
DI unsigned wo_row(unsigned kk) {
    const unsigned ks = kk >> 5, g = (kk >> 3) & 3, j = kk & 7;
    unsigned h, dp;
    if (ks < 16) { h = 2 * ks + (g >> 1); dp = (j & 3) + 8 * (j >> 2) + 4 * (g & 1); }
    else { h = (ks - 16) * 16 + g * 4 + (j >> 1); dp = 16 + (j & 1); }
    const unsigned d = (h & 1) ? (dp < 16 ? dp + 2 : dp - 16) : dp;
    return h * 18 + d;
}
__global__ void __launch_bounds__(512, 2) k3_out(const char* __restrict__ WA, const char* __restrict__ WB, const char* __restrict__ ws, char* __restrict__ out) {
    extern __shared__ __attribute__((aligned(16))) char smem[];
    const int tid = threadIdx.x, lane = tid & 63, w = __builtin_amdgcn_readfirstlane(tid >> 6);
    const unsigned lds0 = (unsigned)(size_t)smem;
    const int b = blockIdx.x, xcd = b & 7, idx = b >> 3;
    const int cb = (xcd & 1) * 4 + (idx & 3), rb = (xcd >> 1) * 8 + (idx >> 2);
    const int rt = w & 3, kh = w >> 2, g = lane >> 4, m = lane & 15;
    bf16x8 af[18];
#pragma unroll
    for (int s = 0; s < 6; s++)
#pragma unroll
        for (int i = 0; i < 3; i++)
            af[s * 3 + i] = *(const bf16x8*)(ws + OFF_O + (size_t)((s / 3) * 128 + rb * 4 + rt) * 18432 + ((s % 3) * 6 + 3 * kh + i) * 1024 + lane * 16);
    auto stage = [&](int s) {
#pragma unroll
        for (int i = 0; i < 4; i++) {
            const int pcs = w + 8 * i;
            if (pcs < 27) {
                const unsigned o = pcs * 1024u + lane * 16u, lr = o / 144u, pc = (o % 144u) >> 4;
                const unsigned k = (lr & ~7u) | ((lr & 1u) << 2) | ((lr >> 1) & 3u);
                glds16(wo_row((s % 3) * 192 + k) * 1152u + cb * 144u + pc * 16u, s < 3 ? WA : WB, lds0 + (s % 5) * ST3 + pcs * 1024);
            }
        }
    };
    stage(0); stage(1); stage(2); stage(3); stage(4);
    const int q4 = m >> 2, p = m & 3;
    const unsigned aW = lds0 + (g * 8 + q4 * 2) * 144 + 8 * p + kh * 3 * 4608;
    const unsigned aW4 = lds0 + (g * 8 + q4 * 2) * 144 + 128 + 8 * (p & 1) + kh * 3 * 4608;
    f32x4 acc[5];
#pragma unroll
    for (int t = 0; t < 5; t++) acc[t] = (f32x4){0, 0, 0, 0};
#pragma unroll
    for (int s = 0; s < 6; s++) {
        if (s == 0) WAITV_BAR(12); else if (s == 1) WAITV_BAR(9); else if (s == 2) WAITV_BAR(9); else if (s == 3) WAITV_BAR(6); else if (s == 4) WAITV_BAR(3); else WAITV_BAR(0);
        if (s == 1) stage(5);
#pragma unroll
        for (int i = 0; i < 3; i++)
#pragma unroll
            for (int t = 0; t < 5; t++) {
                bf16x8 wf = trfrag((t < 4 ? aW + 32 * t : aW4) + (s % 5) * ST3 + i * 4608, 144);
                acc[t] = mfma16(wf, af[s * 3 + i], acc[t]);
            }
    }
    __syncthreads();
    f32x4* red = (f32x4*)smem;
    if (kh) {
#pragma unroll
        for (int t = 0; t < 5; t++) red[(rt * 5 + t) * 64 + lane] = acc[t];
    }
    __syncthreads();
    if (!kh) {
        char* orow = out + (size_t)(rb * 64 + rt * 16 + m) * 1152 + cb * 144 + g * 8;
#pragma unroll
        for (int t = 0; t < 5; t++) {
            f32x4 v = acc[t] + red[(rt * 5 + t) * 64 + lane];
            if (t < 4 || g < 2) *(u32x2*)(orow + t * 32) = (u32x2){pk(v[0], v[1]), pk(v[2], v[3])};
        }
    }
}

extern "C" void kernel_launch(void* const* d_in, const int* in_sizes, int n_in, void* d_out, int out_size, void* d_ws, size_t ws_size,
                              hipStream_t stream) {
    const char* x = (const char*)d_in[0];
    const char* wqkvA = (const char*)d_in[1];
    const char* woA = (const char*)d_in[2];
    const char* wqkvB = (const char*)d_in[4];
    const char* woB = (const char*)d_in[5];
    const unsigned* gw = (const unsigned*)d_in[6];
    char* ws = (char*)d_ws;
    (void)hipFuncSetAttribute((const void*)k1_qkv, hipFuncAttributeMaxDynamicSharedMemorySize, 3 * ST1);
    (void)hipFuncSetAttribute((const void*)k2_attn, hipFuncAttributeMaxDynamicSharedMemorySize, LDS2);
    (void)hipFuncSetAttribute((const void*)k3_out, hipFuncAttributeMaxDynamicSharedMemorySize, 5 * ST3);
    k1_qkv<<<256, 512, 3 * ST1, stream>>>(x, wqkvA, wqkvB, gw, ws);
    k2_attn<<<256, 512, LDS2, stream>>>(ws);
    k3_out<<<256, 512, 5 * ST3, stream>>>(woA, woB, ws, (char*)d_out);
}
```

```cpp
#include <hip/hip_runtime.h>
#include <stdint.h>

typedef __attribute__((ext_vector_type(8))) __bf16 bf16x8;
typedef __attribute__((ext_vector_type(16))) __bf16 bf16x16;
typedef __attribute__((ext_vector_type(2))) __bf16 bf16x2;
typedef __attribute__((ext_vector_type(4))) short s16x4;
typedef __attribute__((ext_vector_type(2))) float f32x2;
typedef __attribute__((ext_vector_type(4))) float f32x4;
typedef __attribute__((ext_vector_type(8))) float f32x8;
typedef __attribute__((ext_vector_type(16))) float f32x16;
typedef __attribute__((ext_vector_type(32))) float f32x32;
typedef __attribute__((ext_vector_type(4))) unsigned u32x4;
typedef __attribute__((ext_vector_type(2))) unsigned u32x2;
typedef unsigned long long u64;
typedef __attribute__((address_space(1))) u64 gu64;
#define LAS __attribute__((address_space(3)))
#define DI __device__ __forceinline__
#define RLX_AGENT __ATOMIC_RELAXED, __HIP_MEMORY_SCOPE_AGENT

constexpr unsigned CHUNK = 18432;
constexpr unsigned KV_HEAD = 8 * CHUNK;
constexpr unsigned OFF_EPOCH = 0;
constexpr unsigned OFF_FA = 256;
constexpr unsigned OFF_F1 = OFF_FA + 8192;
constexpr unsigned OFF_FB = OFF_F1 + 4096;
constexpr unsigned OFF_F2 = OFF_FB + 16384;
constexpr unsigned OFF_GATE = OFF_F2 + 16384;
constexpr unsigned OFF_KVP = OFF_GATE + 8192;
constexpr unsigned OFF_Q2 = OFF_KVP + 32 * 16 * 1408;
constexpr unsigned OFF_Q1 = OFF_Q2 + 2 * 32 * 8192;
constexpr unsigned OFF_KV = OFF_Q1 + 2 * 32 * 65536;
constexpr unsigned OFF_O = OFF_KV + 32 * KV_HEAD;
constexpr unsigned OFF_OR = OFF_O + 2 * 128 * 16384;
constexpr unsigned WS_USED = OFF_OR + 2 * 64 * 4096;

constexpr unsigned ST1 = 44032;
constexpr unsigned L_RED1 = 57344;
constexpr unsigned L_W3 = 3 * CHUNK;
constexpr unsigned L_W3B = L_W3 + 73728;
constexpr unsigned L_EX = L_W3 + 82944;
constexpr unsigned L_KVT = L_EX + 24576;
constexpr unsigned L_FLAG = L_KVT + 768;
constexpr unsigned LDS_BYTES = 163840;

constexpr float QSCALE = 0.23570226039551584f * 1.4426950408889634f;
constexpr unsigned ONES = 0x3f803f80u;

#define STAMP(i)
#define STAMP_DECL
#define STAMP_ARGS
#define STAMP_PASS
#define STAMP_OUT

DI unsigned pk(float a, float b) { bf16x2 v = __builtin_convertvector((f32x2){a, b}, bf16x2); return __builtin_bit_cast(unsigned, v); }
DI float bflo(unsigned u) { return __uint_as_float(u << 16); }
DI float bfhi(unsigned u) { return __uint_as_float(u & 0xffff0000u); }
DI f32x4 phi4(f32x4 a) {
    const f32x4 s = a * 1.4426950408889634f;
    f32x4 e, x;
#pragma unroll
    for (int i = 0; i < 4; i++) {
        asm("v_exp_f32_e64 %0, %1 clamp" : "=v"(e[i]) : "v"(s[i]));
        asm("v_max_f32 %0, 0, %1" : "=v"(x[i]) : "v"(a[i]));
    }
    return (x + 1e-6f) + e;
}
DI void glds16(unsigned voff, const void* sbase, unsigned lds_dst) {
    unsigned keep;
    asm volatile("s_mov_b32 %0, m0\n\ts_mov_b32 m0, %3\n\ts_nop 0\n\tglobal_load_lds_dwordx4 %1, %2\n\ts_mov_b32 m0, %0"
                 : "=&s"(keep) : "v"(voff), "s"(sbase), "s"(lds_dst) : "memory");
}
DI void glds16_dev(unsigned voff, const void* sbase, unsigned lds_dst) {
    unsigned keep;
    asm volatile("s_mov_b32 %0, m0\n\ts_mov_b32 m0, %3\n\ts_nop 0\n\tglobal_load_lds_dwordx4 %1, %2 sc1\n\ts_mov_b32 m0, %0"
                 : "=&s"(keep) : "v"(voff), "s"(sbase), "s"(lds_dst) : "memory");
}
#define WAITV_BAR(n) asm volatile("s_waitcnt vmcnt(" #n ") lgkmcnt(0)\n\ts_barrier" ::: "memory")
#define LBAR() asm volatile("s_waitcnt lgkmcnt(0)\n\ts_barrier" ::: "memory")
#define BAR() asm volatile("s_barrier" ::: "memory")
#define WAITV() do { asm volatile("s_waitcnt vmcnt(0)" ::: "memory"); __builtin_amdgcn_s_waitcnt(0x0F70); } while (0)
#define OPAQUE(x) asm volatile("" : "+v"(x))
template <class T> DI LAS T* lds(unsigned a) { return (LAS T*)(size_t)a; }
DI bf16x8 ldsr128(unsigned a) { return *(const LAS bf16x8*)(size_t)a; }
DI s16x4 trr(unsigned a) { return __builtin_amdgcn_ds_read_tr16_b64_v4i16((LAS s16x4*)(size_t)a); }
DI bf16x8 trfrag(unsigned a, unsigned b) {
    s16x4 lo = trr(a), hi = trr(b);
    return __builtin_bit_cast(bf16x8, __builtin_shufflevector(lo, hi, 0, 1, 2, 3, 4, 5, 6, 7));
}
DI f32x4 mfma16(bf16x8 a, bf16x8 b, f32x4 c) { return __builtin_amdgcn_mfma_f32_16x16x32_bf16(a, b, c, 0, 0, 0); }
template <int CTRL> DI float dpp(float v) { return __int_as_float(__builtin_amdgcn_mov_dpp(__float_as_int(v), CTRL, 0xf, 0xf, true)); }
DI float swap16_add(float a, float b) { auto r = __builtin_amdgcn_permlane16_swap(__float_as_uint(a), __float_as_uint(b), false, false); return __uint_as_float(r[0]) + __uint_as_float(r[1]); }
DI float swap32_add(float a, float b) { auto r = __builtin_amdgcn_permlane32_swap(__float_as_uint(a), __float_as_uint(b), false, false); return __uint_as_float(r[0]) + __uint_as_float(r[1]); }
DI unsigned umax(unsigned a, unsigned b) { return a > b ? a : b; }
template <int CTRL> DI unsigned dppu(unsigned v) { return __builtin_amdgcn_mov_dpp(v, CTRL, 0xf, 0xf, true); }
DI unsigned swap16_umax(unsigned a) { auto r = __builtin_amdgcn_permlane16_swap(a, a, false, false); return umax(r[0], r[1]); }
DI unsigned swap32_umax(unsigned a) { auto r = __builtin_amdgcn_permlane32_swap(a, a, false, false); return umax(r[0], r[1]); }
DI unsigned row_umax(unsigned x) { x = umax(x, dppu<0xB1>(x)); x = umax(x, dppu<0x4E>(x)); x = umax(x, dppu<0x141>(x)); x = umax(x, dppu<0x140>(x)); return x; }
DI float swap32_max(float a) { auto r = __builtin_amdgcn_permlane32_swap(__float_as_uint(a), __float_as_uint(a), false, false); return fmaxf(__uint_as_float(r[0]), __uint_as_float(r[1])); }
DI float row_sum(float x) {
    x += dpp<0xB1>(x); x += dpp<0x4E>(x); x += dpp<0x141>(x); x += dpp<0x140>(x); return x; }

typedef __amdgpu_buffer_rsrc_t rsrc_t;
DI void st128(rsrc_t r, unsigned off, u32x4 v) { __builtin_amdgcn_raw_buffer_store_b128(v, r, off, 0, 16); }
DI void st128s(rsrc_t r, unsigned off, unsigned soff, u32x4 v) { st128(r, off + soff, v); }
DI void st64(rsrc_t r, unsigned off, u32x2 v) { __builtin_amdgcn_raw_buffer_store_b64(v, r, off, 0, 16); }
DI void st32(rsrc_t r, unsigned off, unsigned v) { __builtin_amdgcn_raw_buffer_store_b32(v, r, off, 0, 16); }
DI u32x4 ld128(rsrc_t r, unsigned off) { return __builtin_amdgcn_raw_buffer_load_b128(r, off, 0, 16); }
DI unsigned ld32(rsrc_t r, unsigned off) { return __builtin_amdgcn_raw_buffer_load_b32(r, off, 0, 16); }

template <int MASK, int SLEEP>
DI u64 wait_flags(const char* words, u64 tag, u64 seen, int lane) {
    gu64* p = (gu64*)(size_t)words + (lane & MASK);
    if (!__all((seen ^ tag) < 0x1000000ull)) {
        const u64 t0 = __builtin_amdgcn_s_memrealtime();
        for (;;) {
            seen = __hip_atomic_load(p, RLX_AGENT);
            if (__all((seen ^ tag) < 0x1000000ull)) break;
            __builtin_amdgcn_s_sleep(SLEEP);
            if (__builtin_amdgcn_s_memrealtime() - t0 > 2000000ull) break;
        }
    }
    asm volatile("" ::: "memory");
    return seen;
}
template <int MASK> DI u64 peek_flags(const char* words, int lane) { return __hip_atomic_load((gu64*)(size_t)words + (lane & MASK), RLX_AGENT); }
DI void post_flag(char* word, u64 val, int lane) {
    WAITV();
    if (lane == 0) __hip_atomic_store((gu64*)(size_t)word, val, RLX_AGENT);
}
DI u64 make_tag(u64 epoch) { return ((epoch + 1) * 0x9E3779B97F4A7C15ull) << 24; }

#define P1_PRO \
    "ds_read_b128 v[152:155], v212 offset:0\n" \
    "ds_read_b128 v[156:159], v212 offset:2048\n" \
    "ds_read_b128 v[160:163], v212 offset:4096\n" \
    "ds_read_b128 v[164:167], v212 offset:6144\n" \
    "ds_read_b64_tr_b16 v[184:185], v214 offset:0\n" \
    "ds_read_b64_tr_b16 v[186:187], v214 offset:432\n" \
    "ds_read_b64_tr_b16 v[188:189], v214 offset:40\n" \
    "ds_read_b64_tr_b16 v[190:191], v214 offset:472\n" \
    "ds_read_b64_tr_b16 v[192:193], v214 offset:144\n" \
    "ds_read_b64_tr_b16 v[194:195], v214 offset:576\n" \
    "ds_read_b64_tr_b16 v[196:197], v214 offset:184\n" \
    "ds_read_b64_tr_b16 v[198:199], v214 offset:616\n" \
    "ds_read_b64_tr_b16 v[200:201], v214 offset:288\n" \
    "ds_read_b64_tr_b16 v[202:203], v214 offset:720\n" \
    "ds_read_b64_tr_b16 v[204:205], v214 offset:328\n" \
    "ds_read_b64_tr_b16 v[206:207], v214 offset:760\n" \
    "ds_read_b64_tr_b16 v[208:209], v215 offset:0\n" \
    "ds_read_b64_tr_b16 v[210:211], v215 offset:432\n" \
    "s_waitcnt lgkmcnt(0)\n"
#define P1_BODY \
    "v_mfma_f32_16x16x32_bf16 v[40:43], v[184:187], v[152:155], v[40:43]\n" \
    "ds_read_b128 v[168:171], v213 offset:0\n" \
    "v_mfma_f32_16x16x32_bf16 v[44:47], v[184:187], v[156:159], v[44:47]\n" \
    "ds_read_b128 v[172:175], v213 offset:2048\n" \
    "v_mfma_f32_16x16x32_bf16 v[48:51], v[184:187], v[160:163], v[48:51]\n" \
    "ds_read_b128 v[176:179], v213 offset:4096\n" \
    "v_mfma_f32_16x16x32_bf16 v[52:55], v[184:187], v[164:167], v[52:55]\n" \
    "ds_read_b128 v[180:183], v213 offset:6144\n" \
    "v_mfma_f32_16x16x32_bf16 v[56:59], v[188:191], v[152:155], v[56:59]\n" \
    "ds_read_b64_tr_b16 v[184:185], v214 offset:13824\n" \
    "v_mfma_f32_16x16x32_bf16 v[60:63], v[188:191], v[156:159], v[60:63]\n" \
    "ds_read_b64_tr_b16 v[186:187], v214 offset:14256\n" \
    "v_mfma_f32_16x16x32_bf16 v[64:67], v[188:191], v[160:163], v[64:67]\n" \
    "v_mfma_f32_16x16x32_bf16 v[68:71], v[188:191], v[164:167], v[68:71]\n" \
    "MFK 72, 192, 152\n" \
    "ds_read_b64_tr_b16 v[188:189], v214 offset:13864\n" \
    "MFK 76, 192, 156\n" \
    "ds_read_b64_tr_b16 v[190:191], v214 offset:14296\n" \
    "MFK 80, 192, 160\n" \
    "MFK 84, 192, 164\n" \
    "MFK 88, 196, 152\n" \
    "ds_read_b64_tr_b16 v[192:193], v214 offset:13968\n" \
    "MFK 92, 196, 156\n" \
    "ds_read_b64_tr_b16 v[194:195], v214 offset:14400\n" \
    "MFK 96, 196, 160\n" \
    "MFK 100, 196, 164\n" \
    "v_mfma_f32_16x16x32_bf16 v[104:107], v[152:155], v[200:203], v[104:107]\n" \
    "ds_read_b64_tr_b16 v[196:197], v214 offset:14008\n" \
    "v_mfma_f32_16x16x32_bf16 v[108:111], v[156:159], v[200:203], v[108:111]\n" \
    "ds_read_b64_tr_b16 v[198:199], v214 offset:14440\n" \
    "v_mfma_f32_16x16x32_bf16 v[112:115], v[160:163], v[200:203], v[112:115]\n" \
    "v_mfma_f32_16x16x32_bf16 v[116:119], v[164:167], v[200:203], v[116:119]\n" \
    "v_mfma_f32_16x16x32_bf16 v[120:123], v[152:155], v[204:207], v[120:123]\n" \
    "ds_read_b64_tr_b16 v[200:201], v214 offset:14112\n" \
    "v_mfma_f32_16x16x32_bf16 v[124:127], v[156:159], v[204:207], v[124:127]\n" \
    "ds_read_b64_tr_b16 v[202:203], v214 offset:14544\n" \
    "v_mfma_f32_16x16x32_bf16 v[128:131], v[160:163], v[204:207], v[128:131]\n" \
    "v_mfma_f32_16x16x32_bf16 v[132:135], v[164:167], v[204:207], v[132:135]\n" \
    "v_mfma_f32_16x16x32_bf16 v[136:139], v[152:155], v[208:211], v[136:139]\n" \
    "ds_read_b64_tr_b16 v[204:205], v214 offset:14152\n" \
    "v_mfma_f32_16x16x32_bf16 v[140:143], v[156:159], v[208:211], v[140:143]\n" \
    "ds_read_b64_tr_b16 v[206:207], v214 offset:14584\n" \
    "v_mfma_f32_16x16x32_bf16 v[144:147], v[160:163], v[208:211], v[144:147]\n" \
    "v_mfma_f32_16x16x32_bf16 v[148:151], v[164:167], v[208:211], v[148:151]\n" \
    "s_waitcnt lgkmcnt(10)\n" \
    "v_mfma_f32_16x16x32_bf16 v[40:43], v[184:187], v[168:171], v[40:43]\n" \
    "ds_read_b64_tr_b16 v[208:209], v215 offset:13824\n" \
    "v_mfma_f32_16x16x32_bf16 v[44:47], v[184:187], v[172:175], v[44:47]\n" \
    "ds_read_b64_tr_b16 v[210:211], v215 offset:14256\n" \
    "v_mfma_f32_16x16x32_bf16 v[48:51], v[184:187], v[176:179], v[48:51]\n" \
    "v_mfma_f32_16x16x32_bf16 v[52:55], v[184:187], v[180:183], v[52:55]\n" \
    "s_waitcnt lgkmcnt(10)\n" \
    "v_mfma_f32_16x16x32_bf16 v[56:59], v[188:191], v[168:171], v[56:59]\n" \
    "v_mfma_f32_16x16x32_bf16 v[60:63], v[188:191], v[172:175], v[60:63]\n" \
    "v_mfma_f32_16x16x32_bf16 v[64:67], v[188:191], v[176:179], v[64:67]\n" \
    "v_mfma_f32_16x16x32_bf16 v[68:71], v[188:191], v[180:183], v[68:71]\n" \
    "s_waitcnt lgkmcnt(8)\n" \
    "MFK 72, 192, 168\n" \
    "MFK 76, 192, 172\n" \
    "MFK 80, 192, 176\n" \
    "MFK 84, 192, 180\n" \
    "s_waitcnt lgkmcnt(6)\n" \
    "MFK 88, 196, 168\n" \
    "MFK 92, 196, 172\n" \
    "MFK 96, 196, 176\n" \
    "MFK 100, 196, 180\n" \
    "s_waitcnt lgkmcnt(4)\n" \
    "v_mfma_f32_16x16x32_bf16 v[104:107], v[168:171], v[200:203], v[104:107]\n" \
    "v_mfma_f32_16x16x32_bf16 v[108:111], v[172:175], v[200:203], v[108:111]\n" \
    "v_mfma_f32_16x16x32_bf16 v[112:115], v[176:179], v[200:203], v[112:115]\n" \
    "v_mfma_f32_16x16x32_bf16 v[116:119], v[180:183], v[200:203], v[116:119]\n" \
    "s_waitcnt lgkmcnt(2)\n" \
    "v_mfma_f32_16x16x32_bf16 v[120:123], v[168:171], v[204:207], v[120:123]\n" \
    "v_mfma_f32_16x16x32_bf16 v[124:127], v[172:175], v[204:207], v[124:127]\n" \
    "v_mfma_f32_16x16x32_bf16 v[128:131], v[176:179], v[204:207], v[128:131]\n" \
    "v_mfma_f32_16x16x32_bf16 v[132:135], v[180:183], v[204:207], v[132:135]\n" \
    "s_waitcnt lgkmcnt(0)\n" \
    "v_mfma_f32_16x16x32_bf16 v[136:139], v[168:171], v[208:211], v[136:139]\n" \
    "v_mfma_f32_16x16x32_bf16 v[140:143], v[172:175], v[208:211], v[140:143]\n" \
    "v_mfma_f32_16x16x32_bf16 v[144:147], v[176:179], v[208:211], v[144:147]\n" \
    "v_mfma_f32_16x16x32_bf16 v[148:151], v[180:183], v[208:211], v[148:151]\n"
#define P1_TAIL \
    "v_mfma_f32_16x16x32_bf16 v[40:43], v[184:187], v[152:155], v[40:43]\n" \
    "ds_read_b128 v[168:171], v213 offset:0\n" \
    "v_mfma_f32_16x16x32_bf16 v[44:47], v[184:187], v[156:159], v[44:47]\n" \
    "ds_read_b128 v[172:175], v213 offset:2048\n" \
    "v_mfma_f32_16x16x32_bf16 v[48:51], v[184:187], v[160:163], v[48:51]\n" \
    "ds_read_b128 v[176:179], v213 offset:4096\n" \
    "v_mfma_f32_16x16x32_bf16 v[52:55], v[184:187], v[164:167], v[52:55]\n" \
    "ds_read_b128 v[180:183], v213 offset:6144\n" \
    "v_mfma_f32_16x16x32_bf16 v[56:59], v[188:191], v[152:155], v[56:59]\n" \
    "ds_read_b64_tr_b16 v[184:185], v214 offset:13824\n" \
    "v_mfma_f32_16x16x32_bf16 v[60:63], v[188:191], v[156:159], v[60:63]\n" \
    "ds_read_b64_tr_b16 v[186:187], v214 offset:14256\n" \
    "v_mfma_f32_16x16x32_bf16 v[64:67], v[188:191], v[160:163], v[64:67]\n" \
    "v_mfma_f32_16x16x32_bf16 v[68:71], v[188:191], v[164:167], v[68:71]\n" \
    "MFK 72, 192, 152\n" \
    "ds_read_b64_tr_b16 v[188:189], v214 offset:13864\n" \
    "MFK 76, 192, 156\n" \
    "ds_read_b64_tr_b16 v[190:191], v214 offset:14296\n" \
    "MFK 80, 192, 160\n" \
    "MFK 84, 192, 164\n" \
    "MFK 88, 196, 152\n" \
    "ds_read_b64_tr_b16 v[192:193], v214 offset:13968\n" \
    "MFK 92, 196, 156\n" \
    "ds_read_b64_tr_b16 v[194:195], v214 offset:14400\n" \
    "MFK 96, 196, 160\n" \
    "MFK 100, 196, 164\n" \
    "v_mfma_f32_16x16x32_bf16 v[104:107], v[152:155], v[200:203], v[104:107]\n" \
    "ds_read_b64_tr_b16 v[196:197], v214 offset:14008\n" \
    "v_mfma_f32_16x16x32_bf16 v[108:111], v[156:159], v[200:203], v[108:111]\n" \
    "ds_read_b64_tr_b16 v[198:199], v214 offset:14440\n" \
    "v_mfma_f32_16x16x32_bf16 v[112:115], v[160:163], v[200:203], v[112:115]\n" \
    "v_mfma_f32_16x16x32_bf16 v[116:119], v[164:167], v[200:203], v[116:119]\n" \
    "v_mfma_f32_16x16x32_bf16 v[120:123], v[152:155], v[204:207], v[120:123]\n" \
    "ds_read_b64_tr_b16 v[200:201], v214 offset:14112\n" \
    "v_mfma_f32_16x16x32_bf16 v[124:127], v[156:159], v[204:207], v[124:127]\n" \
    "ds_read_b64_tr_b16 v[202:203], v214 offset:14544\n" \
    "v_mfma_f32_16x16x32_bf16 v[128:131], v[160:163], v[204:207], v[128:131]\n" \
    "v_mfma_f32_16x16x32_bf16 v[132:135], v[164:167], v[204:207], v[132:135]\n" \
    "v_mfma_f32_16x16x32_bf16 v[136:139], v[152:155], v[208:211], v[136:139]\n" \
    "ds_read_b64_tr_b16 v[204:205], v214 offset:14152\n" \
    "v_mfma_f32_16x16x32_bf16 v[140:143], v[156:159], v[208:211], v[140:143]\n" \
    "ds_read_b64_tr_b16 v[206:207], v214 offset:14584\n" \
    "v_mfma_f32_16x16x32_bf16 v[144:147], v[160:163], v[208:211], v[144:147]\n" \
    "v_mfma_f32_16x16x32_bf16 v[148:151], v[164:167], v[208:211], v[148:151]\n" \
    "s_waitcnt lgkmcnt(10)\n" \
    "v_mfma_f32_16x16x32_bf16 v[40:43], v[184:187], v[168:171], v[40:43]\n" \
    "ds_read_b64_tr_b16 v[208:209], v215 offset:13824\n" \
    "v_mfma_f32_16x16x32_bf16 v[44:47], v[184:187], v[172:175], v[44:47]\n" \
    "ds_read_b64_tr_b16 v[210:211], v215 offset:14256\n" \
    "v_mfma_f32_16x16x32_bf16 v[48:51], v[184:187], v[176:179], v[48:51]\n" \
    "v_mfma_f32_16x16x32_bf16 v[52:55], v[184:187], v[180:183], v[52:55]\n" \
    "s_waitcnt lgkmcnt(10)\n" \
    "v_mfma_f32_16x16x32_bf16 v[56:59], v[188:191], v[168:171], v[56:59]\n" \
    "v_mfma_f32_16x16x32_bf16 v[60:63], v[188:191], v[172:175], v[60:63]\n" \
    "v_mfma_f32_16x16x32_bf16 v[64:67], v[188:191], v[176:179], v[64:67]\n" \
    "v_mfma_f32_16x16x32_bf16 v[68:71], v[188:191], v[180:183], v[68:71]\n" \
    "s_waitcnt lgkmcnt(8)\n" \
    "MFK 72, 192, 168\n" \
    "MFK 76, 192, 172\n" \
    "MFK 80, 192, 176\n" \
    "MFK 84, 192, 180\n" \
    "s_waitcnt lgkmcnt(6)\n" \
    "MFK 88, 196, 168\n" \
    "MFK 92, 196, 172\n" \
    "MFK 96, 196, 176\n" \
    "MFK 100, 196, 180\n" \
    "s_waitcnt lgkmcnt(4)\n" \
    "v_mfma_f32_16x16x32_bf16 v[104:107], v[168:171], v[200:203], v[104:107]\n" \
    "v_mfma_f32_16x16x32_bf16 v[108:111], v[172:175], v[200:203], v[108:111]\n" \
    "v_mfma_f32_16x16x32_bf16 v[112:115], v[176:179], v[200:203], v[112:115]\n" \
    "v_mfma_f32_16x16x32_bf16 v[116:119], v[180:183], v[200:203], v[116:119]\n" \
    "s_waitcnt lgkmcnt(2)\n" \
    "v_mfma_f32_16x16x32_bf16 v[120:123], v[168:171], v[204:207], v[120:123]\n" \
    "v_mfma_f32_16x16x32_bf16 v[124:127], v[172:175], v[204:207], v[124:127]\n" \
    "v_mfma_f32_16x16x32_bf16 v[128:131], v[176:179], v[204:207], v[128:131]\n" \
    "v_mfma_f32_16x16x32_bf16 v[132:135], v[180:183], v[204:207], v[132:135]\n" \
    "s_waitcnt lgkmcnt(0)\n" \
    "v_mfma_f32_16x16x32_bf16 v[136:139], v[168:171], v[208:211], v[136:139]\n" \
    "v_mfma_f32_16x16x32_bf16 v[140:143], v[172:175], v[208:211], v[140:143]\n" \
    "v_mfma_f32_16x16x32_bf16 v[144:147], v[176:179], v[208:211], v[144:147]\n" \
    "v_mfma_f32_16x16x32_bf16 v[148:151], v[180:183], v[208:211], v[148:151]\n"

DI u32x4 fm_pack(f32x4 t0, f32x4 t1) {
    unsigned a0 = pk(t0[0], t0[1]), a1 = pk(t0[2], t0[3]), b0 = pk(t1[0], t1[1]), b1 = pk(t1[2], t1[3]);
    auto r0 = __builtin_amdgcn_permlane16_swap(a0, b0, false, false);
    auto r1 = __builtin_amdgcn_permlane16_swap(a1, b1, false, false);
    return (u32x4){r0[0], r1[0], r0[1], r1[1]};
}
DI u32x4 tm_pack(f32x4 t0, f32x4 t1) { return (u32x4){pk(t0[0], t0[1]), pk(t0[2], t0[3]), pk(t1[0], t1[1]), pk(t1[2], t1[3])}; }
DI f32x4 sub4(const f32x16& a, int r) { return (f32x4){a[4 * r], a[4 * r + 1], a[4 * r + 2], a[4 * r + 3]}; }

DI float nrm8(u32x4 v) {
    float s = __builtin_amdgcn_fdot2_f32_bf16(__builtin_bit_cast(bf16x2, v[0]), __builtin_bit_cast(bf16x2, v[0]), 0.f, false);
#pragma unroll
    for (int i = 1; i < 4; i++) s = __builtin_amdgcn_fdot2_f32_bf16(__builtin_bit_cast(bf16x2, v[i]), __builtin_bit_cast(bf16x2, v[i]), s, false);
    return s;
}
DI u32x4 pair_up(u32x4 vr, bool odd, unsigned selLo, unsigned selHi) {
    const unsigned a = odd ? vr[2] : vr[0], b = odd ? vr[3] : vr[1];
    const unsigned c = dppu<0xB1>(odd ? vr[0] : vr[2]), d = dppu<0xB1>(odd ? vr[1] : vr[3]);
    return (u32x4){__builtin_amdgcn_perm(a, c, selLo), __builtin_amdgcn_perm(a, c, selHi), __builtin_amdgcn_perm(b, d, selLo), __builtin_amdgcn_perm(b, d, selHi)};
}

DI void p1_loader(const char* X, const char* W, unsigned c0, unsigned c1, unsigned c2, int r, int rot, int lane, int lw) {
    unsigned xo, wo[7];
    { const int row = lw * 8 + (lane >> 3); xo = (unsigned)(r * 128 + row) * 1152u + (((lane & 7) ^ (row & 7)) << 4); }
#pragma unroll
    for (int i = 0; i < 4; i++) glds16(xo, X + rot * 128 + i * 32 * 1152, (lw + 4 * i) * 1024);
#pragma unroll
    for (int i = 0; i < 7; i++) {
        unsigned u = (lw + 4 * i) * 64u + lane; if (i == 6 && u >= 1728u) u = 0;
        const unsigned lr = (u * 2428u) >> 16, pc = u - lr * 27u;
        const unsigned k = (lr & ~7u) | ((lr & 1u) << 2) | ((lr >> 1) & 3u);
        wo[i] = k * 3456u + pc * 16u + (pc < 9 ? c0 : pc < 18 ? c1 : c2);
        if (i < 6 || lw < 3) glds16(wo[i], W + rot * 64 * 3456, 16384 + (lw + 4 * i) * 1024);
    }
    unsigned dst = ST1 + lw * 1024;
#pragma unroll 1
    for (int s = 1; s < 9; s++) {
        rot = rot == 8 ? 0 : rot + 1;
        const char* xb = X + rot * 128; const char* wb = W + rot * 64 * 3456;
#pragma unroll
        for (int i = 0; i < 4; i++) glds16(xo, xb + i * 32 * 1152, dst + i * 4096);
#pragma unroll
        for (int i = 0; i < 6; i++) glds16(wo[i], wb, dst + 16384 + i * 4096);
        if (lw < 3) glds16(wo[6], wb, dst + 16384 + 24576);
        dst = dst >= 2 * ST1 ? dst - 2 * ST1 : dst + ST1;
        WAITV_BAR(10);
    }
    WAITV_BAR(0);
}

template <int TY>
DI unsigned p1_consumer(char* ws, rsrc_t rs, int quad, int r, int lane, int w STAMP_ARGS) {
    constexpr bool ISB = TY == 2;
    const int rh = w & 1, pr = w >> 1, g = lane >> 4, m = lane & 15, q4 = m >> 2, p = m & 3;
    unsigned aW = 16384 + (g * 8 + q4 * 2) * 432 + pr * 72 + 8 * p;
    unsigned aR = 16384 + (g * 8 + q4 * 2) * 432 + pr * 72 + (p < 2 ? p : 2) * 144 + 32;
    unsigned aX0 = (64 * rh + m) * 128 + ((g ^ (m & 7)) << 4), aX1 = aX0 ^ 64;
    const int h0 = 4 * quad + 2 * pr, hA = 8 * (quad >> 1) + 2 * pr, T0 = 4 * r + 2 * rh;
    const int rho = ((m >> 2) << 3) + ((g & 1) << 2) + (m & 3);
    const unsigned fmoff = (g >> 1) * 512 + rho * 16, kvo = OFF_KV + (T0 >> 3) * CHUNK, u0 = T0 & 7, pofs = (8 * g + 4 * (m & 1)) * 4;
    const bool odd = lane & 1;
    unsigned selLo = odd ? 0x05040100u : 0x01000504u, selHi = odd ? 0x07060302u : 0x03020706u;
    unsigned oF, oV = 0, oR, oR1;
    if (ISB) {
        oF = OFF_Q1 + (32 + h0) * 65536 + T0 * 1024 + fmoff;
        oR = m < 4 ? OFF_Q2 + (32 + h0 + (m >> 1)) * 8192 + T0 * 128 + pofs : WS_USED; oR1 = oR + 128;
    } else {
        const int hr = hA + ((m >> 1) & 1) + (m & 4);
        oF = (TY == 0 ? OFF_Q1 + hA * 65536 + T0 * 1024 : kvo + hA * KV_HEAD + u0 * 1024) + fmoff;
        oV = kvo + h0 * KV_HEAD + 9216 + u0 * 1152 + g * 256 + ((m ^ ((g >> 1) << 3)) << 4);
        oR = (TY == 0 ? OFF_Q2 + hr * 8192 + T0 * 128 : kvo + hr * KV_HEAD + 8192 + u0 * 128) + pofs; oR1 = oR + 128;
        if (m >= 8) { oR = kvo + (h0 + ((m >> 1) & 1)) * KV_HEAD + 9216 + u0 * 1152 + 1024 + g * 32 + (m & 1) * 16; oR1 = oR + 1152; }
        if (m >= 12) { oR = WS_USED; oR1 = WS_USED; }
    }
    OPAQUE(oF); OPAQUE(oV); OPAQUE(oR); OPAQUE(oR1); OPAQUE(selLo); OPAQUE(selHi);
    f32x16 acc[7];
    unsigned cnt, ph, dl;
    asm volatile(
        ".macro MFK a, w, x\n"
        ".if %[isb]\n v_mfma_f32_16x16x32_bf16 v[\\a:\\a+3], v[\\x:\\x+3], v[\\w:\\w+3], v[\\a:\\a+3]\n"
        ".else\n v_mfma_f32_16x16x32_bf16 v[\\a:\\a+3], v[\\w:\\w+3], v[\\x:\\x+3], v[\\a:\\a+3]\n .endif\n .endm\n"
        ".set r_, 40\n .rept 56\n v_mov_b64 v[r_:r_+1], 0\n .set r_, r_+2\n .endr\n"
        "s_mov_b32 %[c], 8\n s_mov_b32 %[p], 0\n"
        "s_waitcnt lgkmcnt(0)\n s_barrier\n"
        P1_PRO
        "1:\n"
        P1_BODY
        "s_sub_u32 %[c], %[c], 1\n s_cmp_lg_u32 %[c], 0\n s_cbranch_scc1 1b\n"
        P1_TAIL
        "s_nop 7\n"
        ".purgem MFK\n"
        : "={v[40:55]}"(acc[0]), "={v[56:71]}"(acc[1]), "={v[72:87]}"(acc[2]), "={v[88:103]}"(acc[3]), "={v[104:119]}"(acc[4]),
          "={v[120:135]}"(acc[5]), "={v[136:151]}"(acc[6]), "+{v212}"(aX0), "+{v213}"(aX1), "+{v214}"(aW), "+{v215}"(aR),
          [c] "=&s"(cnt), [p] "=&s"(ph), [d] "=&s"(dl)
        : [neg] "s"(-2 * (int)ST1), [isb] "i"(ISB ? 1 : 0)
        : "memory", "scc", "v152", "v153", "v154", "v155", "v156", "v157", "v158", "v159", "v160", "v161", "v162", "v163", "v164", "v165",
          "v166", "v167", "v168", "v169", "v170", "v171", "v172", "v173", "v174", "v175", "v176", "v177", "v178", "v179", "v180", "v181",
          "v182", "v183", "v184", "v185", "v186", "v187", "v188", "v189", "v190", "v191", "v192", "v193", "v194", "v195", "v196", "v197",
          "v198", "v199", "v200", "v201", "v202", "v203", "v204", "v205", "v206", "v207", "v208", "v209", "v210", "v211");
    STAMP(11);
    if (!ISB) {
        constexpr unsigned HS = TY == 0 ? 65536 : KV_HEAD;
        const float rsc = TY == 0 && m < 8 ? QSCALE : 1.f;
        unsigned kn0 = 0, kn1 = 0, rm = 0;
#pragma unroll
        for (int tl = 0; tl < 2; tl++) {
            float nn[4];
#pragma unroll
            for (int jj = 0; jj < 4; jj++) {
                f32x4 t0 = sub4(acc[jj], 2 * tl), t1 = sub4(acc[jj], 2 * tl + 1);
                if (TY == 0) { t0 *= QSCALE; t1 *= QSCALE; }
                const u32x4 pc = fm_pack(t0, t1);
                st128s(rs, oF + tl * 1024, ((jj & 1) + 2 * (jj & 2)) * HS, pc);
                if (TY == 1) nn[jj] = nrm8(pc);
            }
#pragma unroll
            for (int jj = 0; jj < 2; jj++) st128s(rs, oV + tl * 1152, jj * KV_HEAD, tm_pack(sub4(acc[4 + jj], 2 * tl), sub4(acc[4 + jj], 2 * tl + 1)));
            f32x4 r0 = sub4(acc[6], 2 * tl), r1 = sub4(acc[6], 2 * tl + 1);
            if (TY == 0) { r0 *= rsc; r1 *= rsc; }
            const u32x4 vr = tm_pack(r0, r1), v = pair_up(vr, odd, selLo, selHi);
            st128(rs, tl ? oR1 : oR, m >= 8 ? vr : v);
            if (TY == 1) {
                kn0 = umax(kn0, __float_as_uint(swap32_add(nn[0], nn[1]))); kn1 = umax(kn1, __float_as_uint(swap32_add(nn[2], nn[3])));
#pragma unroll
                for (int i = 0; i < 4; i++)
                    rm = umax(rm, __float_as_uint(__builtin_amdgcn_fdot2_f32_bf16(__builtin_bit_cast(bf16x2, v[i]), __builtin_bit_cast(bf16x2, v[i]), 0.f, false)));
            }
        }
        if (TY == 0) return 0;
        kn0 = swap16_umax(row_umax(kn0)); kn1 = swap16_umax(row_umax(kn1));
        rm = swap32_umax(swap16_umax(umax(rm, dppu<0xB1>(rm))));
        unsigned kb = (__float_as_uint(__uint_as_float(m & 4 ? kn1 : kn0) + __uint_as_float(rm)) + 0x7ffffu) >> 19;
        kb = kb > 0xfffu ? 0xfffu : kb;
        const unsigned p0 = __builtin_amdgcn_readlane(kb, 0) | (__builtin_amdgcn_readlane(kb, 34) << 12);
        const unsigned p1 = __builtin_amdgcn_readlane(kb, 4) | (__builtin_amdgcn_readlane(kb, 38) << 12);
        return odd ? p1 : p0;
    } else {
        f32x4 M0 = {0, 0, 0, 0}, M1 = M0, M2 = M0, M3 = M0, M4 = M0, M5 = M0, M6 = M0;
#pragma unroll
        for (int tl = 0; tl < 2; tl++) {
            const f32x4 r0 = sub4(acc[6], 2 * tl), r1 = sub4(acc[6], 2 * tl + 1), p0 = phi4(r0), p1 = phi4(r1);
            const bf16x8 k0 = __builtin_bit_cast(bf16x8, tm_pack(phi4(sub4(acc[2], 2 * tl)), phi4(sub4(acc[2], 2 * tl + 1))));
            const bf16x8 k1 = __builtin_bit_cast(bf16x8, tm_pack(phi4(sub4(acc[3], 2 * tl)), phi4(sub4(acc[3], 2 * tl + 1))));
            const u32x4 kru = tm_pack(p0, p1);
            const bf16x8 kr = __builtin_bit_cast(bf16x8, kru);
            const bf16x8 v0 = __builtin_bit_cast(bf16x8, tm_pack(sub4(acc[4], 2 * tl), sub4(acc[4], 2 * tl + 1)));
            const bf16x8 v1 = __builtin_bit_cast(bf16x8, tm_pack(sub4(acc[5], 2 * tl), sub4(acc[5], 2 * tl + 1)));
            u32x4 vru = tm_pack(r0, r1);
            if (m >= 12) vru = (u32x4){ONES, ONES, ONES, ONES};
            const bf16x8 vr = __builtin_bit_cast(bf16x8, vru);
            M0 = mfma16(k0, v0, M0); M4 = mfma16(k1, v1, M4); M1 = mfma16(k0, vr, M1); M5 = mfma16(k1, vr, M5);
            M2 = mfma16(kr, v0, M2); M6 = mfma16(kr, v1, M6); M3 = mfma16(kr, vr, M3);
            st128(rs, tl ? oR1 : oR, pair_up(kru, odd, selLo, selHi));
        }
        const bool h1c = m == 10 || m == 11 || m == 13;
        f32x4 A2 = h1c ? M5 : M1, A3 = {M2[0], M2[1], M6[2], M6[3]};
        LAS f32x4* red = lds<f32x4>(L_RED1) + pr * 320 + lane;
        volatile LAS unsigned* hs = lds<unsigned>(L_FLAG + 4 + 4 * pr);
        if (rh) {
            red[0] = M0; red[64] = M4; red[128] = A2; red[192] = A3; red[256] = M3;
            asm volatile("s_waitcnt lgkmcnt(0)" ::: "memory");
            if (lane == 0) *hs = 1;
        }
#pragma unroll
        for (int tl = 0; tl < 2; tl++)
#pragma unroll
            for (int jj = 0; jj < 2; jj++) st128s(rs, oF + tl * 1024, jj * 65536, fm_pack(phi4(sub4(acc[jj], 2 * tl)), phi4(sub4(acc[jj], 2 * tl + 1))));
        if (!rh) {
            while (*hs == 0) __builtin_amdgcn_s_sleep(1);
            asm volatile("" ::: "memory");
            M0 += red[0]; M4 += red[64]; A2 += red[128]; A3 += red[192]; M3 += red[256];
            const unsigned kb = OFF_KVP + (h0 * 16 + r) * 1408, e = m >= 12 ? 18 : 16 + (m & 1);
            const bool ex = m >= 8 && m < 14;
            const unsigned kbx = kb + (h1c ? 16 * 1408 : 0);
            st128(rs, kb + m * 64 + g * 16, __builtin_bit_cast(u32x4, M0));
            st128(rs, kb + 16 * 1408 + m * 64 + g * 16, __builtin_bit_cast(u32x4, M4));
            st128(rs, ex ? kbx + e * 64 + g * 16 : WS_USED, __builtin_bit_cast(u32x4, A2));
            const u32x4 a3 = __builtin_bit_cast(u32x4, A3), m3 = __builtin_bit_cast(u32x4, M3);
            st64(rs, g == 1 ? kb + 1216 + m * 8 : WS_USED, (u32x2){a3[0], a3[1]});
            st64(rs, g == 1 ? kb + 16 * 1408 + 1216 + m * 8 : WS_USED, (u32x2){a3[2], a3[3]});
            st64(rs, g == 1 && ex ? kbx + 1216 + e * 8 : WS_USED, h1c ? (u32x2){m3[2], m3[3]} : (u32x2){m3[0], m3[1]});
        }
        return 0;
    }
}

#define ATT_MACROS \
    ".macro EXPS a, b\n .set r_, \\a\n .rept (\\b)-(\\a)\n v_exp_f32 v[r_], v[r_]\n .set r_, r_+1\n .endr\n .endm\n" \
    ".macro VRD dst, addr, off\n s_mov_b64 exec, %[vm]\n ds_read_b128 v[\\dst:\\dst+3], \\addr offset:\\off\n s_mov_b64 exec, -1\n .endm\n" \
    ".macro DEXP n, r\n"                                    \
    ".set pi_, (\\n)-3+5*(%[wm]-1)\n" \
    ".if (\\n) < 0 || (\\n) > 7 || ((\\n) < 3 && %[sn] < 0) || ((\\n) >= 3 && %[wm] == 0)\n v_exp_f32 v[\\r], v[\\r]\n" \
    ".elseif (\\n) == 0\n s_add_u32 m0, %[m2], %[sn]*18432\n v_exp_f32 v[\\r], v[\\r]\n global_load_lds_dwordx4 v190, %[sc] sc1\n" \
    ".elseif (\\n) == 1\n v_exp_f32 v[\\r], v[\\r]\n global_load_lds_dwordx4 v190, %[sc] offset:1024 sc1\n" \
    ".elseif (\\n) == 2\n s_add_u32 m0, %[m3], %[sn]*18432\n v_exp_f32 v[\\r], v[\\r]\n global_load_lds_dwordx4 v191, %[sc] sc1\n" \
    ".elseif pi_ < 8\n s_add_u32 s98, s96, 82944*pi_\n s_addc_u32 s99, s97, 0\n s_add_u32 m0, %[m1], 55296+8192*pi_+1024*(pi_&4)\n" \
    " v_exp_f32 v[\\r], v[\\r]\n global_load_lds_dwordx4 %[w0], s[98:99]\n" \
    ".elseif pi_ == 8\n s_mov_b32 m0, %[mr]\n v_exp_f32 v[\\r], v[\\r]\n global_load_lds_dwordx4 %[w1], s[96:97]\n" \
    ".else\n s_add_u32 m0, %[m1], 129024\n v_exp_f32 v[\\r], v[\\r]\n global_load_lds_dwordx4 %[w2], s[96:97]\n" \
    ".endif\n .endm\n" \
      \
    ".macro TILE C, N, o1, o0, kt, qk, prio, d\n" \
    ".if %[fair]\n s_setprio \\prio\n .endif\n" \
    "EXPS \\C, \\C+2\n" \
    "v_smfmac_f32_32x32x32_bf16 v[144:159], v[180:183], v[\\N+24:\\N+31], v184\n" \
    "VRD 180, v189, \\o1\n" \
    "EXPS \\C+2, \\C+5\n" \
    ".if \\qk\n s_waitcnt lgkmcnt(3)\n" \
    "v_mfma_f32_32x32x4_2b_bf16 v[\\N:\\N+31], v[174:175], v[168:169], 0 cbsz:1\n .endif\n" \
    ".if \\kt >= 0\n ds_read_b32 v174, v187 offset:%[sq]*18432+(\\kt)*128\n .endif\n" \
    "EXPS \\C+5, \\C+8\n DEXP \\d, \\C+8\n EXPS \\C+9, \\C+13\n" \
    ".if \\kt >= 0\n s_waitcnt lgkmcnt(2)\n .else\n s_waitcnt lgkmcnt(1)\n .endif\n" \
    "v_smfmac_f32_32x32x32_bf16 v[128:143], v[176:179], v[\\C:\\C+7], v184\n" \
    "EXPS \\C+13, \\C+18\n" \
    ".if \\kt >= 0\n s_waitcnt lgkmcnt(1)\n .else\n s_waitcnt lgkmcnt(0)\n .endif\n" \
    "v_smfmac_f32_32x32x32_bf16 v[128:143], v[180:183], v[\\C+8:\\C+15], v184\n" \
    "EXPS \\C+18, \\C+19\n DEXP \\d+1, \\C+19\n EXPS \\C+20, \\C+21\n" \
    ".if \\qk\n v_mfma_f32_32x32x16_bf16 v[\\N:\\N+15], v[170:173], v[160:163], v[\\N:\\N+15]\n .endif\n" \
    "EXPS \\C+21, \\C+25\n" \
    ".if \\qk\n v_mfma_f32_32x32x16_bf16 v[\\N+16:\\N+31], v[170:173], v[164:167], v[\\N+16:\\N+31]\n .endif\n" \
    ".if \\kt >= 0\n ds_read_b128 v[170:173], v186 offset:%[sq]*18432+(\\kt)*1024\n .endif\n" \
    "EXPS \\C+25, \\C+28\n" \
    "v_smfmac_f32_32x32x32_bf16 v[144:159], v[176:179], v[\\C+16:\\C+23], v184\n" \
    ".if \\o0 >= 0\n VRD 176, v188, \\o0\n .endif\n" \
    "EXPS \\C+28, \\C+30\n DEXP \\d+2, \\C+30\n EXPS \\C+31, \\C+32\n" \
    ".endm\n" \
    "s_mov_b32 s95, m0\n"
#define ATT_END "s_mov_b32 m0, s95\n .purgem EXPS\n .purgem VRD\n .purgem DEXP\n .purgem TILE\n"
typedef __attribute__((ext_vector_type(16))) unsigned u32x16;
typedef __attribute__((ext_vector_type(8))) unsigned u32x8;
struct Att {
    f32x32 S0, S1, O; u32x16 F; u32x8 q1; u32x2 q2; u32x4 ad; u32x2 vc;
    const char* kvsrc; unsigned m1, m2, m3, mr;
};
template <int J, int WM, int FAIR>
DI void att_block(Att& A, const char* Wo, unsigned w0, unsigned w1, unsigned w2) {
#define ATT_OPS : "+{v[64:95]}"(A.S0), "+{v[96:127]}"(A.S1), "+{v[128:159]}"(A.O), "+{v[170:185]}"(A.F) \
                : "{v[160:167]}"(A.q1), "{v[168:169]}"(A.q2), "{v[186:189]}"(A.ad), "{v[190:191]}"(A.vc), [vm] "s"(0x0003ffff0003ffffull), \
                  [sc] "s"(A.kvsrc + (J + 1) * CHUNK), [m1] "s"(A.m1), [m2] "s"(A.m2), [m3] "s"(A.m3), [mr] "s"(A.mr), "{s[96:97]}"(Wo), \
                  [w0] "v"(w0), [w1] "v"(w1), [w2] "v"(w2), [sp] "i"((J + 2) % 3), [sq] "i"(J % 3), [sn] "i"(J < 7 ? (J + 1) % 3 : -1), \
                  [wm] "i"(WM), [fair] "i"(FAIR) \
                : "memory", "scc", "s95", "s98", "s99"
    if constexpr (J == 0)
        asm volatile(ATT_MACROS
            "ds_read_b32 v174, v187\n ds_read_b128 v[170:173], v186\n"
            "s_waitcnt lgkmcnt(1)\n"
            "v_mfma_f32_32x32x4_2b_bf16 v[64:95], v[174:175], v[168:169], 0 cbsz:1\n"
            "ds_read_b32 v174, v187 offset:128\n"
            "s_waitcnt lgkmcnt(1)\n s_nop 15\n"
            "v_mfma_f32_32x32x16_bf16 v[64:79], v[170:173], v[160:163], v[64:79]\n"
            "v_mfma_f32_32x32x16_bf16 v[80:95], v[170:173], v[164:167], v[80:95]\n"
            "ds_read_b128 v[170:173], v186 offset:1024\n"
            "VRD 176, v188, 0\n"
            "s_nop 7\n"
            "TILE 64, 96, 0, 1152, 2, 1, 1, 0\n"
            "TILE 96, 64, 1152, 2304, 3, 1, 0, 9\n"
            ATT_END ATT_OPS);
    else if constexpr (J < 8)
        asm volatile(ATT_MACROS
            "TILE 64, 96, %[sp]*18432+2304, %[sp]*18432+3456, 0, 1, 2, 0\n"
            "TILE 96, 64, %[sp]*18432+3456, %[sq]*18432, 1, 1, 2, 3\n"
            "TILE 64, 96, %[sq]*18432, %[sq]*18432+1152, 2, 1, 1, 6\n"
            "TILE 96, 64, %[sq]*18432+1152, %[sq]*18432+2304, 3, 1, 0, 9\n"
            ATT_END ATT_OPS);
    else
        asm volatile(ATT_MACROS
            "TILE 64, 96, %[sp]*18432+2304, %[sp]*18432+3456, -1, 1, 0, 9\n"
            "TILE 96, 64, %[sp]*18432+3456, -1, -1, 0, 0, 9\n"
            "s_nop 1\n"
            "v_smfmac_f32_32x32x32_bf16 v[144:159], v[180:183], v[120:127], v184\n"
            "s_nop 15\n"
            ATT_END ATT_OPS);
}

DI void store_o(rsrc_t rs, int br, const f32x16& o, float eps, int h, int qt, int lane) {
    const int hh = lane >> 5, rho = lane & 31;
    auto dn = __builtin_amdgcn_permlane32_swap(__float_as_uint(o[10]), __float_as_uint(o[10]), false, false);
    const float sc = __builtin_amdgcn_rcpf(__uint_as_float(dn[0]) + eps);
    const int tok = (((rho >> 2) & 1) << 4) + ((rho >> 3) << 2) + (rho & 3);
    const unsigned base = OFF_O + (br * 128 + 2 * qt + (tok >> 4)) * 16384 + (tok & 15) * 16;
    const u32x4 v = {pk(o[0] * sc, o[1] * sc), pk(o[2] * sc, o[3] * sc), pk(o[4] * sc, o[5] * sc), pk(o[6] * sc, o[7] * sc)};
    st128(rs, base + (h >> 1) * 1024 + (2 * (h & 1) + hh) * 256, v);
    st32(rs, hh ? WS_USED : OFF_OR + ((br * 64 + qt) * 32 + h) * 128 + tok * 4, pk(o[8] * sc, o[9] * sc));
}

struct W3Src { unsigned om, orm, ob; };
DI W3Src w3src(int cb, int lane, int w) {
    const unsigned j = lane >> 3, colb = cb * 144 + ((((lane >> 1) & 3) ^ (((lane >> 4) & 1) + 2 * (w & 1))) << 5) + ((lane & 1) << 4);
    const unsigned hq = lane >> 4, jj = lane & 7;
    W3Src r;
    r.om = ((j & 3) + 8 * (j >> 2) + 4 * (w & 1) + 18 * (w >> 1) + (w & 2)) * 1152 + colb;
    r.orm = (72 * w + 18 * (j >> 1) + ((j & 2) ? 0 : 16) + (j & 1)) * 1152 + colb;
    r.ob = (72 * w + 18 * hq + 2 * (hq & 1) + (jj & 3) + 8 * (jj >> 2) + 4 * ((lane >> 3) & 1)) * 1152 + cb * 144 + 128;
    return r;
}
DI void w3_last_piece(const char* Wo, int cb, int lane) {
    const unsigned hr = lane >> 1;
    glds16((18 * hr + ((hr & 1) ? 0 : 16) + (lane & 1)) * 1152 + cb * 144 + 128, Wo, L_W3B + 8192);
}
DI void load_chunk(const char* kvsrc, int c, int slot, int lane, int lw) {
#pragma unroll
    for (int i = 0; i < 4; i++) glds16_dev(lane * 16, kvsrc + c * CHUNK + (lw + 4 * i) * 1024, slot * CHUNK + (lw + 4 * i) * 1024);
    if (lw < 2) glds16_dev(lane * 16, kvsrc + c * CHUNK + 16384 + lw * 1024, slot * CHUNK + 16384 + lw * 1024);
}
struct W3Addr { unsigned a[4], b, b8; };
DI bf16x8 w3frag(const W3Addr& A, int i, int t) {
    return t < 4 ? trfrag(A.a[t] + i * 4096, A.a[t] + i * 4096 + 512) : i < 8 ? trfrag(A.b + i * 512, A.b + i * 512 + 64) : trfrag(A.b8, A.b8 + 64);
}
DI W3Addr w3addr(int kh, int lane) {
    const int g = lane >> 4, m = lane & 15;
    W3Addr A;
    const unsigned base = L_W3 + kh * 9 * 4096 + g * 1024 + (m >> 2) * 128 + 8 * (m & 3), key = ((m >> 3) + 2 * (g & 1)) << 5;
#pragma unroll
    for (int t = 0; t < 4; t++) { A.a[t] = base + ((t * 32) ^ key); OPAQUE(A.a[t]); }
    A.b = L_W3B + kh * 4096 + (8 * g + (m >> 2)) * 16 + 8 * (m & 1); OPAQUE(A.b);
    A.b8 = A.b + 8192 - kh * 3584;
    return A;
}
DI void load_af(u32x4 (&af)[9], rsrc_t rs, int br, int rtile, int kh, int lane) {
    const unsigned a = OFF_O + (br * 128 + rtile) * 16384 + kh * 8192 + lane * 16;
    const unsigned o = OFF_OR + ((br * 64 + (rtile >> 1)) * 32 + 16 * kh + 4 * (lane >> 4)) * 128 + (16 * (rtile & 1) + (lane & 15)) * 4;
#pragma unroll
    for (int i = 0; i < 8; i++) af[i] = ld128(rs, a + i * 1024);
#pragma unroll
    for (int j = 0; j < 4; j++) af[8][j] = ld32(rs, o + j * 128);
}
DI void gemm3(f32x4 (&acc)[5], const u32x4 (&af)[9], int kh, int lane) {
    __builtin_amdgcn_s_setprio(3);
    const W3Addr A = w3addr(kh, lane);
    bf16x8 wf[2][5];
#pragma unroll
    for (int t = 0; t < 5; t++) { wf[0][t] = w3frag(A, 0, t); acc[t] = (f32x4){0, 0, 0, 0}; }
    __builtin_amdgcn_sched_barrier(0);
#pragma unroll
    for (int i = 0; i < 9; i++) {
        if (i < 8) {
#pragma unroll
            for (int t = 0; t < 5; t++) wf[(i + 1) & 1][t] = w3frag(A, i + 1, t);
        }
#pragma unroll
        for (int t = 0; t < 5; t++) acc[t] = mfma16(wf[i & 1][t], __builtin_bit_cast(bf16x8, af[i]), acc[t]);
#pragma unroll
        for (int t = 0; t < 5; t++) { __builtin_amdgcn_sched_group_barrier(0x100, 2, 0); __builtin_amdgcn_sched_group_barrier(0x8, 1, 0); }
        __builtin_amdgcn_sched_barrier(0);
    }
    __builtin_amdgcn_s_setprio(0);
}

__global__ void __launch_bounds__(512, 2) fused(const char* X, const char* WqA, const char* WqB, const char* WoA, const char* WoB,
                                                 const unsigned* gw, char* ws, char* out) {
    const int tid = threadIdx.x, lane = tid & 63, w = __builtin_amdgcn_readfirstlane(tid >> 6);
    const int b = blockIdx.x, xcd = b & 7, idx = b >> 3;
    const rsrc_t rs = __builtin_amdgcn_make_buffer_rsrc(ws, 0, WS_USED, 0x00020000);
    STAMP_DECL;
    STAMP(0);
    const u64 epoch = __hip_atomic_load((gu64*)(size_t)(ws + OFF_EPOCH), RLX_AGENT);
    const int h = xcd * 4 + (idx & 3), qb = idx >> 2;
    const int rb = xcd * 4 + (idx >> 3), cb = idx & 7;
    const int qg = w & 3, kh = w >> 2, hh = lane >> 5, rho = lane & 31, g = lane >> 4, m = lane & 15;
    const bool young = kh;
    const char* kvsrc = ws + OFF_KV + (size_t)h * KV_HEAD;
    const int qt0 = 8 * qb + 2 * qg, qtf = qt0 + kh;
    const int br = idx & 1, quad = (xcd & 3) * 2 + ((idx >> 1) & 1), r = (xcd >> 2) * 8 + (idx >> 2);
    u64 tag;
    Att A;
    if (young) {
        const unsigned c2 = 2016 + quad * 144, c0 = br ? quad * 144 : (quad & 1) * 1152 + (quad >> 1) * 288, c1 = br ? c0 + 1008 : c0;
        const int rot = ((idx >> 2) + 2 * (idx & 3)) % 9;
        p1_loader(X, br ? WqB : WqA, c0, c1, c2, r, rot, lane, qg);
        STAMP(10);
        tag = make_tag(epoch);
        {
            const int row = 256 * qb + 8 * h + 2 * qg + hh;
            const unsigned* xr = (const unsigned*)(X + (size_t)row * 1152) + rho;
            float s = 0.f;
#pragma unroll
            for (int i = 0; i < 9; i++) s = __builtin_amdgcn_fdot2_f32_bf16(__builtin_bit_cast(bf16x2, xr[32 * i]), __builtin_bit_cast(bf16x2, gw[rho + 32 * i]), s, false);
            s = row_sum(s); s = swap16_add(s, s);
            if (rho == 0) st32(rs, OFF_GATE + row * 4, __float_as_uint(__builtin_amdgcn_rcpf(1.f + __expf(-s))));
        }
        STAMP(13);
        volatile LAS unsigned* go = lds<unsigned>(L_FLAG + 12);
        unsigned kmax;
        if (qg == 0) {
            const u64 f = wait_flags<63, 4>(ws + OFF_FA + (h >> 1) * 512, tag, ~tag, lane);
            kmax = swap32_umax(swap16_umax(row_umax((((unsigned)f >> (12 * (h & 1))) & 0xfffu) << 19))) | 1u;
            *go = kmax;
        } else {
            while ((kmax = *go) == 0) __builtin_amdgcn_s_sleep(1);
            asm volatile("" ::: "memory");
        }
        STAMP(14);
        const u32x4 q1a = ld128(rs, OFF_Q1 + h * 65536 + qt0 * 1024 + lane * 16), q1b = ld128(rs, OFF_Q1 + h * 65536 + qt0 * 1024 + 1024 + lane * 16);
        const unsigned q2r = ld32(rs, OFF_Q2 + h * 8192 + qt0 * 128 + lane * 4);
        load_chunk(kvsrc, 0, 0, lane, qg);
        const float n = __builtin_amdgcn_fdot2_f32_bf16(__builtin_bit_cast(bf16x2, q2r), __builtin_bit_cast(bf16x2, q2r), swap32_add(nrm8(q1a), nrm8(q1b)), false);
        const float mneg = -1.01f * __builtin_amdgcn_sqrtf(n * __uint_as_float(kmax));
        A.q2 = (u32x2){q2r, pk(mneg, 0.f)};
        A.q1 = __builtin_shufflevector(q1a, q1b, 0, 1, 2, 3, 4, 5, 6, 7);
        lds<u32x4>(L_EX)[qg * 128 + lane] = q1a; lds<u32x4>(L_EX)[qg * 128 + 64 + lane] = q1b; lds<u32x2>(L_EX + 8192)[qg * 64 + lane] = A.q2;
    } else {
        if (tid < 4) lds<unsigned>(L_FLAG)[tid] = 0;
        const unsigned pay = br ? p1_consumer<2>(ws, rs, quad, r, lane, w STAMP_PASS)
                                : (quad & 1) ? p1_consumer<1>(ws, rs, quad, r, lane, w STAMP_PASS) : p1_consumer<0>(ws, rs, quad, r, lane, w STAMP_PASS);
        STAMP(12);
        tag = make_tag(epoch);
        const unsigned fo = br ? OFF_F1 + ((quad * 2 + (w >> 1)) * 32 + 2 * r + (w & 1)) * 8
                               : OFF_FA + ((4 * (quad >> 1) + (w >> 1) + 2 * lane) * 64 + (quad & 1) * 32 + 2 * r + (w & 1)) * 8;
        WAITV();
        if (lane < 2 - br) __hip_atomic_store((gu64*)(size_t)(ws + fo), tag | pay, RLX_AGENT);
        STAMP(13);
    }
    {
        const unsigned vinit = rho == 18 ? ONES : 0u;
        asm volatile(".set r_, 120\n .rept 20\n v_mov_b64 v[r_:r_+1], 0\n .set r_, r_+2\n .endr\n"
                     ".set r_, 176\n .rept 8\n v_mov_b32 v[r_], %4\n .set r_, r_+1\n .endr\n"
                     "v_mov_b32 v175, 0x3f803f80\n v_mov_b32 v184, 0xdddddddd\n"
                     : "={v[64:95]}"(A.S0), "={v[96:127]}"(A.S1), "={v[128:159]}"(A.O), "=&{v[170:185]}"(A.F) : "{v192}"(vinit));
    }
    const unsigned bK = kh * 4096 + lane * 16, bK2 = 8192 + kh * 512 + rho * 4, bV = 9216 + kh * 4608 + (rho < 16 ? hh * 256 + rho * 16 : 768 + hh * 32 + rho * 16);
    const unsigned dV = rho < 8 ? 640 : rho < 16 ? 384 : 64;
    A.ad = (u32x4){bK, bK2, bV, bV + dV};
    A.kvsrc = kvsrc; A.m1 = w * 1024; A.m2 = w * 2048; A.m3 = 16384 + (w & 1) * 1024; A.mr = L_W3 + (32 + w + 8 * (w & 4)) * 1024;
    A.vc = (u32x2){lane * 16 + A.m2, lane * 16 + A.m3};
    asm volatile("" : "+{v[186:189]}"(A.ad), "+{v[190:191]}"(A.vc));
    W3Src ws3 = w3src(cb, lane, w);
    OPAQUE(ws3.om); OPAQUE(ws3.orm); OPAQUE(ws3.ob);
    STAMP(16);
    WAITV_BAR(0);
    STAMP(17);
    u64 seen;
    if (!young) {
        seen = peek_flags<31>(ws + OFF_F1 + (h >> 1) * 256, lane);
        const u32x4 q1a = lds<u32x4>(L_EX)[qg * 128 + lane], q1b = lds<u32x4>(L_EX)[qg * 128 + 64 + lane];
        A.q1 = __builtin_shufflevector(q1a, q1b, 0, 1, 2, 3, 4, 5, 6, 7); A.q2 = lds<u32x2>(L_EX + 8192)[qg * 64 + lane];
    }
    att_block<0, 0, 0>(A, WoB, 0, 0, 0);
    WAITV();
    u32x4 kvp[8];
    if (!young) {
        wait_flags<31, 4>(ws + OFF_F1 + (h >> 1) * 256, tag, seen, lane);
        const unsigned o = rho < 22 ? OFF_KVP + (h * 16 + hh) * 1408 + (qg * 22 + rho) * 16 : WS_USED;
#pragma unroll
        for (int i = 0; i < 8; i++) kvp[i] = ld128(rs, o + i * 2816);
        if (w == 0) w3_last_piece(WoB, cb, lane);
    }
    BAR();
    STAMP(18);
    if (young) {
        kvp[0] = ld128(rs, OFF_Q1 + (32 + h) * 65536 + qt0 * 1024 + lane * 16);
        kvp[1] = ld128(rs, OFF_Q1 + (32 + h) * 65536 + qt0 * 1024 + 1024 + lane * 16);
        kvp[2][0] = ld32(rs, OFF_Q2 + (32 + h) * 8192 + qt0 * 128 + lane * 4);
    }
    att_block<1, 1, 0>(A, WoB, ws3.om, 0, 0);
    WAITV();
    if (!young) {
        f32x4 s4 = __builtin_bit_cast(f32x4, kvp[0]);
#pragma unroll
        for (int i = 1; i < 8; i++) s4 += __builtin_bit_cast(f32x4, kvp[i]);
        const float lo = swap32_add(s4[0], s4[2]), hi = swap32_add(s4[1], s4[3]);
        const int u = qg * 22 + rho;
        if (rho < 22) *lds<unsigned>(L_KVT + u * 8 + hh * 4 + (u < 76 ? 0 : 32)) = pk(lo, hi);
        asm volatile("s_waitcnt lgkmcnt(0)" ::: "memory");
    }
    BAR();
    STAMP(19);
    if (young) {
        __builtin_amdgcn_s_setprio(3);
        const int e = rho < 19 ? rho : 18;
        const bf16x8 a1 = ldsr128(L_KVT + e * 32 + hh * 16);
        const u32x2 a2 = {*lds<unsigned>(L_KVT + 640 + e * 4), 0u};
        const u32x2 b2 = {kvp[2][0], 0u};
        f32x32 o = {};
        o = __builtin_amdgcn_mfma_f32_32x32x4bf16_1k(__builtin_bit_cast(s16x4, a2), __builtin_bit_cast(s16x4, b2), o, 1, 0, 0);
        f32x16 oa = __builtin_shufflevector(o, o, 0, 1, 2, 3, 4, 5, 6, 7, 8, 9, 10, 11, 12, 13, 14, 15);
        f32x16 ob = __builtin_shufflevector(o, o, 16, 17, 18, 19, 20, 21, 22, 23, 24, 25, 26, 27, 28, 29, 30, 31);
        oa = __builtin_amdgcn_mfma_f32_32x32x16_bf16(a1, __builtin_bit_cast(bf16x8, kvp[0]), oa, 0, 0, 0);
        ob = __builtin_amdgcn_mfma_f32_32x32x16_bf16(a1, __builtin_bit_cast(bf16x8, kvp[1]), ob, 0, 0, 0);
        store_o(rs, 1, oa, 1e-6f, h, qt0, lane);
        store_o(rs, 1, ob, 1e-6f, h, qt0 + 1, lane);
        __builtin_amdgcn_s_setprio(0);
    }
    att_block<2, 2, 1>(A, WoB, ws3.om, ws3.orm, ws3.ob);
    WAITV();
    if (young && lane < 2) __hip_atomic_store((gu64*)(size_t)(ws + OFF_FB) + (qt0 + lane) * 32 + h, tag, RLX_AGENT);
    BAR();
    STAMP(20);
    const int rt = w & 3, qt3 = 2 * rb + (rt >> 1);
    att_block<3, 0, 0>(A, WoB, 0, 0, 0);
    WAITV();
    u32x4 af[9];
    float gate;
    if (!young) {
        wait_flags<31, 4>(ws + OFF_FB + qt3 * 256, tag, ~tag, lane);
        load_af(af, rs, 1, rb * 4 + rt, kh, lane);
        gate = __uint_as_float(ld32(rs, OFF_GATE + (rb * 64 + rt * 16 + m) * 4));
        if (b == 0 && tid == 0) __hip_atomic_store((gu64*)(size_t)(ws + OFF_EPOCH), epoch + 1, RLX_AGENT);
    }
    BAR();
    STAMP(21);
    if (young) {
        load_af(af, rs, 1, rb * 4 + rt, kh, lane);
        gate = __uint_as_float(ld32(rs, OFF_GATE + (rb * 64 + rt * 16 + m) * 4));
    }
    att_block<4, 0, 0>(A, WoB, 0, 0, 0);
    WAITV();
    f32x4 accb[5];
    if (!young) gemm3(accb, af, kh, lane);
    BAR();
    STAMP(22);
    if (young) gemm3(accb, af, kh, lane);
    att_block<5, 0, 1>(A, WoB, 0, 0, 0);
    WAITV();
    BAR();
    STAMP(23);
    ws3 = w3src(cb, lane, w);
    if (w == 4) w3_last_piece(WoA, cb, lane);
    att_block<6, 1, 1>(A, WoA, ws3.om, 0, 0);
    asm volatile("s_waitcnt vmcnt(5)\n\ts_barrier" ::: "memory");
    STAMP(24);
    att_block<7, 2, 1>(A, WoA, ws3.om, ws3.orm, ws3.ob);
    att_block<8, 0, 0>(A, WoA, 0, 0, 0);
    STAMP(27);
    f32x16 Oa = __builtin_shufflevector(A.O, A.O, 0, 1, 2, 3, 4, 5, 6, 7, 8, 9, 10, 11, 12, 13, 14, 15);
    f32x16 Ob = __builtin_shufflevector(A.O, A.O, 16, 17, 18, 19, 20, 21, 22, 23, 24, 25, 26, 27, 28, 29, 30, 31);
    LAS f32x4* ex = lds<f32x4>(L_EX);
    {
        const float thr = 9.094947e-13f;
        const bool bad = hh == 0 && !(Oa[10] >= thr && Oa[10] < 1e30f && Ob[10] >= thr && Ob[10] < 1e30f);
        if (__any(bad) && lane == 0) *(volatile LAS unsigned*)lds<unsigned>(L_FLAG) = 1;
#pragma unroll
        for (int i = 0; i < 3; i++) ex[(w * 3 + i) * 64 + lane] = kh ? sub4(Oa, i) : sub4(Ob, i);
        WAITV_BAR(0);
        if (*(volatile LAS unsigned*)lds<unsigned>(L_FLAG)) {
            const u32x2 b2 = {A.q2[0], 0u};
            const u32x4 q1a = __builtin_shufflevector(A.q1, A.q1, 0, 1, 2, 3), q1b = __builtin_shufflevector(A.q1, A.q1, 4, 5, 6, 7);
            float xa = -INFINITY, xb = -INFINITY;
            for (int pass = 0; pass < 2; pass++) {
                for (int c = 0; c < 8; c++) {
                    LBAR();
                    if (young) load_chunk(kvsrc, c, 0, lane, qg);
                    WAITV_BAR(0);
                    for (int t = 0; t < 4; t++) {
                        const bf16x8 kf = ldsr128(bK + t * 1024);
                        const u32x2 k2 = {*lds<unsigned>(bK2 + t * 128), 0u};
                        f32x32 s = {};
                        s = __builtin_amdgcn_mfma_f32_32x32x4bf16_1k(__builtin_bit_cast(s16x4, k2), __builtin_bit_cast(s16x4, b2), s, 1, 0, 0);
                        f32x16 sa = __builtin_shufflevector(s, s, 0, 1, 2, 3, 4, 5, 6, 7, 8, 9, 10, 11, 12, 13, 14, 15);
                        f32x16 sb = __builtin_shufflevector(s, s, 16, 17, 18, 19, 20, 21, 22, 23, 24, 25, 26, 27, 28, 29, 30, 31);
                        sa = __builtin_amdgcn_mfma_f32_32x32x16_bf16(kf, __builtin_bit_cast(bf16x8, q1a), sa, 0, 0, 0);
                        sb = __builtin_amdgcn_mfma_f32_32x32x16_bf16(kf, __builtin_bit_cast(bf16x8, q1b), sb, 0, 0, 0);
                        if (pass == 0) {
#pragma unroll
                            for (int i = 0; i < 16; i++) { xa = fmaxf(xa, sa[i]); xb = fmaxf(xb, sb[i]); }
                        } else {
#pragma unroll
                            for (int i = 0; i < 16; i++) { sa[i] = __builtin_amdgcn_exp2f(sa[i] - xa); sb[i] = __builtin_amdgcn_exp2f(sb[i] - xb); }
#pragma unroll
                            for (int st = 0; st < 2; st++) {
                                u32x4 vf = {ONES, ONES, ONES, ONES};
                                if (rho < 18) vf = __builtin_bit_cast(u32x4, ldsr128(bV + t * 1152 + st * dV));
                                f32x8 pa, pb;
#pragma unroll
                                for (int i = 0; i < 8; i++) { pa[i] = sa[8 * st + i]; pb[i] = sb[8 * st + i]; }
                                Oa = __builtin_amdgcn_smfmac_f32_32x32x32_bf16(__builtin_bit_cast(bf16x8, vf), __builtin_bit_cast(bf16x16, pa), Oa, (int)0xdddddddd, 0, 0);
                                Ob = __builtin_amdgcn_smfmac_f32_32x32x32_bf16(__builtin_bit_cast(bf16x8, vf), __builtin_bit_cast(bf16x16, pb), Ob, (int)0xdddddddd, 0, 0);
                            }
                        }
                    }
                }
                if (pass == 0) {
                    xa = swap32_max(xa); xb = swap32_max(xb);
                    LAS float* mx = lds<float>(L_EX);
                    mx[w * 64 + lane] = hh ? xb : xa;
                    LBAR();
                    xa = fmaxf(xa, mx[(w ^ 4) * 64 + rho]); xb = fmaxf(xb, mx[(w ^ 4) * 64 + 32 + rho]);
#pragma unroll
                    for (int i = 0; i < 16; i++) { Oa[i] = 0.f; Ob[i] = 0.f; }
                }
            }
#pragma unroll
            for (int i = 0; i < 3; i++) ex[(w * 3 + i) * 64 + lane] = kh ? sub4(Oa, i) : sub4(Ob, i);
            LBAR();
        }
    }
    {
        f32x16 mine = kh ? Ob : Oa;
#pragma unroll
        for (int i = 0; i < 3; i++) {
            const f32x4 v = ex[((w ^ 4) * 3 + i) * 64 + lane];
#pragma unroll
            for (int k = 0; k < 4; k++) mine[4 * i + k] += v[k];
        }
        store_o(rs, 0, mine, 0.f, h, qtf, lane);
    }
    STAMP(28);
    const W3Addr WA = w3addr(kh, lane);
    bf16x8 wf[7][5];
#pragma unroll
    for (int i = 0; i < 3; i++)
#pragma unroll
        for (int t = 0; t < 5; t++) wf[i][t] = w3frag(WA, i, t);
    post_flag(ws + OFF_F2 + (qtf * 32 + h) * 8, tag, lane);
    STAMP(29);
#pragma unroll
    for (int i = 3; i < 7; i++)
#pragma unroll
        for (int t = 0; t < 5; t++) wf[i][t] = w3frag(WA, i, t);
    wait_flags<15, 4>(ws + OFF_F2 + qt3 * 256 + kh * 128, tag, ~tag, lane);
    STAMP(30);
    load_af(af, rs, 0, rb * 4 + rt, kh, lane);
    __builtin_amdgcn_sched_barrier(0);
    f32x4 acc[5];
#pragma unroll
    for (int i = 0; i < 9; i++) {
#pragma unroll
        for (int t = 0; t < 5; t++) acc[t] = mfma16(wf[i % 7][t], __builtin_bit_cast(bf16x8, af[i]), i ? acc[t] : (f32x4){0, 0, 0, 0});
        if (i < 2) {
#pragma unroll
            for (int t = 0; t < 5; t++) wf[i][t] = w3frag(WA, 7 + i, t);
        }
        __builtin_amdgcn_sched_barrier(0);
    }
    STAMP(31);
    LAS f32x4* red = lds<f32x4>(0);
#pragma unroll
    for (int t = 0; t < 5; t++) acc[t] = acc[t] * gate + accb[t] * (1.f - gate);
#pragma unroll
    for (int t = 0; t < 5; t++) if ((t < 3) == (kh == 1)) red[(rt * 5 + t) * 64 + lane] = acc[t];
    LBAR();
    char* orow = out + (size_t)(rb * 64 + rt * 16 + m) * 1152 + cb * 144 + g * 8;
#pragma unroll
    for (int t = 0; t < 5; t++)
        if ((t < 3) == (kh == 0)) {
            const f32x4 v = acc[t] + red[(rt * 5 + t) * 64 + lane];
            if (t < 4 || g < 2) *(u32x2*)(orow + t * 32) = (u32x2){pk(v[0], v[1]), pk(v[2], v[3])};
        }
    STAMP(32);
    STAMP_OUT;
}

extern "C" void kernel_launch(void* const* d_in, const int* in_sizes, int n_in, void* d_out, int out_size, void* d_ws, size_t ws_size,
                              hipStream_t stream) {
    (void)hipFuncSetAttribute((const void*)fused, hipFuncAttributeMaxDynamicSharedMemorySize, LDS_BYTES);
    fused<<<256, 512, LDS_BYTES, stream>>>((const char*)d_in[0], (const char*)d_in[1], (const char*)d_in[4], (const char*)d_in[2],
                                          (const char*)d_in[5], (const unsigned*)d_in[6], (char*)d_ws, (char*)d_out);
}
```

```cpp
#include <hip/hip_runtime.h>
#include <stdint.h>

typedef __attribute__((ext_vector_type(8))) __bf16 bf16x8;
typedef __attribute__((ext_vector_type(16))) __bf16 bf16x16;
typedef __attribute__((ext_vector_type(2))) __bf16 bf16x2;
typedef __attribute__((ext_vector_type(4))) short s16x4;
typedef __attribute__((ext_vector_type(2))) float f32x2;
typedef __attribute__((ext_vector_type(4))) float f32x4;
typedef __attribute__((ext_vector_type(8))) float f32x8;
typedef __attribute__((ext_vector_type(16))) float f32x16;
typedef __attribute__((ext_vector_type(32))) float f32x32;
typedef __attribute__((ext_vector_type(4))) unsigned u32x4;
typedef __attribute__((ext_vector_type(2))) unsigned u32x2;
typedef unsigned long long u64;
typedef __attribute__((address_space(1))) u64 gu64;
#define LAS __attribute__((address_space(3)))
#define DI __device__ __forceinline__
#define RLX_AGENT __ATOMIC_RELAXED, __HIP_MEMORY_SCOPE_AGENT

constexpr unsigned CHUNK = 18432;
constexpr unsigned KV_HEAD = 8 * CHUNK;
constexpr unsigned OFF_EPOCH = 0;
constexpr unsigned OFF_FA = 256;
constexpr unsigned OFF_F1 = OFF_FA + 8192;
constexpr unsigned OFF_FB = OFF_F1 + 4096;
constexpr unsigned OFF_F2 = OFF_FB + 16384;
constexpr unsigned OFF_GATE = OFF_F2 + 16384;
constexpr unsigned OFF_KVP = OFF_GATE + 8192;
constexpr unsigned OFF_Q2 = OFF_KVP + 32 * 16 * 1408;
constexpr unsigned OFF_Q1 = OFF_Q2 + 2 * 32 * 8192;
constexpr unsigned OFF_KV = OFF_Q1 + 2 * 32 * 65536;
constexpr unsigned OFF_O = OFF_KV + 32 * KV_HEAD;
constexpr unsigned OFF_OR = OFF_O + 2 * 128 * 16384;
constexpr unsigned WS_USED = OFF_OR + 2 * 64 * 4096;

constexpr unsigned ST1 = 44032;
constexpr unsigned L_RED1 = 57344;
constexpr unsigned L_W3 = 3 * CHUNK;
constexpr unsigned L_W3B = L_W3 + 73728;
constexpr unsigned L_EX = L_W3 + 82944;
constexpr unsigned L_KVT = L_EX + 24576;
constexpr unsigned L_FLAG = L_KVT + 768;
constexpr unsigned LDS_BYTES = 163840;

constexpr float QSCALE = 0.23570226039551584f * 1.4426950408889634f;
constexpr unsigned ONES = 0x3f803f80u;

#define STAMP(i)
#define STAMP_DECL
#define STAMP_ARGS
#define STAMP_PASS
#define STAMP_OUT

DI unsigned pk(float a, float b) { bf16x2 v = __builtin_convertvector((f32x2){a, b}, bf16x2); return __builtin_bit_cast(unsigned, v); }
DI float bflo(unsigned u) { return __uint_as_float(u << 16); }
DI float bfhi(unsigned u) { return __uint_as_float(u & 0xffff0000u); }
DI f32x4 phi4(f32x4 a) {
    const f32x4 s = a * 1.4426950408889634f;
    f32x4 e, x;
#pragma unroll
    for (int i = 0; i < 4; i++) {
        asm("v_exp_f32_e64 %0, %1 clamp" : "=v"(e[i]) : "v"(s[i]));
        asm("v_max_f32 %0, 0, %1" : "=v"(x[i]) : "v"(a[i]));
    }
    return (x + 1e-6f) + e;
}
DI void glds16(unsigned voff, const void* sbase, unsigned lds_dst) {
    unsigned keep;
    asm volatile("s_mov_b32 %0, m0\n\ts_mov_b32 m0, %3\n\ts_nop 0\n\tglobal_load_lds_dwordx4 %1, %2\n\ts_mov_b32 m0, %0"
                 : "=&s"(keep) : "v"(voff), "s"(sbase), "s"(lds_dst) : "memory");
}
DI void glds16_dev(unsigned voff, const void* sbase, unsigned lds_dst) {
    unsigned keep;
    asm volatile("s_mov_b32 %0, m0\n\ts_mov_b32 m0, %3\n\ts_nop 0\n\tglobal_load_lds_dwordx4 %1, %2 sc1\n\ts_mov_b32 m0, %0"
                 : "=&s"(keep) : "v"(voff), "s"(sbase), "s"(lds_dst) : "memory");
}
#define WAITV_BAR(n) asm volatile("s_waitcnt vmcnt(" #n ") lgkmcnt(0)\n\ts_barrier" ::: "memory")
#define LBAR() asm volatile("s_waitcnt lgkmcnt(0)\n\ts_barrier" ::: "memory")
#define BAR() asm volatile("s_barrier" ::: "memory")
#define WAITV() do { asm volatile("s_waitcnt vmcnt(0)" ::: "memory"); __builtin_amdgcn_s_waitcnt(0x0F70); } while (0)
#define OPAQUE(x) asm volatile("" : "+v"(x))
template <class T> DI LAS T* lds(unsigned a) { return (LAS T*)(size_t)a; }
DI bf16x8 ldsr128(unsigned a) { return *(const LAS bf16x8*)(size_t)a; }
DI s16x4 trr(unsigned a) { return __builtin_amdgcn_ds_read_tr16_b64_v4i16((LAS s16x4*)(size_t)a); }
DI bf16x8 trfrag(unsigned a, unsigned b) {
    s16x4 lo = trr(a), hi = trr(b);
    return __builtin_bit_cast(bf16x8, __builtin_shufflevector(lo, hi, 0, 1, 2, 3, 4, 5, 6, 7));
}
DI f32x4 mfma16(bf16x8 a, bf16x8 b, f32x4 c) { return __builtin_amdgcn_mfma_f32_16x16x32_bf16(a, b, c, 0, 0, 0); }
template <int CTRL> DI float dpp(float v) { return __int_as_float(__builtin_amdgcn_mov_dpp(__float_as_int(v), CTRL, 0xf, 0xf, true)); }
DI float swap16_add(float a, float b) { auto r = __builtin_amdgcn_permlane16_swap(__float_as_uint(a), __float_as_uint(b), false, false); return __uint_as_float(r[0]) + __uint_as_float(r[1]); }
DI float swap32_add(float a, float b) { auto r = __builtin_amdgcn_permlane32_swap(__float_as_uint(a), __float_as_uint(b), false, false); return __uint_as_float(r[0]) + __uint_as_float(r[1]); }
DI unsigned umax(unsigned a, unsigned b) { return a > b ? a : b; }
template <int CTRL> DI unsigned dppu(unsigned v) { return __builtin_amdgcn_mov_dpp(v, CTRL, 0xf, 0xf, true); }
DI unsigned swap16_umax(unsigned a) { auto r = __builtin_amdgcn_permlane16_swap(a, a, false, false); return umax(r[0], r[1]); }
DI unsigned swap32_umax(unsigned a) { auto r = __builtin_amdgcn_permlane32_swap(a, a, false, false); return umax(r[0], r[1]); }
DI unsigned row_umax(unsigned x) { x = umax(x, dppu<0xB1>(x)); x = umax(x, dppu<0x4E>(x)); x = umax(x, dppu<0x141>(x)); x = umax(x, dppu<0x140>(x)); return x; }
DI float swap32_max(float a) { auto r = __builtin_amdgcn_permlane32_swap(__float_as_uint(a), __float_as_uint(a), false, false); return fmaxf(__uint_as_float(r[0]), __uint_as_float(r[1])); }
DI float row_sum(float x) {
    x += dpp<0xB1>(x); x += dpp<0x4E>(x); x += dpp<0x141>(x); x += dpp<0x140>(x); return x; }

typedef __amdgpu_buffer_rsrc_t rsrc_t;
DI void st128(rsrc_t r, unsigned off, u32x4 v) { __builtin_amdgcn_raw_buffer_store_b128(v, r, off, 0, 16); }
DI void st128s(rsrc_t r, unsigned off, unsigned soff, u32x4 v) { st128(r, off + soff, v); }
DI void st64(rsrc_t r, unsigned off, u32x2 v) { __builtin_amdgcn_raw_buffer_store_b64(v, r, off, 0, 16); }
DI void st32(rsrc_t r, unsigned off, unsigned v) { __builtin_amdgcn_raw_buffer_store_b32(v, r, off, 0, 16); }
DI u32x4 ld128(rsrc_t r, unsigned off) { return __builtin_amdgcn_raw_buffer_load_b128(r, off, 0, 16); }
DI unsigned ld32(rsrc_t r, unsigned off) { return __builtin_amdgcn_raw_buffer_load_b32(r, off, 0, 16); }

template <int MASK, int SLEEP>
DI u64 wait_flags(const char* words, u64 tag, u64 seen, int lane) {
    gu64* p = (gu64*)(size_t)words + (lane & MASK);
    if (!__all((seen ^ tag) < 0x1000000ull)) {
        const u64 t0 = __builtin_amdgcn_s_memrealtime();
        for (;;) {
            seen = __hip_atomic_load(p, RLX_AGENT);
            if (__all((seen ^ tag) < 0x1000000ull)) break;
            __builtin_amdgcn_s_sleep(SLEEP);
            if (__builtin_amdgcn_s_memrealtime() - t0 > 2000000ull) break;
        }
    }
    asm volatile("" ::: "memory");
    return seen;
}
template <int MASK> DI u64 peek_flags(const char* words, int lane) { return __hip_atomic_load((gu64*)(size_t)words + (lane & MASK), RLX_AGENT); }
DI void post_flag(char* word, u64 val, int lane) {
    WAITV();
    if (lane == 0) __hip_atomic_store((gu64*)(size_t)word, val, RLX_AGENT);
}
DI u64 make_tag(u64 epoch) { return ((epoch + 1) * 0x9E3779B97F4A7C15ull) << 24; }

#define P1_PRO \
    "ds_read_b128 v[152:155], v212 offset:0\n" \
    "ds_read_b128 v[156:159], v212 offset:2048\n" \
    "ds_read_b128 v[160:163], v212 offset:4096\n" \
    "ds_read_b128 v[164:167], v212 offset:6144\n" \
    "ds_read_b64_tr_b16 v[184:185], v214 offset:0\n" \
    "ds_read_b64_tr_b16 v[186:187], v214 offset:432\n" \
    "ds_read_b64_tr_b16 v[188:189], v214 offset:40\n" \
    "ds_read_b64_tr_b16 v[190:191], v214 offset:472\n" \
    "ds_read_b64_tr_b16 v[192:193], v214 offset:144\n" \
    "ds_read_b64_tr_b16 v[194:195], v214 offset:576\n" \
    "ds_read_b64_tr_b16 v[196:197], v214 offset:184\n" \
    "ds_read_b64_tr_b16 v[198:199], v214 offset:616\n" \
    "ds_read_b64_tr_b16 v[200:201], v214 offset:288\n" \
    "ds_read_b64_tr_b16 v[202:203], v214 offset:720\n" \
    "ds_read_b64_tr_b16 v[204:205], v214 offset:328\n" \
    "ds_read_b64_tr_b16 v[206:207], v214 offset:760\n" \
    "ds_read_b64_tr_b16 v[208:209], v215 offset:0\n" \
    "ds_read_b64_tr_b16 v[210:211], v215 offset:432\n" \
    "s_waitcnt lgkmcnt(0)\n"
#define P1_BODY \
    "v_mfma_f32_16x16x32_bf16 v[40:43], v[184:187], v[152:155], v[40:43]\n" \
    "ds_read_b128 v[168:171], v213 offset:0\n" \
    "v_mfma_f32_16x16x32_bf16 v[44:47], v[184:187], v[156:159], v[44:47]\n" \
    "ds_read_b128 v[172:175], v213 offset:2048\n" \
    "v_mfma_f32_16x16x32_bf16 v[48:51], v[184:187], v[160:163], v[48:51]\n" \
    "ds_read_b128 v[176:179], v213 offset:4096\n" \
    "v_mfma_f32_16x16x32_bf16 v[52:55], v[184:187], v[164:167], v[52:55]\n" \
    "ds_read_b128 v[180:183], v213 offset:6144\n" \
    "v_mfma_f32_16x16x32_bf16 v[56:59], v[188:191], v[152:155], v[56:59]\n" \
    "ds_read_b64_tr_b16 v[184:185], v214 offset:13824\n" \
    "v_mfma_f32_16x16x32_bf16 v[60:63], v[188:191], v[156:159], v[60:63]\n" \
    "ds_read_b64_tr_b16 v[186:187], v214 offset:14256\n" \
    "v_mfma_f32_16x16x32_bf16 v[64:67], v[188:191], v[160:163], v[64:67]\n" \
    "v_mfma_f32_16x16x32_bf16 v[68:71], v[188:191], v[164:167], v[68:71]\n" \
    "MFK 72, 192, 152\n" \
    "ds_read_b64_tr_b16 v[188:189], v214 offset:13864\n" \
    "MFK 76, 192, 156\n" \
    "ds_read_b64_tr_b16 v[190:191], v214 offset:14296\n" \
    "MFK 80, 192, 160\n" \
    "MFK 84, 192, 164\n" \
    "MFK 88, 196, 152\n" \
    "ds_read_b64_tr_b16 v[192:193], v214 offset:13968\n" \
    "MFK 92, 196, 156\n" \
    "ds_read_b64_tr_b16 v[194:195], v214 offset:14400\n" \
    "MFK 96, 196, 160\n" \
    "MFK 100, 196, 164\n" \
    "v_mfma_f32_16x16x32_bf16 v[104:107], v[152:155], v[200:203], v[104:107]\n" \
    "ds_read_b64_tr_b16 v[196:197], v214 offset:14008\n" \
    "v_mfma_f32_16x16x32_bf16 v[108:111], v[156:159], v[200:203], v[108:111]\n" \
    "ds_read_b64_tr_b16 v[198:199], v214 offset:14440\n" \
    "v_mfma_f32_16x16x32_bf16 v[112:115], v[160:163], v[200:203], v[112:115]\n" \
    "v_mfma_f32_16x16x32_bf16 v[116:119], v[164:167], v[200:203], v[116:119]\n" \
    "v_mfma_f32_16x16x32_bf16 v[120:123], v[152:155], v[204:207], v[120:123]\n" \
    "ds_read_b64_tr_b16 v[200:201], v214 offset:14112\n" \
    "v_mfma_f32_16x16x32_bf16 v[124:127], v[156:159], v[204:207], v[124:127]\n" \
    "ds_read_b64_tr_b16 v[202:203], v214 offset:14544\n" \
    "v_mfma_f32_16x16x32_bf16 v[128:131], v[160:163], v[204:207], v[128:131]\n" \
    "v_mfma_f32_16x16x32_bf16 v[132:135], v[164:167], v[204:207], v[132:135]\n" \
    "v_mfma_f32_16x16x32_bf16 v[136:139], v[152:155], v[208:211], v[136:139]\n" \
    "ds_read_b64_tr_b16 v[204:205], v214 offset:14152\n" \
    "v_mfma_f32_16x16x32_bf16 v[140:143], v[156:159], v[208:211], v[140:143]\n" \
    "ds_read_b64_tr_b16 v[206:207], v214 offset:14584\n" \
    "v_mfma_f32_16x16x32_bf16 v[144:147], v[160:163], v[208:211], v[144:147]\n" \
    "v_mfma_f32_16x16x32_bf16 v[148:151], v[164:167], v[208:211], v[148:151]\n" \
    "s_waitcnt lgkmcnt(10)\n" \
    "v_mfma_f32_16x16x32_bf16 v[40:43], v[184:187], v[168:171], v[40:43]\n" \
    "ds_read_b64_tr_b16 v[208:209], v215 offset:13824\n" \
    "v_mfma_f32_16x16x32_bf16 v[44:47], v[184:187], v[172:175], v[44:47]\n" \
    "ds_read_b64_tr_b16 v[210:211], v215 offset:14256\n" \
    "v_mfma_f32_16x16x32_bf16 v[48:51], v[184:187], v[176:179], v[48:51]\n" \
    "v_mfma_f32_16x16x32_bf16 v[52:55], v[184:187], v[180:183], v[52:55]\n" \
    "s_waitcnt lgkmcnt(10)\n" \
    "v_mfma_f32_16x16x32_bf16 v[56:59], v[188:191], v[168:171], v[56:59]\n" \
    "v_mfma_f32_16x16x32_bf16 v[60:63], v[188:191], v[172:175], v[60:63]\n" \
    "v_mfma_f32_16x16x32_bf16 v[64:67], v[188:191], v[176:179], v[64:67]\n" \
    "v_mfma_f32_16x16x32_bf16 v[68:71], v[188:191], v[180:183], v[68:71]\n" \
    "s_waitcnt lgkmcnt(8)\n" \
    "MFK 72, 192, 168\n" \
    "MFK 76, 192, 172\n" \
    "MFK 80, 192, 176\n" \
    "MFK 84, 192, 180\n" \
    "s_waitcnt lgkmcnt(6)\n" \
    "MFK 88, 196, 168\n" \
    "MFK 92, 196, 172\n" \
    "MFK 96, 196, 176\n" \
    "MFK 100, 196, 180\n" \
    "s_waitcnt lgkmcnt(4)\n" \
    "v_mfma_f32_16x16x32_bf16 v[104:107], v[168:171], v[200:203], v[104:107]\n" \
    "v_mfma_f32_16x16x32_bf16 v[108:111], v[172:175], v[200:203], v[108:111]\n" \
    "v_mfma_f32_16x16x32_bf16 v[112:115], v[176:179], v[200:203], v[112:115]\n" \
    "v_mfma_f32_16x16x32_bf16 v[116:119], v[180:183], v[200:203], v[116:119]\n" \
    "s_waitcnt lgkmcnt(2)\n" \
    "v_mfma_f32_16x16x32_bf16 v[120:123], v[168:171], v[204:207], v[120:123]\n" \
    "v_mfma_f32_16x16x32_bf16 v[124:127], v[172:175], v[204:207], v[124:127]\n" \
    "v_mfma_f32_16x16x32_bf16 v[128:131], v[176:179], v[204:207], v[128:131]\n" \
    "v_mfma_f32_16x16x32_bf16 v[132:135], v[180:183], v[204:207], v[132:135]\n" \
    "s_waitcnt lgkmcnt(0)\n" \
    "v_mfma_f32_16x16x32_bf16 v[136:139], v[168:171], v[208:211], v[136:139]\n" \
    "v_mfma_f32_16x16x32_bf16 v[140:143], v[172:175], v[208:211], v[140:143]\n" \
    "v_mfma_f32_16x16x32_bf16 v[144:147], v[176:179], v[208:211], v[144:147]\n" \
    "v_mfma_f32_16x16x32_bf16 v[148:151], v[180:183], v[208:211], v[148:151]\n"
#define P1_TAIL \
    "v_mfma_f32_16x16x32_bf16 v[40:43], v[184:187], v[152:155], v[40:43]\n" \
    "ds_read_b128 v[168:171], v213 offset:0\n" \
    "v_mfma_f32_16x16x32_bf16 v[44:47], v[184:187], v[156:159], v[44:47]\n" \
    "ds_read_b128 v[172:175], v213 offset:2048\n" \
    "v_mfma_f32_16x16x32_bf16 v[48:51], v[184:187], v[160:163], v[48:51]\n" \
    "ds_read_b128 v[176:179], v213 offset:4096\n" \
    "v_mfma_f32_16x16x32_bf16 v[52:55], v[184:187], v[164:167], v[52:55]\n" \
    "ds_read_b128 v[180:183], v213 offset:6144\n" \
    "v_mfma_f32_16x16x32_bf16 v[56:59], v[188:191], v[152:155], v[56:59]\n" \
    "ds_read_b64_tr_b16 v[184:185], v214 offset:13824\n" \
    "v_mfma_f32_16x16x32_bf16 v[60:63], v[188:191], v[156:159], v[60:63]\n" \
    "ds_read_b64_tr_b16 v[186:187], v214 offset:14256\n" \
    "v_mfma_f32_16x16x32_bf16 v[64:67], v[188:191], v[160:163], v[64:67]\n" \
    "v_mfma_f32_16x16x32_bf16 v[68:71], v[188:191], v[164:167], v[68:71]\n" \
    "MFK 72, 192, 152\n" \
    "ds_read_b64_tr_b16 v[188:189], v214 offset:13864\n" \
    "MFK 76, 192, 156\n" \
    "ds_read_b64_tr_b16 v[190:191], v214 offset:14296\n" \
    "MFK 80, 192, 160\n" \
    "MFK 84, 192, 164\n" \
    "MFK 88, 196, 152\n" \
    "ds_read_b64_tr_b16 v[192:193], v214 offset:13968\n" \
    "MFK 92, 196, 156\n" \
    "ds_read_b64_tr_b16 v[194:195], v214 offset:14400\n" \
    "MFK 96, 196, 160\n" \
    "MFK 100, 196, 164\n" \
    "v_mfma_f32_16x16x32_bf16 v[104:107], v[152:155], v[200:203], v[104:107]\n" \
    "ds_read_b64_tr_b16 v[196:197], v214 offset:14008\n" \
    "v_mfma_f32_16x16x32_bf16 v[108:111], v[156:159], v[200:203], v[108:111]\n" \
    "ds_read_b64_tr_b16 v[198:199], v214 offset:14440\n" \
    "v_mfma_f32_16x16x32_bf16 v[112:115], v[160:163], v[200:203], v[112:115]\n" \
    "v_mfma_f32_16x16x32_bf16 v[116:119], v[164:167], v[200:203], v[116:119]\n" \
    "v_mfma_f32_16x16x32_bf16 v[120:123], v[152:155], v[204:207], v[120:123]\n" \
    "ds_read_b64_tr_b16 v[200:201], v214 offset:14112\n" \
    "v_mfma_f32_16x16x32_bf16 v[124:127], v[156:159], v[204:207], v[124:127]\n" \
    "ds_read_b64_tr_b16 v[202:203], v214 offset:14544\n" \
    "v_mfma_f32_16x16x32_bf16 v[128:131], v[160:163], v[204:207], v[128:131]\n" \
    "v_mfma_f32_16x16x32_bf16 v[132:135], v[164:167], v[204:207], v[132:135]\n" \
    "v_mfma_f32_16x16x32_bf16 v[136:139], v[152:155], v[208:211], v[136:139]\n" \
    "ds_read_b64_tr_b16 v[204:205], v214 offset:14152\n" \
    "v_mfma_f32_16x16x32_bf16 v[140:143], v[156:159], v[208:211], v[140:143]\n" \
    "ds_read_b64_tr_b16 v[206:207], v214 offset:14584\n" \
    "v_mfma_f32_16x16x32_bf16 v[144:147], v[160:163], v[208:211], v[144:147]\n" \
    "v_mfma_f32_16x16x32_bf16 v[148:151], v[164:167], v[208:211], v[148:151]\n" \
    "s_waitcnt lgkmcnt(10)\n" \
    "v_mfma_f32_16x16x32_bf16 v[40:43], v[184:187], v[168:171], v[40:43]\n" \
    "ds_read_b64_tr_b16 v[208:209], v215 offset:13824\n" \
    "v_mfma_f32_16x16x32_bf16 v[44:47], v[184:187], v[172:175], v[44:47]\n" \
    "ds_read_b64_tr_b16 v[210:211], v215 offset:14256\n" \
    "v_mfma_f32_16x16x32_bf16 v[48:51], v[184:187], v[176:179], v[48:51]\n" \
    "v_mfma_f32_16x16x32_bf16 v[52:55], v[184:187], v[180:183], v[52:55]\n" \
    "s_waitcnt lgkmcnt(10)\n" \
    "v_mfma_f32_16x16x32_bf16 v[56:59], v[188:191], v[168:171], v[56:59]\n" \
    "v_mfma_f32_16x16x32_bf16 v[60:63], v[188:191], v[172:175], v[60:63]\n" \
    "v_mfma_f32_16x16x32_bf16 v[64:67], v[188:191], v[176:179], v[64:67]\n" \
    "v_mfma_f32_16x16x32_bf16 v[68:71], v[188:191], v[180:183], v[68:71]\n" \
    "s_waitcnt lgkmcnt(8)\n" \
    "MFK 72, 192, 168\n" \
    "MFK 76, 192, 172\n" \
    "MFK 80, 192, 176\n" \
    "MFK 84, 192, 180\n" \
    "s_waitcnt lgkmcnt(6)\n" \
    "MFK 88, 196, 168\n" \
    "MFK 92, 196, 172\n" \
    "MFK 96, 196, 176\n" \
    "MFK 100, 196, 180\n" \
    "s_waitcnt lgkmcnt(4)\n" \
    "v_mfma_f32_16x16x32_bf16 v[104:107], v[168:171], v[200:203], v[104:107]\n" \
    "v_mfma_f32_16x16x32_bf16 v[108:111], v[172:175], v[200:203], v[108:111]\n" \
    "v_mfma_f32_16x16x32_bf16 v[112:115], v[176:179], v[200:203], v[112:115]\n" \
    "v_mfma_f32_16x16x32_bf16 v[116:119], v[180:183], v[200:203], v[116:119]\n" \
    "s_waitcnt lgkmcnt(2)\n" \
    "v_mfma_f32_16x16x32_bf16 v[120:123], v[168:171], v[204:207], v[120:123]\n" \
    "v_mfma_f32_16x16x32_bf16 v[124:127], v[172:175], v[204:207], v[124:127]\n" \
    "v_mfma_f32_16x16x32_bf16 v[128:131], v[176:179], v[204:207], v[128:131]\n" \
    "v_mfma_f32_16x16x32_bf16 v[132:135], v[180:183], v[204:207], v[132:135]\n" \
    "s_waitcnt lgkmcnt(0)\n" \
    "v_mfma_f32_16x16x32_bf16 v[136:139], v[168:171], v[208:211], v[136:139]\n" \
    "v_mfma_f32_16x16x32_bf16 v[140:143], v[172:175], v[208:211], v[140:143]\n" \
    "v_mfma_f32_16x16x32_bf16 v[144:147], v[176:179], v[208:211], v[144:147]\n" \
    "v_mfma_f32_16x16x32_bf16 v[148:151], v[180:183], v[208:211], v[148:151]\n"

DI u32x4 fm_pack(f32x4 t0, f32x4 t1) {
    unsigned a0 = pk(t0[0], t0[1]), a1 = pk(t0[2], t0[3]), b0 = pk(t1[0], t1[1]), b1 = pk(t1[2], t1[3]);
    auto r0 = __builtin_amdgcn_permlane16_swap(a0, b0, false, false);
    auto r1 = __builtin_amdgcn_permlane16_swap(a1, b1, false, false);
    return (u32x4){r0[0], r1[0], r0[1], r1[1]};
}
DI u32x4 tm_pack(f32x4 t0, f32x4 t1) { return (u32x4){pk(t0[0], t0[1]), pk(t0[2], t0[3]), pk(t1[0], t1[1]), pk(t1[2], t1[3])}; }
DI f32x4 sub4(const f32x16& a, int r) { return (f32x4){a[4 * r], a[4 * r + 1], a[4 * r + 2], a[4 * r + 3]}; }

DI float nrm8(u32x4 v) {
    float s = __builtin_amdgcn_fdot2_f32_bf16(__builtin_bit_cast(bf16x2, v[0]), __builtin_bit_cast(bf16x2, v[0]), 0.f, false);
#pragma unroll
    for (int i = 1; i < 4; i++) s = __builtin_amdgcn_fdot2_f32_bf16(__builtin_bit_cast(bf16x2, v[i]), __builtin_bit_cast(bf16x2, v[i]), s, false);
    return s;
}
DI u32x4 pair_up(u32x4 vr, bool odd, unsigned selLo, unsigned selHi) {
    const unsigned a = odd ? vr[2] : vr[0], b = odd ? vr[3] : vr[1];
    const unsigned c = dppu<0xB1>(odd ? vr[0] : vr[2]), d = dppu<0xB1>(odd ? vr[1] : vr[3]);
    return (u32x4){__builtin_amdgcn_perm(a, c, selLo), __builtin_amdgcn_perm(a, c, selHi), __builtin_amdgcn_perm(b, d, selLo), __builtin_amdgcn_perm(b, d, selHi)};
}

DI void p1_loader(const char* X, const char* W, unsigned c0, unsigned c1, unsigned c2, int r, int rot, int lane, int lw) {
    unsigned xo, wo[7];
    { const int row = lw * 8 + (lane >> 3); xo = (unsigned)(r * 128 + row) * 1152u + (((lane & 7) ^ (row & 7)) << 4); }
#pragma unroll
    for (int i = 0; i < 4; i++) glds16(xo, X + rot * 128 + i * 32 * 1152, (lw + 4 * i) * 1024);
#pragma unroll
    for (int i = 0; i < 7; i++) {
        unsigned u = (lw + 4 * i) * 64u + lane; if (i == 6 && u >= 1728u) u = 0;
        const unsigned lr = (u * 2428u) >> 16, pc = u - lr * 27u;
        const unsigned k = (lr & ~7u) | ((lr & 1u) << 2) | ((lr >> 1) & 3u);
        wo[i] = k * 3456u + pc * 16u + (pc < 9 ? c0 : pc < 18 ? c1 : c2);
        if (i < 6 || lw < 3) glds16(wo[i], W + rot * 64 * 3456, 16384 + (lw + 4 * i) * 1024);
    }
    unsigned dst = ST1 + lw * 1024;
#pragma unroll 1
    for (int s = 1; s < 9; s++) {
        rot = rot == 8 ? 0 : rot + 1;
        const char* xb = X + rot * 128; const char* wb = W + rot * 64 * 3456;
#pragma unroll
        for (int i = 0; i < 4; i++) glds16(xo, xb + i * 32 * 1152, dst + i * 4096);
#pragma unroll
        for (int i = 0; i < 6; i++) glds16(wo[i], wb, dst + 16384 + i * 4096);
        if (lw < 3) glds16(wo[6], wb, dst + 16384 + 24576);
        dst = dst >= 2 * ST1 ? dst - 2 * ST1 : dst + ST1;
        WAITV_BAR(10);
    }
    WAITV_BAR(0);
}

template <int TY>
DI unsigned p1_consumer(char* ws, rsrc_t rs, int quad, int r, int lane, int w STAMP_ARGS) {
    constexpr bool ISB = TY == 2;
    const int rh = w & 1, pr = w >> 1, g = lane >> 4, m = lane & 15, q4 = m >> 2, p = m & 3;
    unsigned aW = 16384 + (g * 8 + q4 * 2) * 432 + pr * 72 + 8 * p;
    unsigned aR = 16384 + (g * 8 + q4 * 2) * 432 + pr * 72 + (p < 2 ? p : 2) * 144 + 32;
    unsigned aX0 = (64 * rh + m) * 128 + ((g ^ (m & 7)) << 4), aX1 = aX0 ^ 64;
    const int h0 = 4 * quad + 2 * pr, hA = 8 * (quad >> 1) + 2 * pr, T0 = 4 * r + 2 * rh;
    const int rho = ((m >> 2) << 3) + ((g & 1) << 2) + (m & 3);
    const unsigned fmoff = (g >> 1) * 512 + rho * 16, kvo = OFF_KV + (T0 >> 3) * CHUNK, u0 = T0 & 7, pofs = (8 * g + 4 * (m & 1)) * 4;
    const bool odd = lane & 1;
    unsigned selLo = odd ? 0x05040100u : 0x01000504u, selHi = odd ? 0x07060302u : 0x03020706u;
    unsigned oF, oV = 0, oR, oR1;
    if (ISB) {
        oF = OFF_Q1 + (32 + h0) * 65536 + T0 * 1024 + fmoff;
        oR = m < 4 ? OFF_Q2 + (32 + h0 + (m >> 1)) * 8192 + T0 * 128 + pofs : WS_USED; oR1 = oR + 128;
    } else {
        const int hr = hA + ((m >> 1) & 1) + (m & 4);
        oF = (TY == 0 ? OFF_Q1 + hA * 65536 + T0 * 1024 : kvo + hA * KV_HEAD + u0 * 1024) + fmoff;
        oV = kvo + h0 * KV_HEAD + 9216 + u0 * 1152 + g * 256 + ((m ^ ((g >> 1) << 3)) << 4);
        oR = (TY == 0 ? OFF_Q2 + hr * 8192 + T0 * 128 : kvo + hr * KV_HEAD + 8192 + u0 * 128) + pofs; oR1 = oR + 128;
        if (m >= 8) { oR = kvo + (h0 + ((m >> 1) & 1)) * KV_HEAD + 9216 + u0 * 1152 + 1024 + g * 32 + (m & 1) * 16; oR1 = oR + 1152; }
        if (m >= 12) { oR = WS_USED; oR1 = WS_USED; }
    }
    OPAQUE(oF); OPAQUE(oV); OPAQUE(oR); OPAQUE(oR1); OPAQUE(selLo); OPAQUE(selHi);
    f32x16 acc[7];
    unsigned cnt, ph, dl;
    asm volatile(
        ".macro MFK a, w, x\n"
        ".if %[isb]\n v_mfma_f32_16x16x32_bf16 v[\\a:\\a+3], v[\\x:\\x+3], v[\\w:\\w+3], v[\\a:\\a+3]\n"
        ".else\n v_mfma_f32_16x16x32_bf16 v[\\a:\\a+3], v[\\w:\\w+3], v[\\x:\\x+3], v[\\a:\\a+3]\n .endif\n .endm\n"
        ".set r_, 40\n .rept 56\n v_mov_b64 v[r_:r_+1], 0\n .set r_, r_+2\n .endr\n"
        "s_mov_b32 %[c], 8\n s_mov_b32 %[p], 0\n"
        "s_waitcnt lgkmcnt(0)\n s_barrier\n"
        P1_PRO
        "1:\n"
        P1_BODY
        "s_sub_u32 %[c], %[c], 1\n s_cmp_lg_u32 %[c], 0\n s_cbranch_scc1 1b\n"
        P1_TAIL
        "s_nop 7\n"
        ".purgem MFK\n"
        : "={v[40:55]}"(acc[0]), "={v[56:71]}"(acc[1]), "={v[72:87]}"(acc[2]), "={v[88:103]}"(acc[3]), "={v[104:119]}"(acc[4]),
          "={v[120:135]}"(acc[5]), "={v[136:151]}"(acc[6]), "+{v212}"(aX0), "+{v213}"(aX1), "+{v214}"(aW), "+{v215}"(aR),
          [c] "=&s"(cnt), [p] "=&s"(ph), [d] "=&s"(dl)
        : [neg] "s"(-2 * (int)ST1), [isb] "i"(ISB ? 1 : 0)
        : "memory", "scc", "v152", "v153", "v154", "v155", "v156", "v157", "v158", "v159", "v160", "v161", "v162", "v163", "v164", "v165",
          "v166", "v167", "v168", "v169", "v170", "v171", "v172", "v173", "v174", "v175", "v176", "v177", "v178", "v179", "v180", "v181",
          "v182", "v183", "v184", "v185", "v186", "v187", "v188", "v189", "v190", "v191", "v192", "v193", "v194", "v195", "v196", "v197",
          "v198", "v199", "v200", "v201", "v202", "v203", "v204", "v205", "v206", "v207", "v208", "v209", "v210", "v211");
    STAMP(11);
    if (!ISB) {
        constexpr unsigned HS = TY == 0 ? 65536 : KV_HEAD;
        const float rsc = TY == 0 && m < 8 ? QSCALE : 1.f;
        unsigned kn0 = 0, kn1 = 0, rm = 0;
#pragma unroll
        for (int tl = 0; tl < 2; tl++) {
            float nn[4];
#pragma unroll
            for (int jj = 0; jj < 4; jj++) {
                f32x4 t0 = sub4(acc[jj], 2 * tl), t1 = sub4(acc[jj], 2 * tl + 1);
                if (TY == 0) { t0 *= QSCALE; t1 *= QSCALE; }
                const u32x4 pc = fm_pack(t0, t1);
                st128s(rs, oF + tl * 1024, ((jj & 1) + 2 * (jj & 2)) * HS, pc);
                if (TY == 1) nn[jj] = nrm8(pc);
            }
#pragma unroll
            for (int jj = 0; jj < 2; jj++) st128s(rs, oV + tl * 1152, jj * KV_HEAD, tm_pack(sub4(acc[4 + jj], 2 * tl), sub4(acc[4 + jj], 2 * tl + 1)));
            f32x4 r0 = sub4(acc[6], 2 * tl), r1 = sub4(acc[6], 2 * tl + 1);
            if (TY == 0) { r0 *= rsc; r1 *= rsc; }
            const u32x4 vr = tm_pack(r0, r1), v = pair_up(vr, odd, selLo, selHi);
            st128(rs, tl ? oR1 : oR, m >= 8 ? vr : v);
            if (TY == 1) {
                kn0 = umax(kn0, __float_as_uint(swap32_add(nn[0], nn[1]))); kn1 = umax(kn1, __float_as_uint(swap32_add(nn[2], nn[3])));
#pragma unroll
                for (int i = 0; i < 4; i++)
                    rm = umax(rm, __float_as_uint(__builtin_amdgcn_fdot2_f32_bf16(__builtin_bit_cast(bf16x2, v[i]), __builtin_bit_cast(bf16x2, v[i]), 0.f, false)));
            }
        }
        if (TY == 0) return 0;
        kn0 = swap16_umax(row_umax(kn0)); kn1 = swap16_umax(row_umax(kn1));
        rm = swap32_umax(swap16_umax(umax(rm, dppu<0xB1>(rm))));
        unsigned kb = (__float_as_uint(__uint_as_float(m & 4 ? kn1 : kn0) + __uint_as_float(rm)) + 0x7ffffu) >> 19;
        kb = kb > 0xfffu ? 0xfffu : kb;
        const unsigned p0 = __builtin_amdgcn_readlane(kb, 0) | (__builtin_amdgcn_readlane(kb, 34) << 12);
        const unsigned p1 = __builtin_amdgcn_readlane(kb, 4) | (__builtin_amdgcn_readlane(kb, 38) << 12);
        return odd ? p1 : p0;
    } else {
        f32x4 M0 = {0, 0, 0, 0}, M1 = M0, M2 = M0, M3 = M0, M4 = M0, M5 = M0, M6 = M0;
#pragma unroll
        for (int tl = 0; tl < 2; tl++) {
            const f32x4 r0 = sub4(acc[6], 2 * tl), r1 = sub4(acc[6], 2 * tl + 1), p0 = phi4(r0), p1 = phi4(r1);
            const bf16x8 k0 = __builtin_bit_cast(bf16x8, tm_pack(phi4(sub4(acc[2], 2 * tl)), phi4(sub4(acc[2], 2 * tl + 1))));
            const bf16x8 k1 = __builtin_bit_cast(bf16x8, tm_pack(phi4(sub4(acc[3], 2 * tl)), phi4(sub4(acc[3], 2 * tl + 1))));
            const u32x4 kru = tm_pack(p0, p1);
            const bf16x8 kr = __builtin_bit_cast(bf16x8, kru);
            const bf16x8 v0 = __builtin_bit_cast(bf16x8, tm_pack(sub4(acc[4], 2 * tl), sub4(acc[4], 2 * tl + 1)));
            const bf16x8 v1 = __builtin_bit_cast(bf16x8, tm_pack(sub4(acc[5], 2 * tl), sub4(acc[5], 2 * tl + 1)));
            u32x4 vru = tm_pack(r0, r1);
            if (m >= 12) vru = (u32x4){ONES, ONES, ONES, ONES};
            const bf16x8 vr = __builtin_bit_cast(bf16x8, vru);
            M0 = mfma16(k0, v0, M0); M4 = mfma16(k1, v1, M4); M1 = mfma16(k0, vr, M1); M5 = mfma16(k1, vr, M5);
            M2 = mfma16(kr, v0, M2); M6 = mfma16(kr, v1, M6); M3 = mfma16(kr, vr, M3);
            st128(rs, tl ? oR1 : oR, pair_up(kru, odd, selLo, selHi));
        }
        const bool h1c = m == 10 || m == 11 || m == 13;
        f32x4 A2 = h1c ? M5 : M1, A3 = {M2[0], M2[1], M6[2], M6[3]};
        LAS f32x4* red = lds<f32x4>(L_RED1) + pr * 320 + lane;
        volatile LAS unsigned* hs = lds<unsigned>(L_FLAG + 4 + 4 * pr);
        if (rh) {
            red[0] = M0; red[64] = M4; red[128] = A2; red[192] = A3; red[256] = M3;
            asm volatile("s_waitcnt lgkmcnt(0)" ::: "memory");
            if (lane == 0) *hs = 1;
        }
#pragma unroll
        for (int tl = 0; tl < 2; tl++)
#pragma unroll
            for (int jj = 0; jj < 2; jj++) st128s(rs, oF + tl * 1024, jj * 65536, fm_pack(phi4(sub4(acc[jj], 2 * tl)), phi4(sub4(acc[jj], 2 * tl + 1))));
        if (!rh) {
            while (*hs == 0) __builtin_amdgcn_s_sleep(1);
            asm volatile("" ::: "memory");
            M0 += red[0]; M4 += red[64]; A2 += red[128]; A3 += red[192]; M3 += red[256];
            const unsigned kb = OFF_KVP + (h0 * 16 + r) * 1408, e = m >= 12 ? 18 : 16 + (m & 1);
            const bool ex = m >= 8 && m < 14;
            const unsigned kbx = kb + (h1c ? 16 * 1408 : 0);
            st128(rs, kb + m * 64 + g * 16, __builtin_bit_cast(u32x4, M0));
            st128(rs, kb + 16 * 1408 + m * 64 + g * 16, __builtin_bit_cast(u32x4, M4));
            st128(rs, ex ? kbx + e * 64 + g * 16 : WS_USED, __builtin_bit_cast(u32x4, A2));
            const u32x4 a3 = __builtin_bit_cast(u32x4, A3), m3 = __builtin_bit_cast(u32x4, M3);
            st64(rs, g == 1 ? kb + 1216 + m * 8 : WS_USED, (u32x2){a3[0], a3[1]});
            st64(rs, g == 1 ? kb + 16 * 1408 + 1216 + m * 8 : WS_USED, (u32x2){a3[2], a3[3]});
            st64(rs, g == 1 && ex ? kbx + 1216 + e * 8 : WS_USED, h1c ? (u32x2){m3[2], m3[3]} : (u32x2){m3[0], m3[1]});
        }
        return 0;
    }
}

#define ATT_MACROS \
    ".macro EXPS a, b\n .set r_, \\a\n .rept (\\b)-(\\a)\n v_exp_f32 v[r_], v[r_]\n .set r_, r_+1\n .endr\n .endm\n" \
    ".macro VRD dst, addr, off\n s_mov_b64 exec, %[vm]\n ds_read_b128 v[\\dst:\\dst+3], \\addr offset:\\off\n s_mov_b64 exec, -1\n .endm\n" \
    ".macro DEXP n, r\n"                                    \
    ".set pi_, (\\n)-3+5*(%[wm]-1)\n" \
    ".if (\\n) < 0 || (\\n) > 7 || ((\\n) < 3 && %[sn] < 0) || ((\\n) >= 3 && %[wm] == 0)\n v_exp_f32 v[\\r], v[\\r]\n" \
    ".elseif (\\n) == 0\n s_add_u32 m0, %[m2], %[sn]*18432\n v_exp_f32 v[\\r], v[\\r]\n global_load_lds_dwordx4 v190, %[sc] sc1\n" \
    ".elseif (\\n) == 1\n v_exp_f32 v[\\r], v[\\r]\n global_load_lds_dwordx4 v190, %[sc] offset:1024 sc1\n" \
    ".elseif (\\n) == 2\n s_add_u32 m0, %[m3], %[sn]*18432\n v_exp_f32 v[\\r], v[\\r]\n global_load_lds_dwordx4 v191, %[sc] sc1\n" \
    ".elseif pi_ < 8\n s_add_u32 s98, s96, 82944*pi_\n s_addc_u32 s99, s97, 0\n s_add_u32 m0, %[m1], 55296+8192*pi_+1024*(pi_&4)\n" \
    " v_exp_f32 v[\\r], v[\\r]\n global_load_lds_dwordx4 %[w0], s[98:99]\n" \
    ".elseif pi_ == 8\n s_mov_b32 m0, %[mr]\n v_exp_f32 v[\\r], v[\\r]\n global_load_lds_dwordx4 %[w1], s[96:97]\n" \
    ".else\n s_add_u32 m0, %[m1], 129024\n v_exp_f32 v[\\r], v[\\r]\n global_load_lds_dwordx4 %[w2], s[96:97]\n" \
    ".endif\n .endm\n" \
      \
    ".macro TILE C, N, o1, o0, kt, qk, prio, d\n" \
    ".if %[fair]\n s_setprio \\prio\n .endif\n" \
    "EXPS \\C, \\C+2\n" \
    "v_smfmac_f32_32x32x32_bf16 v[144:159], v[180:183], v[\\N+24:\\N+31], v184\n" \
    "VRD 180, v189, \\o1\n" \
    "EXPS \\C+2, \\C+5\n" \
    ".if \\qk\n s_waitcnt lgkmcnt(3)\n" \
    "v_mfma_f32_32x32x4_2b_bf16 v[\\N:\\N+31], v[174:175], v[168:169], 0 cbsz:1\n .endif\n" \
    ".if \\kt >= 0\n ds_read_b32 v174, v187 offset:%[sq]*18432+(\\kt)*128\n .endif\n" \
    "EXPS \\C+5, \\C+8\n DEXP \\d, \\C+8\n EXPS \\C+9, \\C+13\n" \
    ".if \\kt >= 0\n s_waitcnt lgkmcnt(2)\n .else\n s_waitcnt lgkmcnt(1)\n .endif\n" \
    "v_smfmac_f32_32x32x32_bf16 v[128:143], v[176:179], v[\\C:\\C+7], v184\n" \
    "EXPS \\C+13, \\C+18\n" \
    ".if \\kt >= 0\n s_waitcnt lgkmcnt(1)\n .else\n s_waitcnt lgkmcnt(0)\n .endif\n" \
    "v_smfmac_f32_32x32x32_bf16 v[128:143], v[180:183], v[\\C+8:\\C+15], v184\n" \
    "EXPS \\C+18, \\C+19\n DEXP \\d+1, \\C+19\n EXPS \\C+20, \\C+21\n" \
    ".if \\qk\n v_mfma_f32_32x32x16_bf16 v[\\N:\\N+15], v[170:173], v[160:163], v[\\N:\\N+15]\n .endif\n" \
    "EXPS \\C+21, \\C+25\n" \
    ".if \\qk\n v_mfma_f32_32x32x16_bf16 v[\\N+16:\\N+31], v[170:173], v[164:167], v[\\N+16:\\N+31]\n .endif\n" \
    ".if \\kt >= 0\n ds_read_b128 v[170:173], v186 offset:%[sq]*18432+(\\kt)*1024\n .endif\n" \
    "EXPS \\C+25, \\C+28\n" \
    "v_smfmac_f32_32x32x32_bf16 v[144:159], v[176:179], v[\\C+16:\\C+23], v184\n" \
    ".if \\o0 >= 0\n VRD 176, v188, \\o0\n .endif\n" \
    "EXPS \\C+28, \\C+30\n DEXP \\d+2, \\C+30\n EXPS \\C+31, \\C+32\n" \
    ".endm\n" \
    "s_mov_b32 s95, m0\n"
#define ATT_END "s_mov_b32 m0, s95\n .purgem EXPS\n .purgem VRD\n .purgem DEXP\n .purgem TILE\n"
typedef __attribute__((ext_vector_type(16))) unsigned u32x16;
typedef __attribute__((ext_vector_type(8))) unsigned u32x8;
struct Att {
    f32x32 S0, S1, O; u32x16 F; u32x8 q1; u32x2 q2; u32x4 ad; u32x2 vc;
    const char* kvsrc; unsigned m1, m2, m3, mr;
};
template <int J, int WM, int FAIR>
DI void att_block(Att& A, const char* Wo, unsigned w0, unsigned w1, unsigned w2) {
#define ATT_OPS : "+{v[64:95]}"(A.S0), "+{v[96:127]}"(A.S1), "+{v[128:159]}"(A.O), "+{v[170:185]}"(A.F) \
                : "{v[160:167]}"(A.q1), "{v[168:169]}"(A.q2), "{v[186:189]}"(A.ad), "{v[190:191]}"(A.vc), [vm] "s"(0x0003ffff0003ffffull), \
                  [sc] "s"(A.kvsrc + (J + 1) * CHUNK), [m1] "s"(A.m1), [m2] "s"(A.m2), [m3] "s"(A.m3), [mr] "s"(A.mr), "{s[96:97]}"(Wo), \
                  [w0] "v"(w0), [w1] "v"(w1), [w2] "v"(w2), [sp] "i"((J + 2) % 3), [sq] "i"(J % 3), [sn] "i"(J < 7 ? (J + 1) % 3 : -1), \
                  [wm] "i"(WM), [fair] "i"(FAIR) \
                : "memory", "scc", "s95", "s98", "s99"
    if constexpr (J == 0)
        asm volatile(ATT_MACROS
            "ds_read_b32 v174, v187\n ds_read_b128 v[170:173], v186\n"
            "s_waitcnt lgkmcnt(1)\n"
            "v_mfma_f32_32x32x4_2b_bf16 v[64:95], v[174:175], v[168:169], 0 cbsz:1\n"
            "ds_read_b32 v174, v187 offset:128\n"
            "s_waitcnt lgkmcnt(1)\n s_nop 15\n"
            "v_mfma_f32_32x32x16_bf16 v[64:79], v[170:173], v[160:163], v[64:79]\n"
            "v_mfma_f32_32x32x16_bf16 v[80:95], v[170:173], v[164:167], v[80:95]\n"
            "ds_read_b128 v[170:173], v186 offset:1024\n"
            "VRD 176, v188, 0\n"
            "s_nop 7\n"
            "TILE 64, 96, 0, 1152, 2, 1, 1, 0\n"
            "TILE 96, 64, 1152, 2304, 3, 1, 0, 9\n"
            ATT_END ATT_OPS);
    else if constexpr (J < 8)
        asm volatile(ATT_MACROS
            "TILE 64, 96, %[sp]*18432+2304, %[sp]*18432+3456, 0, 1, 2, 0\n"
            "TILE 96, 64, %[sp]*18432+3456, %[sq]*18432, 1, 1, 2, 3\n"
            "TILE 64, 96, %[sq]*18432, %[sq]*18432+1152, 2, 1, 1, 6\n"
            "TILE 96, 64, %[sq]*18432+1152, %[sq]*18432+2304, 3, 1, 0, 9\n"
            ATT_END ATT_OPS);
    else
        asm volatile(ATT_MACROS
            "TILE 64, 96, %[sp]*18432+2304, %[sp]*18432+3456, -1, 1, 0, 9\n"
            "TILE 96, 64, %[sp]*18432+3456, -1, -1, 0, 0, 9\n"
            "s_nop 1\n"
            "v_smfmac_f32_32x32x32_bf16 v[144:159], v[180:183], v[120:127], v184\n"
            "s_nop 15\n"
            ATT_END ATT_OPS);
}

DI void store_o(rsrc_t rs, int br, const f32x16& o, float eps, int h, int qt, int lane) {
    const int hh = lane >> 5, rho = lane & 31;
    auto dn = __builtin_amdgcn_permlane32_swap(__float_as_uint(o[10]), __float_as_uint(o[10]), false, false);
    const float sc = __builtin_amdgcn_rcpf(__uint_as_float(dn[0]) + eps);
    const int tok = (((rho >> 2) & 1) << 4) + ((rho >> 3) << 2) + (rho & 3);
    const unsigned base = OFF_O + (br * 128 + 2 * qt + (tok >> 4)) * 16384 + (tok & 15) * 16;
    const u32x4 v = {pk(o[0] * sc, o[1] * sc), pk(o[2] * sc, o[3] * sc), pk(o[4] * sc, o[5] * sc), pk(o[6] * sc, o[7] * sc)};
    st128(rs, base + (h >> 1) * 1024 + (2 * (h & 1) + hh) * 256, v);
    st32(rs, hh ? WS_USED : OFF_OR + ((br * 64 + qt) * 32 + h) * 128 + tok * 4, pk(o[8] * sc, o[9] * sc));
}

struct W3Src { unsigned om, orm, ob; };
DI W3Src w3src(int cb, int lane, int w) {
    const unsigned j = lane >> 3, colb = cb * 144 + ((((lane >> 1) & 3) ^ (((lane >> 4) & 1) + 2 * (w & 1))) << 5) + ((lane & 1) << 4);
    const unsigned hq = lane >> 4, jj = lane & 7;
    W3Src r;
    r.om = ((j & 3) + 8 * (j >> 2) + 4 * (w & 1) + 18 * (w >> 1) + (w & 2)) * 1152 + colb;
    r.orm = (72 * w + 18 * (j >> 1) + ((j & 2) ? 0 : 16) + (j & 1)) * 1152 + colb;
    r.ob = (72 * w + 18 * hq + 2 * (hq & 1) + (jj & 3) + 8 * (jj >> 2) + 4 * ((lane >> 3) & 1)) * 1152 + cb * 144 + 128;
    return r;
}
DI void w3_last_piece(const char* Wo, int cb, int lane) {
    const unsigned hr = lane >> 1;
    glds16((18 * hr + ((hr & 1) ? 0 : 16) + (lane & 1)) * 1152 + cb * 144 + 128, Wo, L_W3B + 8192);
}
DI void load_chunk(const char* kvsrc, int c, int slot, int lane, int lw) {
#pragma unroll
    for (int i = 0; i < 4; i++) glds16_dev(lane * 16, kvsrc + c * CHUNK + (lw + 4 * i) * 1024, slot * CHUNK + (lw + 4 * i) * 1024);
    if (lw < 2) glds16_dev(lane * 16, kvsrc + c * CHUNK + 16384 + lw * 1024, slot * CHUNK + 16384 + lw * 1024);
}
struct W3Addr { unsigned a[4], b, b8; };
DI bf16x8 w3frag(const W3Addr& A, int i, int t) {
    return t < 4 ? trfrag(A.a[t] + i * 4096, A.a[t] + i * 4096 + 512) : i < 8 ? trfrag(A.b + i * 512, A.b + i * 512 + 64) : trfrag(A.b8, A.b8 + 64);
}
DI W3Addr w3addr(int kh, int lane) {
    const int g = lane >> 4, m = lane & 15;
    W3Addr A;
    const unsigned base = L_W3 + kh * 9 * 4096 + g * 1024 + (m >> 2) * 128 + 8 * (m & 3), key = ((m >> 3) + 2 * (g & 1)) << 5;
#pragma unroll
    for (int t = 0; t < 4; t++) { A.a[t] = base + ((t * 32) ^ key); OPAQUE(A.a[t]); }
    A.b = L_W3B + kh * 4096 + (8 * g + (m >> 2)) * 16 + 8 * (m & 1); OPAQUE(A.b);
    A.b8 = A.b + 8192 - kh * 3584;
    return A;
}
DI void load_af(u32x4 (&af)[9], rsrc_t rs, int br, int rtile, int kh, int lane) {
    const unsigned a = OFF_O + (br * 128 + rtile) * 16384 + kh * 8192 + lane * 16;
    const unsigned o = OFF_OR + ((br * 64 + (rtile >> 1)) * 32 + 16 * kh + 4 * (lane >> 4)) * 128 + (16 * (rtile & 1) + (lane & 15)) * 4;
#pragma unroll
    for (int i = 0; i < 8; i++) af[i] = ld128(rs, a + i * 1024);
#pragma unroll
    for (int j = 0; j < 4; j++) af[8][j] = ld32(rs, o + j * 128);
}
DI void gemm3(f32x4 (&acc)[5], const u32x4 (&af)[9], int kh, int lane) {
    __builtin_amdgcn_s_setprio(3);
    const W3Addr A = w3addr(kh, lane);
    bf16x8 wf[2][5];
#pragma unroll
    for (int t = 0; t < 5; t++) { wf[0][t] = w3frag(A, 0, t); acc[t] = (f32x4){0, 0, 0, 0}; }
    __builtin_amdgcn_sched_barrier(0);
#pragma unroll
    for (int i = 0; i < 9; i++) {
        if (i < 8) {
#pragma unroll
            for (int t = 0; t < 5; t++) wf[(i + 1) & 1][t] = w3frag(A, i + 1, t);
        }
#pragma unroll
        for (int t = 0; t < 5; t++) acc[t] = mfma16(wf[i & 1][t], __builtin_bit_cast(bf16x8, af[i]), acc[t]);
#pragma unroll
        for (int t = 0; t < 5; t++) { __builtin_amdgcn_sched_group_barrier(0x100, 2, 0); __builtin_amdgcn_sched_group_barrier(0x8, 1, 0); }
        __builtin_amdgcn_sched_barrier(0);
    }
    __builtin_amdgcn_s_setprio(0);
}

__global__ void __launch_bounds__(512, 2) fused(const char* X, const char* WqA, const char* WqB, const char* WoA, const char* WoB,
                                                 const unsigned* gw, char* ws, char* out) {
    const int tid = threadIdx.x, lane = tid & 63, w = __builtin_amdgcn_readfirstlane(tid >> 6);
    const int b = blockIdx.x, xcd = b & 7, idx = b >> 3;
    const rsrc_t rs = __builtin_amdgcn_make_buffer_rsrc(ws, 0, WS_USED, 0x00020000);
    STAMP_DECL;
    STAMP(0);
    u64 pc0; asm volatile("s_getpc_b64 %0" : "=s"(pc0));
    const u64 epoch = __hip_atomic_load((gu64*)(size_t)(ws + OFF_EPOCH), RLX_AGENT);
    const int h = xcd * 4 + (idx & 3), qb = idx >> 2;
    const int rb = xcd * 4 + (idx >> 3), cb = idx & 7;
    const int qg = w & 3, kh = w >> 2, hh = lane >> 5, rho = lane & 31, g = lane >> 4, m = lane & 15;
    const bool young = kh;
    const char* kvsrc = ws + OFF_KV + (size_t)h * KV_HEAD;
    const int qt0 = 8 * qb + 2 * qg, qtf = qt0 + kh;
    const int br = idx & 1, quad = (xcd & 3) * 2 + ((idx >> 1) & 1), r = (xcd >> 2) * 8 + (idx >> 2);
    u64 tag;
    Att A;
    if (young) {
        const unsigned c2 = 2016 + quad * 144, c0 = br ? quad * 144 : (quad & 1) * 1152 + (quad >> 1) * 288, c1 = br ? c0 + 1008 : c0;
        const int rot = ((idx >> 2) + 2 * (idx & 3)) % 9;
        p1_loader(X, br ? WqB : WqA, c0, c1, c2, r, rot, lane, qg);
        STAMP(10);
        tag = make_tag(epoch);
        {
            const int row = 256 * qb + 8 * h + 2 * qg + hh;
            const unsigned* xr = (const unsigned*)(X + (size_t)row * 1152) + rho;
            float s = 0.f;
#pragma unroll
            for (int i = 0; i < 9; i++) s = __builtin_amdgcn_fdot2_f32_bf16(__builtin_bit_cast(bf16x2, xr[32 * i]), __builtin_bit_cast(bf16x2, gw[rho + 32 * i]), s, false);
            s = row_sum(s); s = swap16_add(s, s);
            if (rho == 0) st32(rs, OFF_GATE + row * 4, __float_as_uint(__builtin_amdgcn_rcpf(1.f + __expf(-s))));
        }
        STAMP(13);
        volatile LAS unsigned* go = lds<unsigned>(L_FLAG + 12);
        unsigned kmax;
        if (qg == 0) {
            const u64 f = wait_flags<63, 4>(ws + OFF_FA + (h >> 1) * 512, tag, ~tag, lane);
            kmax = swap32_umax(swap16_umax(row_umax((((unsigned)f >> (12 * (h & 1))) & 0xfffu) << 19))) | 1u;
            *go = kmax;
        } else {
            while ((kmax = *go) == 0) __builtin_amdgcn_s_sleep(1);
            asm volatile("" ::: "memory");
        }
        STAMP(14);
        const u32x4 q1a = ld128(rs, OFF_Q1 + h * 65536 + qt0 * 1024 + lane * 16), q1b = ld128(rs, OFF_Q1 + h * 65536 + qt0 * 1024 + 1024 + lane * 16);
        const unsigned q2r = ld32(rs, OFF_Q2 + h * 8192 + qt0 * 128 + lane * 4);
        load_chunk(kvsrc, 0, 0, lane, qg);
        const float n = __builtin_amdgcn_fdot2_f32_bf16(__builtin_bit_cast(bf16x2, q2r), __builtin_bit_cast(bf16x2, q2r), swap32_add(nrm8(q1a), nrm8(q1b)), false);
        const float mneg = -1.01f * __builtin_amdgcn_sqrtf(n * __uint_as_float(kmax));
        A.q2 = (u32x2){q2r, pk(mneg, 0.f)};
        A.q1 = __builtin_shufflevector(q1a, q1b, 0, 1, 2, 3, 4, 5, 6, 7);
        lds<u32x4>(L_EX)[qg * 128 + lane] = q1a; lds<u32x4>(L_EX)[qg * 128 + 64 + lane] = q1b; lds<u32x2>(L_EX + 8192)[qg * 64 + lane] = A.q2;
    } else {
        unsigned pf = 0;
        asm volatile("global_load_dword %0, %1, %2" : "+v"(pf) : "v"((unsigned)(((idx & 3) * 70 + (tid < 70 ? tid : 0)) * 128)), "s"(pc0) : "memory");
        if (tid < 4) lds<unsigned>(L_FLAG)[tid] = 0;
        const unsigned pay = br ? p1_consumer<2>(ws, rs, quad, r, lane, w STAMP_PASS)
                                : (quad & 1) ? p1_consumer<1>(ws, rs, quad, r, lane, w STAMP_PASS) : p1_consumer<0>(ws, rs, quad, r, lane, w STAMP_PASS);
        STAMP(12);
        tag = make_tag(epoch);
        const unsigned fo = br ? OFF_F1 + ((quad * 2 + (w >> 1)) * 32 + 2 * r + (w & 1)) * 8
                               : OFF_FA + ((4 * (quad >> 1) + (w >> 1) + 2 * lane) * 64 + (quad & 1) * 32 + 2 * r + (w & 1)) * 8;
        WAITV();
        asm volatile("" :: "v"(pf));
        if (lane < 2 - br) __hip_atomic_store((gu64*)(size_t)(ws + fo), tag | pay, RLX_AGENT);
        STAMP(13);
    }
    {
        const unsigned vinit = rho == 18 ? ONES : 0u;
        asm volatile(".set r_, 120\n .rept 20\n v_mov_b64 v[r_:r_+1], 0\n .set r_, r_+2\n .endr\n"
                     ".set r_, 176\n .rept 8\n v_mov_b32 v[r_], %4\n .set r_, r_+1\n .endr\n"
                     "v_mov_b32 v175, 0x3f803f80\n v_mov_b32 v184, 0xdddddddd\n"
                     : "={v[64:95]}"(A.S0), "={v[96:127]}"(A.S1), "={v[128:159]}"(A.O), "=&{v[170:185]}"(A.F) : "{v192}"(vinit));
    }
    const unsigned bK = kh * 4096 + lane * 16, bK2 = 8192 + kh * 512 + rho * 4, bV = 9216 + kh * 4608 + (rho < 16 ? hh * 256 + rho * 16 : 768 + hh * 32 + rho * 16);
    const unsigned dV = rho < 8 ? 640 : rho < 16 ? 384 : 64;
    A.ad = (u32x4){bK, bK2, bV, bV + dV};
    A.kvsrc = kvsrc; A.m1 = w * 1024; A.m2 = w * 2048; A.m3 = 16384 + (w & 1) * 1024; A.mr = L_W3 + (32 + w + 8 * (w & 4)) * 1024;
    A.vc = (u32x2){lane * 16 + A.m2, lane * 16 + A.m3};
    asm volatile("" : "+{v[186:189]}"(A.ad), "+{v[190:191]}"(A.vc));
    W3Src ws3 = w3src(cb, lane, w);
    OPAQUE(ws3.om); OPAQUE(ws3.orm); OPAQUE(ws3.ob);
    STAMP(16);
    WAITV_BAR(0);
    STAMP(17);
    u64 seen;
    if (!young) {
        seen = peek_flags<31>(ws + OFF_F1 + (h >> 1) * 256, lane);
        const u32x4 q1a = lds<u32x4>(L_EX)[qg * 128 + lane], q1b = lds<u32x4>(L_EX)[qg * 128 + 64 + lane];
        A.q1 = __builtin_shufflevector(q1a, q1b, 0, 1, 2, 3, 4, 5, 6, 7); A.q2 = lds<u32x2>(L_EX + 8192)[qg * 64 + lane];
    }
    att_block<0, 0, 0>(A, WoB, 0, 0, 0);
    WAITV();
    u32x4 kvp[8];
    if (!young) {
        wait_flags<31, 4>(ws + OFF_F1 + (h >> 1) * 256, tag, seen, lane);
        const unsigned o = rho < 22 ? OFF_KVP + (h * 16 + hh) * 1408 + (qg * 22 + rho) * 16 : WS_USED;
#pragma unroll
        for (int i = 0; i < 8; i++) kvp[i] = ld128(rs, o + i * 2816);
        if (w == 0) w3_last_piece(WoB, cb, lane);
    }
    BAR();
    STAMP(18);
    if (young) {
        kvp[0] = ld128(rs, OFF_Q1 + (32 + h) * 65536 + qt0 * 1024 + lane * 16);
        kvp[1] = ld128(rs, OFF_Q1 + (32 + h) * 65536 + qt0 * 1024 + 1024 + lane * 16);
        kvp[2][0] = ld32(rs, OFF_Q2 + (32 + h) * 8192 + qt0 * 128 + lane * 4);
    }
    att_block<1, 1, 0>(A, WoB, ws3.om, 0, 0);
    WAITV();
    if (!young) {
        f32x4 s4 = __builtin_bit_cast(f32x4, kvp[0]);
#pragma unroll
        for (int i = 1; i < 8; i++) s4 += __builtin_bit_cast(f32x4, kvp[i]);
        const float lo = swap32_add(s4[0], s4[2]), hi = swap32_add(s4[1], s4[3]);
        const int u = qg * 22 + rho;
        if (rho < 22) *lds<unsigned>(L_KVT + u * 8 + hh * 4 + (u < 76 ? 0 : 32)) = pk(lo, hi);
        asm volatile("s_waitcnt lgkmcnt(0)" ::: "memory");
    }
    BAR();
    STAMP(19);
    if (young) {
        __builtin_amdgcn_s_setprio(3);
        const int e = rho < 19 ? rho : 18;
        const bf16x8 a1 = ldsr128(L_KVT + e * 32 + hh * 16);
        const u32x2 a2 = {*lds<unsigned>(L_KVT + 640 + e * 4), 0u};
        const u32x2 b2 = {kvp[2][0], 0u};
        f32x32 o = {};
        o = __builtin_amdgcn_mfma_f32_32x32x4bf16_1k(__builtin_bit_cast(s16x4, a2), __builtin_bit_cast(s16x4, b2), o, 1, 0, 0);
        f32x16 oa = __builtin_shufflevector(o, o, 0, 1, 2, 3, 4, 5, 6, 7, 8, 9, 10, 11, 12, 13, 14, 15);
        f32x16 ob = __builtin_shufflevector(o, o, 16, 17, 18, 19, 20, 21, 22, 23, 24, 25, 26, 27, 28, 29, 30, 31);
        oa = __builtin_amdgcn_mfma_f32_32x32x16_bf16(a1, __builtin_bit_cast(bf16x8, kvp[0]), oa, 0, 0, 0);
        ob = __builtin_amdgcn_mfma_f32_32x32x16_bf16(a1, __builtin_bit_cast(bf16x8, kvp[1]), ob, 0, 0, 0);
        store_o(rs, 1, oa, 1e-6f, h, qt0, lane);
        store_o(rs, 1, ob, 1e-6f, h, qt0 + 1, lane);
        __builtin_amdgcn_s_setprio(0);
    }
    att_block<2, 2, 1>(A, WoB, ws3.om, ws3.orm, ws3.ob);
    WAITV();
    if (young && lane < 2) __hip_atomic_store((gu64*)(size_t)(ws + OFF_FB) + (qt0 + lane) * 32 + h, tag, RLX_AGENT);
    BAR();
    STAMP(20);
    const int rt = w & 3, qt3 = 2 * rb + (rt >> 1);
    att_block<3, 0, 0>(A, WoB, 0, 0, 0);
    WAITV();
    u32x4 af[9];
    float gate;
    if (!young) {
        wait_flags<31, 4>(ws + OFF_FB + qt3 * 256, tag, ~tag, lane);
        load_af(af, rs, 1, rb * 4 + rt, kh, lane);
        gate = __uint_as_float(ld32(rs, OFF_GATE + (rb * 64 + rt * 16 + m) * 4));
        if (b == 0 && tid == 0) __hip_atomic_store((gu64*)(size_t)(ws + OFF_EPOCH), epoch + 1, RLX_AGENT);
    }
    BAR();
    STAMP(21);
    if (young) {
        load_af(af, rs, 1, rb * 4 + rt, kh, lane);
        gate = __uint_as_float(ld32(rs, OFF_GATE + (rb * 64 + rt * 16 + m) * 4));
    }
    att_block<4, 0, 0>(A, WoB, 0, 0, 0);
    WAITV();
    f32x4 accb[5];
    if (!young) gemm3(accb, af, kh, lane);
    BAR();
    STAMP(22);
    if (young) gemm3(accb, af, kh, lane);
    att_block<5, 0, 1>(A, WoB, 0, 0, 0);
    WAITV();
    BAR();
    STAMP(23);
    ws3 = w3src(cb, lane, w);
    if (w == 4) w3_last_piece(WoA, cb, lane);
    att_block<6, 1, 1>(A, WoA, ws3.om, 0, 0);
    asm volatile("s_waitcnt vmcnt(5)\n\ts_barrier" ::: "memory");
    STAMP(24);
    att_block<7, 2, 1>(A, WoA, ws3.om, ws3.orm, ws3.ob);
    att_block<8, 0, 0>(A, WoA, 0, 0, 0);
    STAMP(27);
    f32x16 Oa = __builtin_shufflevector(A.O, A.O, 0, 1, 2, 3, 4, 5, 6, 7, 8, 9, 10, 11, 12, 13, 14, 15);
    f32x16 Ob = __builtin_shufflevector(A.O, A.O, 16, 17, 18, 19, 20, 21, 22, 23, 24, 25, 26, 27, 28, 29, 30, 31);
    LAS f32x4* ex = lds<f32x4>(L_EX);
    {
        const float thr = 9.094947e-13f;
        const bool bad = hh == 0 && !(Oa[10] >= thr && Oa[10] < 1e30f && Ob[10] >= thr && Ob[10] < 1e30f);
        if (__any(bad) && lane == 0) *(volatile LAS unsigned*)lds<unsigned>(L_FLAG) = 1;
#pragma unroll
        for (int i = 0; i < 3; i++) ex[(w * 3 + i) * 64 + lane] = kh ? sub4(Oa, i) : sub4(Ob, i);
        WAITV_BAR(0);
        if (*(volatile LAS unsigned*)lds<unsigned>(L_FLAG)) {
            const u32x2 b2 = {A.q2[0], 0u};
            const u32x4 q1a = __builtin_shufflevector(A.q1, A.q1, 0, 1, 2, 3), q1b = __builtin_shufflevector(A.q1, A.q1, 4, 5, 6, 7);
            float xa = -INFINITY, xb = -INFINITY;
            for (int pass = 0; pass < 2; pass++) {
                for (int c = 0; c < 8; c++) {
                    LBAR();
                    if (young) load_chunk(kvsrc, c, 0, lane, qg);
                    WAITV_BAR(0);
                    for (int t = 0; t < 4; t++) {
                        const bf16x8 kf = ldsr128(bK + t * 1024);
                        const u32x2 k2 = {*lds<unsigned>(bK2 + t * 128), 0u};
                        f32x32 s = {};
                        s = __builtin_amdgcn_mfma_f32_32x32x4bf16_1k(__builtin_bit_cast(s16x4, k2), __builtin_bit_cast(s16x4, b2), s, 1, 0, 0);
                        f32x16 sa = __builtin_shufflevector(s, s, 0, 1, 2, 3, 4, 5, 6, 7, 8, 9, 10, 11, 12, 13, 14, 15);
                        f32x16 sb = __builtin_shufflevector(s, s, 16, 17, 18, 19, 20, 21, 22, 23, 24, 25, 26, 27, 28, 29, 30, 31);
                        sa = __builtin_amdgcn_mfma_f32_32x32x16_bf16(kf, __builtin_bit_cast(bf16x8, q1a), sa, 0, 0, 0);
                        sb = __builtin_amdgcn_mfma_f32_32x32x16_bf16(kf, __builtin_bit_cast(bf16x8, q1b), sb, 0, 0, 0);
                        if (pass == 0) {
#pragma unroll
                            for (int i = 0; i < 16; i++) { xa = fmaxf(xa, sa[i]); xb = fmaxf(xb, sb[i]); }
                        } else {
#pragma unroll
                            for (int i = 0; i < 16; i++) { sa[i] = __builtin_amdgcn_exp2f(sa[i] - xa); sb[i] = __builtin_amdgcn_exp2f(sb[i] - xb); }
#pragma unroll
                            for (int st = 0; st < 2; st++) {
                                u32x4 vf = {ONES, ONES, ONES, ONES};
                                if (rho < 18) vf = __builtin_bit_cast(u32x4, ldsr128(bV + t * 1152 + st * dV));
                                f32x8 pa, pb;
#pragma unroll
                                for (int i = 0; i < 8; i++) { pa[i] = sa[8 * st + i]; pb[i] = sb[8 * st + i]; }
                                Oa = __builtin_amdgcn_smfmac_f32_32x32x32_bf16(__builtin_bit_cast(bf16x8, vf), __builtin_bit_cast(bf16x16, pa), Oa, (int)0xdddddddd, 0, 0);
                                Ob = __builtin_amdgcn_smfmac_f32_32x32x32_bf16(__builtin_bit_cast(bf16x8, vf), __builtin_bit_cast(bf16x16, pb), Ob, (int)0xdddddddd, 0, 0);
                            }
                        }
                    }
                }
                if (pass == 0) {
                    xa = swap32_max(xa); xb = swap32_max(xb);
                    LAS float* mx = lds<float>(L_EX);
                    mx[w * 64 + lane] = hh ? xb : xa;
                    LBAR();
                    xa = fmaxf(xa, mx[(w ^ 4) * 64 + rho]); xb = fmaxf(xb, mx[(w ^ 4) * 64 + 32 + rho]);
#pragma unroll
                    for (int i = 0; i < 16; i++) { Oa[i] = 0.f; Ob[i] = 0.f; }
                }
            }
#pragma unroll
            for (int i = 0; i < 3; i++) ex[(w * 3 + i) * 64 + lane] = kh ? sub4(Oa, i) : sub4(Ob, i);
            LBAR();
        }
    }
    {
        f32x16 mine = kh ? Ob : Oa;
#pragma unroll
        for (int i = 0; i < 3; i++) {
            const f32x4 v = ex[((w ^ 4) * 3 + i) * 64 + lane];
#pragma unroll
            for (int k = 0; k < 4; k++) mine[4 * i + k] += v[k];
        }
        store_o(rs, 0, mine, 0.f, h, qtf, lane);
    }
    STAMP(28);
    const W3Addr WA = w3addr(kh, lane);
    bf16x8 wf[7][5];
#pragma unroll
    for (int i = 0; i < 3; i++)
#pragma unroll
        for (int t = 0; t < 5; t++) wf[i][t] = w3frag(WA, i, t);
    post_flag(ws + OFF_F2 + (qtf * 32 + h) * 8, tag, lane);
    STAMP(29);
#pragma unroll
    for (int i = 3; i < 7; i++)
#pragma unroll
        for (int t = 0; t < 5; t++) wf[i][t] = w3frag(WA, i, t);
    wait_flags<15, 4>(ws + OFF_F2 + qt3 * 256 + kh * 128, tag, ~tag, lane);
    STAMP(30);
    load_af(af, rs, 0, rb * 4 + rt, kh, lane);
    __builtin_amdgcn_sched_barrier(0);
    f32x4 acc[5];
#pragma unroll
    for (int i = 0; i < 9; i++) {
#pragma unroll
        for (int t = 0; t < 5; t++) acc[t] = mfma16(wf[i % 7][t], __builtin_bit_cast(bf16x8, af[i]), i ? acc[t] : (f32x4){0, 0, 0, 0});
        if (i < 2) {
#pragma unroll
            for (int t = 0; t < 5; t++) wf[i][t] = w3frag(WA, 7 + i, t);
        }
        __builtin_amdgcn_sched_barrier(0);
    }
    STAMP(31);
    LAS f32x4* red = lds<f32x4>(0);
#pragma unroll
    for (int t = 0; t < 5; t++) acc[t] = acc[t] * gate + accb[t] * (1.f - gate);
#pragma unroll
    for (int t = 0; t < 5; t++) if ((t < 3) == (kh == 1)) red[(rt * 5 + t) * 64 + lane] = acc[t];
    LBAR();
    char* orow = out + (size_t)(rb * 64 + rt * 16 + m) * 1152 + cb * 144 + g * 8;
#pragma unroll
    for (int t = 0; t < 5; t++)
        if ((t < 3) == (kh == 0)) {
            const f32x4 v = acc[t] + red[(rt * 5 + t) * 64 + lane];
            if (t < 4 || g < 2) *(u32x2*)(orow + t * 32) = (u32x2){pk(v[0], v[1]), pk(v[2], v[3])};
        }
    STAMP(32);
    STAMP_OUT;
}

extern "C" void kernel_launch(void* const* d_in, const int* in_sizes, int n_in, void* d_out, int out_size, void* d_ws, size_t ws_size,
                              hipStream_t stream) {
    (void)hipFuncSetAttribute((const void*)fused, hipFuncAttributeMaxDynamicSharedMemorySize, LDS_BYTES);
    fused<<<256, 512, LDS_BYTES, stream>>>((const char*)d_in[0], (const char*)d_in[1], (const char*)d_in[4], (const char*)d_in[2],
                                          (const char*)d_in[5], (const unsigned*)d_in[6], (char*)d_ws, (char*)d_out);
}
```

```cpp
#include <hip/hip_runtime.h>
#include <stdint.h>

typedef __attribute__((ext_vector_type(8))) __bf16 bf16x8;
typedef __attribute__((ext_vector_type(16))) __bf16 bf16x16;
typedef __attribute__((ext_vector_type(2))) __bf16 bf16x2;
typedef __attribute__((ext_vector_type(4))) short s16x4;
typedef __attribute__((ext_vector_type(2))) float f32x2;
typedef __attribute__((ext_vector_type(4))) float f32x4;
typedef __attribute__((ext_vector_type(8))) float f32x8;
typedef __attribute__((ext_vector_type(16))) float f32x16;
typedef __attribute__((ext_vector_type(32))) float f32x32;
typedef __attribute__((ext_vector_type(4))) unsigned u32x4;
typedef __attribute__((ext_vector_type(2))) unsigned u32x2;
typedef unsigned long long u64;
typedef __attribute__((address_space(1))) u64 gu64;
#define LAS __attribute__((address_space(3)))
#define DI __device__ __forceinline__
#define RLX_AGENT __ATOMIC_RELAXED, __HIP_MEMORY_SCOPE_AGENT

constexpr unsigned CHUNK = 18432;
constexpr unsigned KV_HEAD = 8 * CHUNK;
constexpr unsigned OFF_EPOCH = 0;
constexpr unsigned OFF_FA = 256;
constexpr unsigned OFF_F1 = OFF_FA + 8192;
constexpr unsigned OFF_FB = OFF_F1 + 4096;
constexpr unsigned OFF_F2 = OFF_FB + 16384;
constexpr unsigned OFF_GATE = OFF_F2 + 16384;
constexpr unsigned OFF_KVP = OFF_GATE + 8192;
constexpr unsigned OFF_Q2 = OFF_KVP + 32 * 16 * 1408;
constexpr unsigned OFF_Q1 = OFF_Q2 + 2 * 32 * 8192;
constexpr unsigned OFF_KV = OFF_Q1 + 2 * 32 * 65536;
constexpr unsigned OFF_O = OFF_KV + 32 * KV_HEAD;
constexpr unsigned OFF_OR = OFF_O + 2 * 128 * 16384;
constexpr unsigned WS_USED = OFF_OR + 2 * 64 * 4096;

constexpr unsigned ST1 = 44032;
constexpr unsigned L_RED1 = 57344;
constexpr unsigned L_W3 = 3 * CHUNK;
constexpr unsigned L_W3B = L_W3 + 73728;
constexpr unsigned L_EX = L_W3 + 82944;
constexpr unsigned L_KVT = L_EX + 24576;
constexpr unsigned L_FLAG = L_KVT + 768;
constexpr unsigned LDS_BYTES = 163840;

constexpr float QSCALE = 0.23570226039551584f * 1.4426950408889634f;
constexpr unsigned ONES = 0x3f803f80u;

#define STAMP(i)
#define STAMP_DECL
#define STAMP_ARGS
#define STAMP_PASS
#define STAMP_OUT

DI unsigned pk(float a, float b) { bf16x2 v = __builtin_convertvector((f32x2){a, b}, bf16x2); return __builtin_bit_cast(unsigned, v); }
DI float bflo(unsigned u) { return __uint_as_float(u << 16); }
DI float bfhi(unsigned u) { return __uint_as_float(u & 0xffff0000u); }
DI f32x4 phi4(f32x4 a) {
    const f32x4 s = a * 1.4426950408889634f;
    f32x4 e, x;
#pragma unroll
    for (int i = 0; i < 4; i++) {
        asm("v_exp_f32_e64 %0, %1 clamp" : "=v"(e[i]) : "v"(s[i]));
        asm("v_max_f32 %0, 0, %1" : "=v"(x[i]) : "v"(a[i]));
    }
    return (x + 1e-6f) + e;
}
DI void glds16(unsigned voff, const void* sbase, unsigned lds_dst) {
    unsigned keep;
    asm volatile("s_mov_b32 %0, m0\n\ts_mov_b32 m0, %3\n\ts_nop 0\n\tglobal_load_lds_dwordx4 %1, %2\n\ts_mov_b32 m0, %0"
                 : "=&s"(keep) : "v"(voff), "s"(sbase), "s"(lds_dst) : "memory");
}
DI void glds16_dev(unsigned voff, const void* sbase, unsigned lds_dst) {
    unsigned keep;
    asm volatile("s_mov_b32 %0, m0\n\ts_mov_b32 m0, %3\n\ts_nop 0\n\tglobal_load_lds_dwordx4 %1, %2 sc1\n\ts_mov_b32 m0, %0"
                 : "=&s"(keep) : "v"(voff), "s"(sbase), "s"(lds_dst) : "memory");
}
#define WAITV_BAR(n) asm volatile("s_waitcnt vmcnt(" #n ") lgkmcnt(0)\n\ts_barrier" ::: "memory")
#define LBAR() asm volatile("s_waitcnt lgkmcnt(0)\n\ts_barrier" ::: "memory")
#define BAR() asm volatile("s_barrier" ::: "memory")
#define WAITV() do { asm volatile("s_waitcnt vmcnt(0)" ::: "memory"); __builtin_amdgcn_s_waitcnt(0x0F70); } while (0)
#define OPAQUE(x) asm volatile("" : "+v"(x))
template <class T> DI LAS T* lds(unsigned a) { return (LAS T*)(size_t)a; }
DI bf16x8 ldsr128(unsigned a) { return *(const LAS bf16x8*)(size_t)a; }
DI s16x4 trr(unsigned a) { return __builtin_amdgcn_ds_read_tr16_b64_v4i16((LAS s16x4*)(size_t)a); }
DI bf16x8 trfrag(unsigned a, unsigned b) {
    s16x4 lo = trr(a), hi = trr(b);
    return __builtin_bit_cast(bf16x8, __builtin_shufflevector(lo, hi, 0, 1, 2, 3, 4, 5, 6, 7));
}
DI f32x4 mfma16(bf16x8 a, bf16x8 b, f32x4 c) { return __builtin_amdgcn_mfma_f32_16x16x32_bf16(a, b, c, 0, 0, 0); }
template <int CTRL> DI float dpp(float v) { return __int_as_float(__builtin_amdgcn_mov_dpp(__float_as_int(v), CTRL, 0xf, 0xf, true)); }
DI float swap16_add(float a, float b) { auto r = __builtin_amdgcn_permlane16_swap(__float_as_uint(a), __float_as_uint(b), false, false); return __uint_as_float(r[0]) + __uint_as_float(r[1]); }
DI float swap32_add(float a, float b) { auto r = __builtin_amdgcn_permlane32_swap(__float_as_uint(a), __float_as_uint(b), false, false); return __uint_as_float(r[0]) + __uint_as_float(r[1]); }
DI unsigned umax(unsigned a, unsigned b) { return a > b ? a : b; }
template <int CTRL> DI unsigned dppu(unsigned v) { return __builtin_amdgcn_mov_dpp(v, CTRL, 0xf, 0xf, true); }
DI unsigned swap16_umax(unsigned a) { auto r = __builtin_amdgcn_permlane16_swap(a, a, false, false); return umax(r[0], r[1]); }
DI unsigned swap32_umax(unsigned a) { auto r = __builtin_amdgcn_permlane32_swap(a, a, false, false); return umax(r[0], r[1]); }
DI unsigned row_umax(unsigned x) { x = umax(x, dppu<0xB1>(x)); x = umax(x, dppu<0x4E>(x)); x = umax(x, dppu<0x141>(x)); x = umax(x, dppu<0x140>(x)); return x; }
DI float swap32_max(float a) { auto r = __builtin_amdgcn_permlane32_swap(__float_as_uint(a), __float_as_uint(a), false, false); return fmaxf(__uint_as_float(r[0]), __uint_as_float(r[1])); }
DI float row_sum(float x) {
    x += dpp<0xB1>(x); x += dpp<0x4E>(x); x += dpp<0x141>(x); x += dpp<0x140>(x); return x; }

typedef __amdgpu_buffer_rsrc_t rsrc_t;
DI void st128(rsrc_t r, unsigned off, u32x4 v) { __builtin_amdgcn_raw_buffer_store_b128(v, r, off, 0, 16); }
DI void st128s(rsrc_t r, unsigned off, unsigned soff, u32x4 v) { st128(r, off + soff, v); }
DI void st64(rsrc_t r, unsigned off, u32x2 v) { __builtin_amdgcn_raw_buffer_store_b64(v, r, off, 0, 16); }
DI void st32(rsrc_t r, unsigned off, unsigned v) { __builtin_amdgcn_raw_buffer_store_b32(v, r, off, 0, 16); }
DI u32x4 ld128(rsrc_t r, unsigned off) { return __builtin_amdgcn_raw_buffer_load_b128(r, off, 0, 16); }
DI unsigned ld32(rsrc_t r, unsigned off) { return __builtin_amdgcn_raw_buffer_load_b32(r, off, 0, 16); }

template <int MASK, int SLEEP>
DI u64 wait_flags(const char* words, u64 tag, u64 seen, int lane) {
    gu64* p = (gu64*)(size_t)words + (lane & MASK);
    if (!__all((seen ^ tag) < 0x1000000ull)) {
        const u64 t0 = __builtin_amdgcn_s_memrealtime();
        for (;;) {
            seen = __hip_atomic_load(p, RLX_AGENT);
            if (__all((seen ^ tag) < 0x1000000ull)) break;
            __builtin_amdgcn_s_sleep(SLEEP);
            if (__builtin_amdgcn_s_memrealtime() - t0 > 2000000ull) break;
        }
    }
    asm volatile("" ::: "memory");
    return seen;
}
template <int MASK> DI u64 peek_flags(const char* words, int lane) { return __hip_atomic_load((gu64*)(size_t)words + (lane & MASK), RLX_AGENT); }
DI void post_flag(char* word, u64 val, int lane) {
    WAITV();
    if (lane == 0) __hip_atomic_store((gu64*)(size_t)word, val, RLX_AGENT);
}
DI u64 make_tag(u64 epoch) { return ((epoch + 1) * 0x9E3779B97F4A7C15ull) << 24; }

#define P1_PRO \
    "ds_read_b128 v[152:155], v212 offset:0\n" \
    "ds_read_b128 v[156:159], v212 offset:2048\n" \
    "ds_read_b128 v[160:163], v212 offset:4096\n" \
    "ds_read_b128 v[164:167], v212 offset:6144\n" \
    "ds_read_b64_tr_b16 v[184:185], v214 offset:0\n" \
    "ds_read_b64_tr_b16 v[186:187], v214 offset:432\n" \
    "ds_read_b64_tr_b16 v[188:189], v214 offset:40\n" \
    "ds_read_b64_tr_b16 v[190:191], v214 offset:472\n" \
    "ds_read_b64_tr_b16 v[192:193], v214 offset:144\n" \
    "ds_read_b64_tr_b16 v[194:195], v214 offset:576\n" \
    "ds_read_b64_tr_b16 v[196:197], v214 offset:184\n" \
    "ds_read_b64_tr_b16 v[198:199], v214 offset:616\n" \
    "ds_read_b64_tr_b16 v[200:201], v214 offset:288\n" \
    "ds_read_b64_tr_b16 v[202:203], v214 offset:720\n" \
    "ds_read_b64_tr_b16 v[204:205], v214 offset:328\n" \
    "ds_read_b64_tr_b16 v[206:207], v214 offset:760\n" \
    "ds_read_b64_tr_b16 v[208:209], v215 offset:0\n" \
    "ds_read_b64_tr_b16 v[210:211], v215 offset:432\n" \
    "s_waitcnt lgkmcnt(0)\n"
#define P1_BODY \
    "v_mfma_f32_16x16x32_bf16 v[40:43], v[184:187], v[152:155], v[40:43]\n" \
    "ds_read_b128 v[168:171], v213 offset:0\n" \
    "v_mfma_f32_16x16x32_bf16 v[44:47], v[184:187], v[156:159], v[44:47]\n" \
    "ds_read_b128 v[172:175], v213 offset:2048\n" \
    "v_mfma_f32_16x16x32_bf16 v[48:51], v[184:187], v[160:163], v[48:51]\n" \
    "ds_read_b128 v[176:179], v213 offset:4096\n" \
    "v_mfma_f32_16x16x32_bf16 v[52:55], v[184:187], v[164:167], v[52:55]\n" \
    "ds_read_b128 v[180:183], v213 offset:6144\n" \
    "v_mfma_f32_16x16x32_bf16 v[56:59], v[188:191], v[152:155], v[56:59]\n" \
    "ds_read_b64_tr_b16 v[184:185], v214 offset:13824\n" \
    "v_mfma_f32_16x16x32_bf16 v[60:63], v[188:191], v[156:159], v[60:63]\n" \
    "ds_read_b64_tr_b16 v[186:187], v214 offset:14256\n" \
    "v_mfma_f32_16x16x32_bf16 v[64:67], v[188:191], v[160:163], v[64:67]\n" \
    "v_mfma_f32_16x16x32_bf16 v[68:71], v[188:191], v[164:167], v[68:71]\n" \
    "MFK 72, 192, 152\n" \
    "ds_read_b64_tr_b16 v[188:189], v214 offset:13864\n" \
    "MFK 76, 192, 156\n" \
    "ds_read_b64_tr_b16 v[190:191], v214 offset:14296\n" \
    "MFK 80, 192, 160\n" \
    "MFK 84, 192, 164\n" \
    "MFK 88, 196, 152\n" \
    "ds_read_b64_tr_b16 v[192:193], v214 offset:13968\n" \
    "MFK 92, 196, 156\n" \
    "ds_read_b64_tr_b16 v[194:195], v214 offset:14400\n" \
    "MFK 96, 196, 160\n" \
    "MFK 100, 196, 164\n" \
    "v_mfma_f32_16x16x32_bf16 v[104:107], v[152:155], v[200:203], v[104:107]\n" \
    "ds_read_b64_tr_b16 v[196:197], v214 offset:14008\n" \
    "v_mfma_f32_16x16x32_bf16 v[108:111], v[156:159], v[200:203], v[108:111]\n" \
    "ds_read_b64_tr_b16 v[198:199], v214 offset:14440\n" \
    "v_mfma_f32_16x16x32_bf16 v[112:115], v[160:163], v[200:203], v[112:115]\n" \
    "v_mfma_f32_16x16x32_bf16 v[116:119], v[164:167], v[200:203], v[116:119]\n" \
    "v_mfma_f32_16x16x32_bf16 v[120:123], v[152:155], v[204:207], v[120:123]\n" \
    "ds_read_b64_tr_b16 v[200:201], v214 offset:14112\n" \
    "v_mfma_f32_16x16x32_bf16 v[124:127], v[156:159], v[204:207], v[124:127]\n" \
    "ds_read_b64_tr_b16 v[202:203], v214 offset:14544\n" \
    "v_mfma_f32_16x16x32_bf16 v[128:131], v[160:163], v[204:207], v[128:131]\n" \
    "v_mfma_f32_16x16x32_bf16 v[132:135], v[164:167], v[204:207], v[132:135]\n" \
    "v_mfma_f32_16x16x32_bf16 v[136:139], v[152:155], v[208:211], v[136:139]\n" \
    "ds_read_b64_tr_b16 v[204:205], v214 offset:14152\n" \
    "v_mfma_f32_16x16x32_bf16 v[140:143], v[156:159], v[208:211], v[140:143]\n" \
    "ds_read_b64_tr_b16 v[206:207], v214 offset:14584\n" \
    "v_mfma_f32_16x16x32_bf16 v[144:147], v[160:163], v[208:211], v[144:147]\n" \
    "v_mfma_f32_16x16x32_bf16 v[148:151], v[164:167], v[208:211], v[148:151]\n" \
    "s_waitcnt lgkmcnt(10)\n" \
    "v_mfma_f32_16x16x32_bf16 v[40:43], v[184:187], v[168:171], v[40:43]\n" \
    "ds_read_b64_tr_b16 v[208:209], v215 offset:13824\n" \
    "v_mfma_f32_16x16x32_bf16 v[44:47], v[184:187], v[172:175], v[44:47]\n" \
    "ds_read_b64_tr_b16 v[210:211], v215 offset:14256\n" \
    "v_mfma_f32_16x16x32_bf16 v[48:51], v[184:187], v[176:179], v[48:51]\n" \
    "v_mfma_f32_16x16x32_bf16 v[52:55], v[184:187], v[180:183], v[52:55]\n" \
    "s_waitcnt lgkmcnt(10)\n" \
    "v_mfma_f32_16x16x32_bf16 v[56:59], v[188:191], v[168:171], v[56:59]\n" \
    "v_mfma_f32_16x16x32_bf16 v[60:63], v[188:191], v[172:175], v[60:63]\n" \
    "v_mfma_f32_16x16x32_bf16 v[64:67], v[188:191], v[176:179], v[64:67]\n" \
    "v_mfma_f32_16x16x32_bf16 v[68:71], v[188:191], v[180:183], v[68:71]\n" \
    "s_waitcnt lgkmcnt(8)\n" \
    "MFK 72, 192, 168\n" \
    "MFK 76, 192, 172\n" \
    "MFK 80, 192, 176\n" \
    "MFK 84, 192, 180\n" \
    "s_waitcnt lgkmcnt(6)\n" \
    "MFK 88, 196, 168\n" \
    "MFK 92, 196, 172\n" \
    "MFK 96, 196, 176\n" \
    "MFK 100, 196, 180\n" \
    "s_waitcnt lgkmcnt(4)\n" \
    "v_mfma_f32_16x16x32_bf16 v[104:107], v[168:171], v[200:203], v[104:107]\n" \
    "v_mfma_f32_16x16x32_bf16 v[108:111], v[172:175], v[200:203], v[108:111]\n" \
    "v_mfma_f32_16x16x32_bf16 v[112:115], v[176:179], v[200:203], v[112:115]\n" \
    "v_mfma_f32_16x16x32_bf16 v[116:119], v[180:183], v[200:203], v[116:119]\n" \
    "s_waitcnt lgkmcnt(2)\n" \
    "v_mfma_f32_16x16x32_bf16 v[120:123], v[168:171], v[204:207], v[120:123]\n" \
    "v_mfma_f32_16x16x32_bf16 v[124:127], v[172:175], v[204:207], v[124:127]\n" \
    "v_mfma_f32_16x16x32_bf16 v[128:131], v[176:179], v[204:207], v[128:131]\n" \
    "v_mfma_f32_16x16x32_bf16 v[132:135], v[180:183], v[204:207], v[132:135]\n" \
    "s_waitcnt lgkmcnt(0)\n" \
    "v_mfma_f32_16x16x32_bf16 v[136:139], v[168:171], v[208:211], v[136:139]\n" \
    "v_mfma_f32_16x16x32_bf16 v[140:143], v[172:175], v[208:211], v[140:143]\n" \
    "v_mfma_f32_16x16x32_bf16 v[144:147], v[176:179], v[208:211], v[144:147]\n" \
    "v_mfma_f32_16x16x32_bf16 v[148:151], v[180:183], v[208:211], v[148:151]\n"
#define P1_TAIL \
    "v_mfma_f32_16x16x32_bf16 v[40:43], v[184:187], v[152:155], v[40:43]\n" \
    "ds_read_b128 v[168:171], v213 offset:0\n" \
    "v_mfma_f32_16x16x32_bf16 v[44:47], v[184:187], v[156:159], v[44:47]\n" \
    "ds_read_b128 v[172:175], v213 offset:2048\n" \
    "v_mfma_f32_16x16x32_bf16 v[48:51], v[184:187], v[160:163], v[48:51]\n" \
    "ds_read_b128 v[176:179], v213 offset:4096\n" \
    "v_mfma_f32_16x16x32_bf16 v[52:55], v[184:187], v[164:167], v[52:55]\n" \
    "ds_read_b128 v[180:183], v213 offset:6144\n" \
    "v_mfma_f32_16x16x32_bf16 v[56:59], v[188:191], v[152:155], v[56:59]\n" \
    "ds_read_b64_tr_b16 v[184:185], v214 offset:13824\n" \
    "v_mfma_f32_16x16x32_bf16 v[60:63], v[188:191], v[156:159], v[60:63]\n" \
    "ds_read_b64_tr_b16 v[186:187], v214 offset:14256\n" \
    "v_mfma_f32_16x16x32_bf16 v[64:67], v[188:191], v[160:163], v[64:67]\n" \
    "v_mfma_f32_16x16x32_bf16 v[68:71], v[188:191], v[164:167], v[68:71]\n" \
    "MFK 72, 192, 152\n" \
    "ds_read_b64_tr_b16 v[188:189], v214 offset:13864\n" \
    "MFK 76, 192, 156\n" \
    "ds_read_b64_tr_b16 v[190:191], v214 offset:14296\n" \
    "MFK 80, 192, 160\n" \
    "MFK 84, 192, 164\n" \
    "MFK 88, 196, 152\n" \
    "ds_read_b64_tr_b16 v[192:193], v214 offset:13968\n" \
    "MFK 92, 196, 156\n" \
    "ds_read_b64_tr_b16 v[194:195], v214 offset:14400\n" \
    "MFK 96, 196, 160\n" \
    "MFK 100, 196, 164\n" \
    "v_mfma_f32_16x16x32_bf16 v[104:107], v[152:155], v[200:203], v[104:107]\n" \
    "ds_read_b64_tr_b16 v[196:197], v214 offset:14008\n" \
    "v_mfma_f32_16x16x32_bf16 v[108:111], v[156:159], v[200:203], v[108:111]\n" \
    "ds_read_b64_tr_b16 v[198:199], v214 offset:14440\n" \
    "v_mfma_f32_16x16x32_bf16 v[112:115], v[160:163], v[200:203], v[112:115]\n" \
    "v_mfma_f32_16x16x32_bf16 v[116:119], v[164:167], v[200:203], v[116:119]\n" \
    "v_mfma_f32_16x16x32_bf16 v[120:123], v[152:155], v[204:207], v[120:123]\n" \
    "ds_read_b64_tr_b16 v[200:201], v214 offset:14112\n" \
    "v_mfma_f32_16x16x32_bf16 v[124:127], v[156:159], v[204:207], v[124:127]\n" \
    "ds_read_b64_tr_b16 v[202:203], v214 offset:14544\n" \
    "v_mfma_f32_16x16x32_bf16 v[128:131], v[160:163], v[204:207], v[128:131]\n" \
    "v_mfma_f32_16x16x32_bf16 v[132:135], v[164:167], v[204:207], v[132:135]\n" \
    "v_mfma_f32_16x16x32_bf16 v[136:139], v[152:155], v[208:211], v[136:139]\n" \
    "ds_read_b64_tr_b16 v[204:205], v214 offset:14152\n" \
    "v_mfma_f32_16x16x32_bf16 v[140:143], v[156:159], v[208:211], v[140:143]\n" \
    "ds_read_b64_tr_b16 v[206:207], v214 offset:14584\n" \
    "v_mfma_f32_16x16x32_bf16 v[144:147], v[160:163], v[208:211], v[144:147]\n" \
    "v_mfma_f32_16x16x32_bf16 v[148:151], v[164:167], v[208:211], v[148:151]\n" \
    "s_waitcnt lgkmcnt(10)\n" \
    "v_mfma_f32_16x16x32_bf16 v[40:43], v[184:187], v[168:171], v[40:43]\n" \
    "ds_read_b64_tr_b16 v[208:209], v215 offset:13824\n" \
    "v_mfma_f32_16x16x32_bf16 v[44:47], v[184:187], v[172:175], v[44:47]\n" \
    "ds_read_b64_tr_b16 v[210:211], v215 offset:14256\n" \
    "v_mfma_f32_16x16x32_bf16 v[48:51], v[184:187], v[176:179], v[48:51]\n" \
    "v_mfma_f32_16x16x32_bf16 v[52:55], v[184:187], v[180:183], v[52:55]\n" \
    "s_waitcnt lgkmcnt(10)\n" \
    "v_mfma_f32_16x16x32_bf16 v[56:59], v[188:191], v[168:171], v[56:59]\n" \
    "v_mfma_f32_16x16x32_bf16 v[60:63], v[188:191], v[172:175], v[60:63]\n" \
    "v_mfma_f32_16x16x32_bf16 v[64:67], v[188:191], v[176:179], v[64:67]\n" \
    "v_mfma_f32_16x16x32_bf16 v[68:71], v[188:191], v[180:183], v[68:71]\n" \
    "s_waitcnt lgkmcnt(8)\n" \
    "MFK 72, 192, 168\n" \
    "MFK 76, 192, 172\n" \
    "MFK 80, 192, 176\n" \
    "MFK 84, 192, 180\n" \
    "s_waitcnt lgkmcnt(6)\n" \
    "MFK 88, 196, 168\n" \
    "MFK 92, 196, 172\n" \
    "MFK 96, 196, 176\n" \
    "MFK 100, 196, 180\n" \
    "s_waitcnt lgkmcnt(4)\n" \
    "v_mfma_f32_16x16x32_bf16 v[104:107], v[168:171], v[200:203], v[104:107]\n" \
    "v_mfma_f32_16x16x32_bf16 v[108:111], v[172:175], v[200:203], v[108:111]\n" \
    "v_mfma_f32_16x16x32_bf16 v[112:115], v[176:179], v[200:203], v[112:115]\n" \
    "v_mfma_f32_16x16x32_bf16 v[116:119], v[180:183], v[200:203], v[116:119]\n" \
    "s_waitcnt lgkmcnt(2)\n" \
    "v_mfma_f32_16x16x32_bf16 v[120:123], v[168:171], v[204:207], v[120:123]\n" \
    "v_mfma_f32_16x16x32_bf16 v[124:127], v[172:175], v[204:207], v[124:127]\n" \
    "v_mfma_f32_16x16x32_bf16 v[128:131], v[176:179], v[204:207], v[128:131]\n" \
    "v_mfma_f32_16x16x32_bf16 v[132:135], v[180:183], v[204:207], v[132:135]\n" \
    "s_waitcnt lgkmcnt(0)\n" \
    "v_mfma_f32_16x16x32_bf16 v[136:139], v[168:171], v[208:211], v[136:139]\n" \
    "v_mfma_f32_16x16x32_bf16 v[140:143], v[172:175], v[208:211], v[140:143]\n" \
    "v_mfma_f32_16x16x32_bf16 v[144:147], v[176:179], v[208:211], v[144:147]\n" \
    "v_mfma_f32_16x16x32_bf16 v[148:151], v[180:183], v[208:211], v[148:151]\n"

DI u32x4 fm_pack(f32x4 t0, f32x4 t1) {
    unsigned a0 = pk(t0[0], t0[1]), a1 = pk(t0[2], t0[3]), b0 = pk(t1[0], t1[1]), b1 = pk(t1[2], t1[3]);
    auto r0 = __builtin_amdgcn_permlane16_swap(a0, b0, false, false);
    auto r1 = __builtin_amdgcn_permlane16_swap(a1, b1, false, false);
    return (u32x4){r0[0], r1[0], r0[1], r1[1]};
}
DI u32x4 tm_pack(f32x4 t0, f32x4 t1) { return (u32x4){pk(t0[0], t0[1]), pk(t0[2], t0[3]), pk(t1[0], t1[1]), pk(t1[2], t1[3])}; }
DI f32x4 sub4(const f32x16& a, int r) { return (f32x4){a[4 * r], a[4 * r + 1], a[4 * r + 2], a[4 * r + 3]}; }

DI float nrm8(u32x4 v) {
    float s = __builtin_amdgcn_fdot2_f32_bf16(__builtin_bit_cast(bf16x2, v[0]), __builtin_bit_cast(bf16x2, v[0]), 0.f, false);
#pragma unroll
    for (int i = 1; i < 4; i++) s = __builtin_amdgcn_fdot2_f32_bf16(__builtin_bit_cast(bf16x2, v[i]), __builtin_bit_cast(bf16x2, v[i]), s, false);
    return s;
}
DI u32x4 pair_up(u32x4 vr, bool odd, unsigned selLo, unsigned selHi) {
    const unsigned a = odd ? vr[2] : vr[0], b = odd ? vr[3] : vr[1];
    const unsigned c = dppu<0xB1>(odd ? vr[0] : vr[2]), d = dppu<0xB1>(odd ? vr[1] : vr[3]);
    return (u32x4){__builtin_amdgcn_perm(a, c, selLo), __builtin_amdgcn_perm(a, c, selHi), __builtin_amdgcn_perm(b, d, selLo), __builtin_amdgcn_perm(b, d, selHi)};
}

DI void p1_loader(const char* X, const char* W, unsigned c0, unsigned c1, unsigned c2, int r, int rot, int lane, int lw) {
    unsigned xo, wo[7];
    { const int row = lw * 8 + (lane >> 3); xo = (unsigned)(r * 128 + row) * 1152u + (((lane & 7) ^ (row & 7)) << 4); }
#pragma unroll
    for (int i = 0; i < 4; i++) glds16(xo, X + rot * 128 + i * 32 * 1152, (lw + 4 * i) * 1024);
#pragma unroll
    for (int i = 0; i < 7; i++) {
        unsigned u = (lw + 4 * i) * 64u + lane; if (i == 6 && u >= 1728u) u = 0;
        const unsigned lr = (u * 2428u) >> 16, pc = u - lr * 27u;
        const unsigned k = (lr & ~7u) | ((lr & 1u) << 2) | ((lr >> 1) & 3u);
        wo[i] = k * 3456u + pc * 16u + (pc < 9 ? c0 : pc < 18 ? c1 : c2);
        if (i < 6 || lw < 3) glds16(wo[i], W + rot * 64 * 3456, 16384 + (lw + 4 * i) * 1024);
    }
    unsigned dst = ST1 + lw * 1024;
#pragma unroll 1
    for (int s = 1; s < 9; s++) {
        rot = rot == 8 ? 0 : rot + 1;
        const char* xb = X + rot * 128; const char* wb = W + rot * 64 * 3456;
#pragma unroll
        for (int i = 0; i < 4; i++) glds16(xo, xb + i * 32 * 1152, dst + i * 4096);
#pragma unroll
        for (int i = 0; i < 6; i++) glds16(wo[i], wb, dst + 16384 + i * 4096);
        if (lw < 3) glds16(wo[6], wb, dst + 16384 + 24576);
        dst = dst >= 2 * ST1 ? dst - 2 * ST1 : dst + ST1;
        WAITV_BAR(10);
    }
    WAITV_BAR(0);
}

template <int TY>
DI unsigned p1_consumer(char* ws, rsrc_t rs, int quad, int r, int lane, int w STAMP_ARGS) {
    constexpr bool ISB = TY == 2;
    const int rh = w & 1, pr = w >> 1, g = lane >> 4, m = lane & 15, q4 = m >> 2, p = m & 3;
    unsigned aW = 16384 + (g * 8 + q4 * 2) * 432 + pr * 72 + 8 * p;
    unsigned aR = 16384 + (g * 8 + q4 * 2) * 432 + pr * 72 + (p < 2 ? p : 2) * 144 + 32;
    unsigned aX0 = (64 * rh + m) * 128 + ((g ^ (m & 7)) << 4), aX1 = aX0 ^ 64;
    const int h0 = 4 * quad + 2 * pr, hA = 8 * (quad >> 1) + 2 * pr, T0 = 4 * r + 2 * rh;
    const int rho = ((m >> 2) << 3) + ((g & 1) << 2) + (m & 3);
    const unsigned fmoff = (g >> 1) * 512 + rho * 16, kvo = OFF_KV + (T0 >> 3) * CHUNK, u0 = T0 & 7, pofs = (8 * g + 4 * (m & 1)) * 4;
    const bool odd = lane & 1;
    unsigned selLo = odd ? 0x05040100u : 0x01000504u, selHi = odd ? 0x07060302u : 0x03020706u;
    unsigned oF, oV = 0, oR, oR1;
    if (ISB) {
        oF = OFF_Q1 + (32 + h0) * 65536 + T0 * 1024 + fmoff;
        oR = m < 4 ? OFF_Q2 + (32 + h0 + (m >> 1)) * 8192 + T0 * 128 + pofs : WS_USED; oR1 = oR + 128;
    } else {
        const int hr = hA + ((m >> 1) & 1) + (m & 4);
        oF = (TY == 0 ? OFF_Q1 + hA * 65536 + T0 * 1024 : kvo + hA * KV_HEAD + u0 * 1024) + fmoff;
        oV = kvo + h0 * KV_HEAD + 9216 + u0 * 1152 + g * 256 + ((m ^ ((g >> 1) << 3)) << 4);
        oR = (TY == 0 ? OFF_Q2 + hr * 8192 + T0 * 128 : kvo + hr * KV_HEAD + 8192 + u0 * 128) + pofs; oR1 = oR + 128;
        if (m >= 8) { oR = kvo + (h0 + ((m >> 1) & 1)) * KV_HEAD + 9216 + u0 * 1152 + 1024 + g * 32 + (m & 1) * 16; oR1 = oR + 1152; }
        if (m >= 12) { oR = WS_USED; oR1 = WS_USED; }
    }
    OPAQUE(oF); OPAQUE(oV); OPAQUE(oR); OPAQUE(oR1); OPAQUE(selLo); OPAQUE(selHi);
    f32x16 acc[7];
    unsigned cnt, ph, dl;
    asm volatile(
        ".macro MFK a, w, x\n"
        ".if %[isb]\n v_mfma_f32_16x16x32_bf16 v[\\a:\\a+3], v[\\x:\\x+3], v[\\w:\\w+3], v[\\a:\\a+3]\n"
        ".else\n v_mfma_f32_16x16x32_bf16 v[\\a:\\a+3], v[\\w:\\w+3], v[\\x:\\x+3], v[\\a:\\a+3]\n .endif\n .endm\n"
        ".set r_, 40\n .rept 56\n v_mov_b64 v[r_:r_+1], 0\n .set r_, r_+2\n .endr\n"
        "s_mov_b32 %[c], 8\n s_mov_b32 %[p], 0\n"
        "s_waitcnt lgkmcnt(0)\n s_barrier\n"
        P1_PRO
        "1:\n"
        P1_BODY
        "s_sub_u32 %[c], %[c], 1\n s_cmp_lg_u32 %[c], 0\n s_cbranch_scc1 1b\n"
        P1_TAIL
        "s_nop 7\n"
        ".purgem MFK\n"
        : "={v[40:55]}"(acc[0]), "={v[56:71]}"(acc[1]), "={v[72:87]}"(acc[2]), "={v[88:103]}"(acc[3]), "={v[104:119]}"(acc[4]),
          "={v[120:135]}"(acc[5]), "={v[136:151]}"(acc[6]), "+{v212}"(aX0), "+{v213}"(aX1), "+{v214}"(aW), "+{v215}"(aR),
          [c] "=&s"(cnt), [p] "=&s"(ph), [d] "=&s"(dl)
        : [neg] "s"(-2 * (int)ST1), [isb] "i"(ISB ? 1 : 0)
        : "memory", "scc", "v152", "v153", "v154", "v155", "v156", "v157", "v158", "v159", "v160", "v161", "v162", "v163", "v164", "v165",
          "v166", "v167", "v168", "v169", "v170", "v171", "v172", "v173", "v174", "v175", "v176", "v177", "v178", "v179", "v180", "v181",
          "v182", "v183", "v184", "v185", "v186", "v187", "v188", "v189", "v190", "v191", "v192", "v193", "v194", "v195", "v196", "v197",
          "v198", "v199", "v200", "v201", "v202", "v203", "v204", "v205", "v206", "v207", "v208", "v209", "v210", "v211");
    STAMP(11);
    if (!ISB) {
        constexpr unsigned HS = TY == 0 ? 65536 : KV_HEAD;
        const float rsc = TY == 0 && m < 8 ? QSCALE : 1.f;
        unsigned kn0 = 0, kn1 = 0, rm = 0;
#pragma unroll
        for (int tl = 0; tl < 2; tl++) {
            float nn[4];
#pragma unroll
            for (int jj = 0; jj < 4; jj++) {
                f32x4 t0 = sub4(acc[jj], 2 * tl), t1 = sub4(acc[jj], 2 * tl + 1);
                if (TY == 0) { t0 *= QSCALE; t1 *= QSCALE; }
                const u32x4 pc = fm_pack(t0, t1);
                st128s(rs, oF + tl * 1024, ((jj & 1) + 2 * (jj & 2)) * HS, pc);
                if (TY == 1) nn[jj] = nrm8(pc);
            }
#pragma unroll
            for (int jj = 0; jj < 2; jj++) st128s(rs, oV + tl * 1152, jj * KV_HEAD, tm_pack(sub4(acc[4 + jj], 2 * tl), sub4(acc[4 + jj], 2 * tl + 1)));
            f32x4 r0 = sub4(acc[6], 2 * tl), r1 = sub4(acc[6], 2 * tl + 1);
            if (TY == 0) { r0 *= rsc; r1 *= rsc; }
            const u32x4 vr = tm_pack(r0, r1), v = pair_up(vr, odd, selLo, selHi);
            st128(rs, tl ? oR1 : oR, m >= 8 ? vr : v);
            if (TY == 1) {
                kn0 = umax(kn0, __float_as_uint(swap32_add(nn[0], nn[1]))); kn1 = umax(kn1, __float_as_uint(swap32_add(nn[2], nn[3])));
#pragma unroll
                for (int i = 0; i < 4; i++)
                    rm = umax(rm, __float_as_uint(__builtin_amdgcn_fdot2_f32_bf16(__builtin_bit_cast(bf16x2, v[i]), __builtin_bit_cast(bf16x2, v[i]), 0.f, false)));
            }
        }
        if (TY == 0) return 0;
        kn0 = swap16_umax(row_umax(kn0)); kn1 = swap16_umax(row_umax(kn1));
        rm = swap32_umax(swap16_umax(umax(rm, dppu<0xB1>(rm))));
        unsigned kb = (__float_as_uint(__uint_as_float(m & 4 ? kn1 : kn0) + __uint_as_float(rm)) + 0x7ffffu) >> 19;
        kb = kb > 0xfffu ? 0xfffu : kb;
        const unsigned p0 = __builtin_amdgcn_readlane(kb, 0) | (__builtin_amdgcn_readlane(kb, 34) << 12);
        const unsigned p1 = __builtin_amdgcn_readlane(kb, 4) | (__builtin_amdgcn_readlane(kb, 38) << 12);
        return odd ? p1 : p0;
    } else {
        f32x4 M0 = {0, 0, 0, 0}, M1 = M0, M2 = M0, M3 = M0, M4 = M0, M5 = M0, M6 = M0;
#pragma unroll
        for (int tl = 0; tl < 2; tl++) {
            const f32x4 r0 = sub4(acc[6], 2 * tl), r1 = sub4(acc[6], 2 * tl + 1), p0 = phi4(r0), p1 = phi4(r1);
            const bf16x8 k0 = __builtin_bit_cast(bf16x8, tm_pack(phi4(sub4(acc[2], 2 * tl)), phi4(sub4(acc[2], 2 * tl + 1))));
            const bf16x8 k1 = __builtin_bit_cast(bf16x8, tm_pack(phi4(sub4(acc[3], 2 * tl)), phi4(sub4(acc[3], 2 * tl + 1))));
            const u32x4 kru = tm_pack(p0, p1);
            const bf16x8 kr = __builtin_bit_cast(bf16x8, kru);
            const bf16x8 v0 = __builtin_bit_cast(bf16x8, tm_pack(sub4(acc[4], 2 * tl), sub4(acc[4], 2 * tl + 1)));
            const bf16x8 v1 = __builtin_bit_cast(bf16x8, tm_pack(sub4(acc[5], 2 * tl), sub4(acc[5], 2 * tl + 1)));
            u32x4 vru = tm_pack(r0, r1);
            if (m >= 12) vru = (u32x4){ONES, ONES, ONES, ONES};
            const bf16x8 vr = __builtin_bit_cast(bf16x8, vru);
            M0 = mfma16(k0, v0, M0); M4 = mfma16(k1, v1, M4); M1 = mfma16(k0, vr, M1); M5 = mfma16(k1, vr, M5);
            M2 = mfma16(kr, v0, M2); M6 = mfma16(kr, v1, M6); M3 = mfma16(kr, vr, M3);
            st128(rs, tl ? oR1 : oR, pair_up(kru, odd, selLo, selHi));
        }
        const bool h1c = m == 10 || m == 11 || m == 13;
        f32x4 A2 = h1c ? M5 : M1, A3 = {M2[0], M2[1], M6[2], M6[3]};
        LAS f32x4* red = lds<f32x4>(L_RED1) + pr * 320 + lane;
        volatile LAS unsigned* hs = lds<unsigned>(L_FLAG + 4 + 4 * pr);
        if (rh) {
            red[0] = M0; red[64] = M4; red[128] = A2; red[192] = A3; red[256] = M3;
            asm volatile("s_waitcnt lgkmcnt(0)" ::: "memory");
            if (lane == 0) *hs = 1;
        }
#pragma unroll
        for (int tl = 0; tl < 2; tl++)
#pragma unroll
            for (int jj = 0; jj < 2; jj++) st128s(rs, oF + tl * 1024, jj * 65536, fm_pack(phi4(sub4(acc[jj], 2 * tl)), phi4(sub4(acc[jj], 2 * tl + 1))));
        if (!rh) {
            while (*hs == 0) __builtin_amdgcn_s_sleep(1);
            asm volatile("" ::: "memory");
            M0 += red[0]; M4 += red[64]; A2 += red[128]; A3 += red[192]; M3 += red[256];
            const unsigned kb = OFF_KVP + (h0 * 16 + r) * 1408, e = m >= 12 ? 18 : 16 + (m & 1);
            const bool ex = m >= 8 && m < 14;
            const unsigned kbx = kb + (h1c ? 16 * 1408 : 0);
            st128(rs, kb + m * 64 + g * 16, __builtin_bit_cast(u32x4, M0));
            st128(rs, kb + 16 * 1408 + m * 64 + g * 16, __builtin_bit_cast(u32x4, M4));
            st128(rs, ex ? kbx + e * 64 + g * 16 : WS_USED, __builtin_bit_cast(u32x4, A2));
            const u32x4 a3 = __builtin_bit_cast(u32x4, A3), m3 = __builtin_bit_cast(u32x4, M3);
            st64(rs, g == 1 ? kb + 1216 + m * 8 : WS_USED, (u32x2){a3[0], a3[1]});
            st64(rs, g == 1 ? kb + 16 * 1408 + 1216 + m * 8 : WS_USED, (u32x2){a3[2], a3[3]});
            st64(rs, g == 1 && ex ? kbx + 1216 + e * 8 : WS_USED, h1c ? (u32x2){m3[2], m3[3]} : (u32x2){m3[0], m3[1]});
        }
        return 0;
    }
}

#define ATT_MACROS \
    ".macro EXPS a, b\n .set r_, \\a\n .rept (\\b)-(\\a)\n v_exp_f32 v[r_], v[r_]\n .set r_, r_+1\n .endr\n .endm\n" \
    ".macro VRD dst, addr, off\n s_mov_b64 exec, %[vm]\n ds_read_b128 v[\\dst:\\dst+3], \\addr offset:\\off\n s_mov_b64 exec, -1\n .endm\n" \
    ".macro DEXP n, r\n"                                    \
    ".set pi_, (\\n)-3+5*(%[wm]-1)\n" \
    ".if (\\n) < 0 || (\\n) > 7 || ((\\n) < 3 && %[sn] < 0) || ((\\n) >= 3 && %[wm] == 0)\n v_exp_f32 v[\\r], v[\\r]\n" \
    ".elseif (\\n) == 0\n s_add_u32 m0, %[m2], %[sn]*18432\n v_exp_f32 v[\\r], v[\\r]\n global_load_lds_dwordx4 v190, %[sc] sc1\n" \
    ".elseif (\\n) == 1\n v_exp_f32 v[\\r], v[\\r]\n global_load_lds_dwordx4 v190, %[sc] offset:1024 sc1\n" \
    ".elseif (\\n) == 2\n s_add_u32 m0, %[m3], %[sn]*18432\n v_exp_f32 v[\\r], v[\\r]\n global_load_lds_dwordx4 v191, %[sc] sc1\n" \
    ".elseif pi_ < 8\n s_add_u32 s98, s96, 82944*pi_\n s_addc_u32 s99, s97, 0\n s_add_u32 m0, %[m1], 55296+8192*pi_+1024*(pi_&4)\n" \
    " v_exp_f32 v[\\r], v[\\r]\n global_load_lds_dwordx4 %[w0], s[98:99]\n" \
    ".elseif pi_ == 8\n s_mov_b32 m0, %[mr]\n v_exp_f32 v[\\r], v[\\r]\n global_load_lds_dwordx4 %[w1], s[96:97]\n" \
    ".else\n s_add_u32 m0, %[m1], 129024\n v_exp_f32 v[\\r], v[\\r]\n global_load_lds_dwordx4 %[w2], s[96:97]\n" \
    ".endif\n .endm\n" \
      \
    ".macro TILE C, N, o1, o0, kt, qk, prio, d\n" \
    ".if %[fair]\n s_setprio \\prio\n .endif\n" \
    "EXPS \\C, \\C+2\n" \
    "v_smfmac_f32_32x32x32_bf16 v[144:159], v[180:183], v[\\N+24:\\N+31], v184\n" \
    "VRD 180, v189, \\o1\n" \
    "EXPS \\C+2, \\C+5\n" \
    ".if \\qk\n s_waitcnt lgkmcnt(3)\n" \
    "v_mfma_f32_32x32x4_2b_bf16 v[\\N:\\N+31], v[174:175], v[168:169], 0 cbsz:1\n .endif\n" \
    ".if \\kt >= 0\n ds_read_b32 v174, v187 offset:%[sq]*18432+(\\kt)*128\n .endif\n" \
    "EXPS \\C+5, \\C+8\n DEXP \\d, \\C+8\n EXPS \\C+9, \\C+13\n" \
    ".if \\kt >= 0\n s_waitcnt lgkmcnt(2)\n .else\n s_waitcnt lgkmcnt(1)\n .endif\n" \
    "v_smfmac_f32_32x32x32_bf16 v[128:143], v[176:179], v[\\C:\\C+7], v184\n" \
    "EXPS \\C+13, \\C+18\n" \
    ".if \\kt >= 0\n s_waitcnt lgkmcnt(1)\n .else\n s_waitcnt lgkmcnt(0)\n .endif\n" \
    "v_smfmac_f32_32x32x32_bf16 v[128:143], v[180:183], v[\\C+8:\\C+15], v184\n" \
    "EXPS \\C+18, \\C+19\n DEXP \\d+1, \\C+19\n EXPS \\C+20, \\C+21\n" \
    ".if \\qk\n v_mfma_f32_32x32x16_bf16 v[\\N:\\N+15], v[170:173], v[160:163], v[\\N:\\N+15]\n .endif\n" \
    "EXPS \\C+21, \\C+25\n" \
    ".if \\qk\n v_mfma_f32_32x32x16_bf16 v[\\N+16:\\N+31], v[170:173], v[164:167], v[\\N+16:\\N+31]\n .endif\n" \
    ".if \\kt >= 0\n ds_read_b128 v[170:173], v186 offset:%[sq]*18432+(\\kt)*1024\n .endif\n" \
    "EXPS \\C+25, \\C+28\n" \
    "v_smfmac_f32_32x32x32_bf16 v[144:159], v[176:179], v[\\C+16:\\C+23], v184\n" \
    ".if \\o0 >= 0\n VRD 176, v188, \\o0\n .endif\n" \
    "EXPS \\C+28, \\C+30\n DEXP \\d+2, \\C+30\n EXPS \\C+31, \\C+32\n" \
    ".endm\n" \
    "s_mov_b32 s95, m0\n"
#define ATT_END "s_mov_b32 m0, s95\n .purgem EXPS\n .purgem VRD\n .purgem DEXP\n .purgem TILE\n"
typedef __attribute__((ext_vector_type(16))) unsigned u32x16;
typedef __attribute__((ext_vector_type(8))) unsigned u32x8;
struct Att {
    f32x32 S0, S1, O; u32x16 F; u32x8 q1; u32x2 q2; u32x4 ad; u32x2 vc;
    const char* kvsrc; unsigned m1, m2, m3, mr;
};
template <int J, int WM, int FAIR>
DI void att_block(Att& A, const char* Wo, unsigned w0, unsigned w1, unsigned w2) {
#define ATT_OPS : "+{v[64:95]}"(A.S0), "+{v[96:127]}"(A.S1), "+{v[128:159]}"(A.O), "+{v[170:185]}"(A.F) \
                : "{v[160:167]}"(A.q1), "{v[168:169]}"(A.q2), "{v[186:189]}"(A.ad), "{v[190:191]}"(A.vc), [vm] "s"(0x0003ffff0003ffffull), \
                  [sc] "s"(A.kvsrc + (J + 1) * CHUNK), [m1] "s"(A.m1), [m2] "s"(A.m2), [m3] "s"(A.m3), [mr] "s"(A.mr), "{s[96:97]}"(Wo), \
                  [w0] "v"(w0), [w1] "v"(w1), [w2] "v"(w2), [sp] "i"((J + 2) % 3), [sq] "i"(J % 3), [sn] "i"(J < 7 ? (J + 1) % 3 : -1), \
                  [wm] "i"(WM), [fair] "i"(FAIR) \
                : "memory", "scc", "s95", "s98", "s99"
    if constexpr (J == 0)
        asm volatile(ATT_MACROS
            "ds_read_b32 v174, v187\n ds_read_b128 v[170:173], v186\n"
            "s_waitcnt lgkmcnt(1)\n"
            "v_mfma_f32_32x32x4_2b_bf16 v[64:95], v[174:175], v[168:169], 0 cbsz:1\n"
            "ds_read_b32 v174, v187 offset:128\n"
            "s_waitcnt lgkmcnt(1)\n s_nop 15\n"
            "v_mfma_f32_32x32x16_bf16 v[64:79], v[170:173], v[160:163], v[64:79]\n"
            "v_mfma_f32_32x32x16_bf16 v[80:95], v[170:173], v[164:167], v[80:95]\n"
            "ds_read_b128 v[170:173], v186 offset:1024\n"
            "VRD 176, v188, 0\n"
            "s_nop 7\n"
            "TILE 64, 96, 0, 1152, 2, 1, 1, 0\n"
            "TILE 96, 64, 1152, 2304, 3, 1, 0, 9\n"
            ATT_END ATT_OPS);
    else if constexpr (J < 8)
        asm volatile(ATT_MACROS
            "TILE 64, 96, %[sp]*18432+2304, %[sp]*18432+3456, 0, 1, 2, 0\n"
            "TILE 96, 64, %[sp]*18432+3456, %[sq]*18432, 1, 1, 2, 3\n"
            "TILE 64, 96, %[sq]*18432, %[sq]*18432+1152, 2, 1, 1, 6\n"
            "TILE 96, 64, %[sq]*18432+1152, %[sq]*18432+2304, 3, 1, 0, 9\n"
            ATT_END ATT_OPS);
    else
        asm volatile(ATT_MACROS
            "TILE 64, 96, %[sp]*18432+2304, %[sp]*18432+3456, -1, 1, 0, 9\n"
            "TILE 96, 64, %[sp]*18432+3456, -1, -1, 0, 0, 9\n"
            "s_nop 1\n"
            "v_smfmac_f32_32x32x32_bf16 v[144:159], v[180:183], v[120:127], v184\n"
            "s_nop 15\n"
            ATT_END ATT_OPS);
}

DI void store_o(rsrc_t rs, int br, const f32x16& o, float eps, int h, int qt, int lane) {
    const int hh = lane >> 5, rho = lane & 31;
    auto dn = __builtin_amdgcn_permlane32_swap(__float_as_uint(o[10]), __float_as_uint(o[10]), false, false);
    const float sc = __builtin_amdgcn_rcpf(__uint_as_float(dn[0]) + eps);
    const int tok = (((rho >> 2) & 1) << 4) + ((rho >> 3) << 2) + (rho & 3);
    const unsigned base = OFF_O + (br * 128 + 2 * qt + (tok >> 4)) * 16384 + (tok & 15) * 16;
    const u32x4 v = {pk(o[0] * sc, o[1] * sc), pk(o[2] * sc, o[3] * sc), pk(o[4] * sc, o[5] * sc), pk(o[6] * sc, o[7] * sc)};
    st128(rs, base + (h >> 1) * 1024 + (2 * (h & 1) + hh) * 256, v);
    st32(rs, hh ? WS_USED : OFF_OR + ((br * 64 + qt) * 32 + h) * 128 + tok * 4, pk(o[8] * sc, o[9] * sc));
}

struct W3Src { unsigned om, orm, ob; };
DI W3Src w3src(int cb, int lane, int w) {
    const unsigned j = lane >> 3, colb = cb * 144 + ((((lane >> 1) & 3) ^ (((lane >> 4) & 1) + 2 * (w & 1))) << 5) + ((lane & 1) << 4);
    const unsigned hq = lane >> 4, jj = lane & 7;
    W3Src r;
    r.om = ((j & 3) + 8 * (j >> 2) + 4 * (w & 1) + 18 * (w >> 1) + (w & 2)) * 1152 + colb;
    r.orm = (72 * w + 18 * (j >> 1) + ((j & 2) ? 0 : 16) + (j & 1)) * 1152 + colb;
    r.ob = (72 * w + 18 * hq + 2 * (hq & 1) + (jj & 3) + 8 * (jj >> 2) + 4 * ((lane >> 3) & 1)) * 1152 + cb * 144 + 128;
    return r;
}
DI void w3_last_piece(const char* Wo, int cb, int lane) {
    const unsigned hr = lane >> 1;
    glds16((18 * hr + ((hr & 1) ? 0 : 16) + (lane & 1)) * 1152 + cb * 144 + 128, Wo, L_W3B + 8192);
}
DI void load_chunk(const char* kvsrc, int c, int slot, int lane, int lw) {
#pragma unroll
    for (int i = 0; i < 4; i++) glds16_dev(lane * 16, kvsrc + c * CHUNK + (lw + 4 * i) * 1024, slot * CHUNK + (lw + 4 * i) * 1024);
    if (lw < 2) glds16_dev(lane * 16, kvsrc + c * CHUNK + 16384 + lw * 1024, slot * CHUNK + 16384 + lw * 1024);
}
struct W3Addr { unsigned a[4], b, b8; };
DI bf16x8 w3frag(const W3Addr& A, int i, int t) {
    return t < 4 ? trfrag(A.a[t] + i * 4096, A.a[t] + i * 4096 + 512) : i < 8 ? trfrag(A.b + i * 512, A.b + i * 512 + 64) : trfrag(A.b8, A.b8 + 64);
}
DI W3Addr w3addr(int kh, int lane) {
    const int g = lane >> 4, m = lane & 15;
    W3Addr A;
    const unsigned base = L_W3 + kh * 9 * 4096 + g * 1024 + (m >> 2) * 128 + 8 * (m & 3), key = ((m >> 3) + 2 * (g & 1)) << 5;
#pragma unroll
    for (int t = 0; t < 4; t++) { A.a[t] = base + ((t * 32) ^ key); OPAQUE(A.a[t]); }
    A.b = L_W3B + kh * 4096 + (8 * g + (m >> 2)) * 16 + 8 * (m & 1); OPAQUE(A.b);
    A.b8 = A.b + 8192 - kh * 3584;
    return A;
}
DI void load_af(u32x4 (&af)[9], rsrc_t rs, int br, int rtile, int kh, int lane) {
    const unsigned a = OFF_O + (br * 128 + rtile) * 16384 + kh * 8192 + lane * 16;
    const unsigned o = OFF_OR + ((br * 64 + (rtile >> 1)) * 32 + 16 * kh + 4 * (lane >> 4)) * 128 + (16 * (rtile & 1) + (lane & 15)) * 4;
#pragma unroll
    for (int i = 0; i < 8; i++) af[i] = ld128(rs, a + i * 1024);
#pragma unroll
    for (int j = 0; j < 4; j++) af[8][j] = ld32(rs, o + j * 128);
}
DI void gemm3(f32x4 (&acc)[5], const u32x4 (&af)[9], int kh, int lane) {
    __builtin_amdgcn_s_setprio(3);
    const W3Addr A = w3addr(kh, lane);
    bf16x8 wf[2][5];
#pragma unroll
    for (int t = 0; t < 5; t++) { wf[0][t] = w3frag(A, 0, t); acc[t] = (f32x4){0, 0, 0, 0}; }
    __builtin_amdgcn_sched_barrier(0);
#pragma unroll
    for (int i = 0; i < 9; i++) {
        if (i < 8) {
#pragma unroll
            for (int t = 0; t < 5; t++) wf[(i + 1) & 1][t] = w3frag(A, i + 1, t);
        }
#pragma unroll
        for (int t = 0; t < 5; t++) acc[t] = mfma16(wf[i & 1][t], __builtin_bit_cast(bf16x8, af[i]), acc[t]);
#pragma unroll
        for (int t = 0; t < 5; t++) { __builtin_amdgcn_sched_group_barrier(0x100, 2, 0); __builtin_amdgcn_sched_group_barrier(0x8, 1, 0); }
        __builtin_amdgcn_sched_barrier(0);
    }
    __builtin_amdgcn_s_setprio(0);
}

__global__ void __launch_bounds__(512, 2) fused(const char* X, const char* WqA, const char* WqB, const char* WoA, const char* WoB,
                                                 const unsigned* gw, char* ws, char* out) {
    const int tid = threadIdx.x, lane = tid & 63, w = __builtin_amdgcn_readfirstlane(tid >> 6);
    const int b = blockIdx.x, xcd = b & 7, idx = b >> 3;
    const rsrc_t rs = __builtin_amdgcn_make_buffer_rsrc(ws, 0, WS_USED, 0x00020000);
    STAMP_DECL;
    STAMP(0);
    u64 pc0; asm volatile("s_getpc_b64 %0" : "=s"(pc0));
    const u64 epoch = __hip_atomic_load((gu64*)(size_t)(ws + OFF_EPOCH), RLX_AGENT);
    const int h = xcd * 4 + (idx & 3), qb = idx >> 2;
    const int rb = xcd * 4 + (idx >> 3), cb = idx & 7;
    const int qg = w & 3, kh = w >> 2, hh = lane >> 5, rho = lane & 31, g = lane >> 4, m = lane & 15;
    const bool young = kh;
    const char* kvsrc = ws + OFF_KV + (size_t)h * KV_HEAD;
    const int qt0 = 8 * qb + 2 * qg, qtf = qt0 + kh;
    const int br = idx & 1, quad = (xcd & 3) * 2 + ((idx >> 1) & 1), r = (xcd >> 2) * 8 + (idx >> 2);
    u64 tag;
    Att A;
    if (young) {
        const unsigned c2 = 2016 + quad * 144, c0 = br ? quad * 144 : (quad & 1) * 1152 + (quad >> 1) * 288, c1 = br ? c0 + 1008 : c0;
        const int rot = ((idx >> 2) + 2 * (idx & 3)) % 9;
        p1_loader(X, br ? WqB : WqA, c0, c1, c2, r, rot, lane, qg);
        STAMP(10);
        tag = make_tag(epoch);
        {
            const int row = 256 * qb + 8 * h + 2 * qg + hh;
            const unsigned* xr = (const unsigned*)(X + (size_t)row * 1152) + rho;
            float s = 0.f;
#pragma unroll
            for (int i = 0; i < 9; i++) s = __builtin_amdgcn_fdot2_f32_bf16(__builtin_bit_cast(bf16x2, xr[32 * i]), __builtin_bit_cast(bf16x2, gw[rho + 32 * i]), s, false);
            s = row_sum(s); s = swap16_add(s, s);
            if (rho == 0) st32(rs, OFF_GATE + row * 4, __float_as_uint(__builtin_amdgcn_rcpf(1.f + __expf(-s))));
        }
        STAMP(13);
        volatile LAS unsigned* go = lds<unsigned>(L_FLAG + 12);
        unsigned kmax;
        if (qg == 0) {
            const u64 f = wait_flags<63, 4>(ws + OFF_FA + (h >> 1) * 512, tag, ~tag, lane);
            kmax = swap32_umax(swap16_umax(row_umax((((unsigned)f >> (12 * (h & 1))) & 0xfffu) << 19))) | 1u;
            *go = kmax;
        } else {
            while ((kmax = *go) == 0) __builtin_amdgcn_s_sleep(1);
            asm volatile("" ::: "memory");
        }
        STAMP(14);
        const u32x4 q1a = ld128(rs, OFF_Q1 + h * 65536 + qt0 * 1024 + lane * 16), q1b = ld128(rs, OFF_Q1 + h * 65536 + qt0 * 1024 + 1024 + lane * 16);
        const unsigned q2r = ld32(rs, OFF_Q2 + h * 8192 + qt0 * 128 + lane * 4);
        load_chunk(kvsrc, 0, 0, lane, qg);
        const float n = __builtin_amdgcn_fdot2_f32_bf16(__builtin_bit_cast(bf16x2, q2r), __builtin_bit_cast(bf16x2, q2r), swap32_add(nrm8(q1a), nrm8(q1b)), false);
        const float mneg = -1.01f * __builtin_amdgcn_sqrtf(n * __uint_as_float(kmax));
        A.q2 = (u32x2){q2r, pk(mneg, 0.f)};
        A.q1 = __builtin_shufflevector(q1a, q1b, 0, 1, 2, 3, 4, 5, 6, 7);
        lds<u32x4>(L_EX)[qg * 128 + lane] = q1a; lds<u32x4>(L_EX)[qg * 128 + 64 + lane] = q1b; lds<u32x2>(L_EX + 8192)[qg * 64 + lane] = A.q2;
    } else {
        unsigned pf = 0;
        asm volatile("global_load_dword %0, %1, %2" : "+v"(pf) : "v"((unsigned)(((idx & 3) * 70 + (tid < 70 ? tid : 0)) * 128)), "s"(pc0) : "memory");
        unsigned pg = 0;
        {
            constexpr unsigned SH = 16, NW = (WS_USED >> SH), NO = (2359296u >> SH), NWO = (663552u >> SH);
            const unsigned n = SH == 12 ? idx * 256 + tid : (idx & 1) * 256 + tid;
            const char* p = n < NW ? ws + ((size_t)n << SH) : n < NW + NO ? out + ((size_t)(n - NW) << SH)
                          : n < NW + NO + NWO ? WoA + ((size_t)(n - NW - NO) << SH) : WoB + ((size_t)(n - NW - NO - NWO) << SH);
            if (n < NW + NO + 2 * NWO) asm volatile("global_load_dword %0, %1, off sc1" : "+v"(pg) : "v"(p) : "memory");
        }
        if (tid < 4) lds<unsigned>(L_FLAG)[tid] = 0;
        const unsigned pay = br ? p1_consumer<2>(ws, rs, quad, r, lane, w STAMP_PASS)
                                : (quad & 1) ? p1_consumer<1>(ws, rs, quad, r, lane, w STAMP_PASS) : p1_consumer<0>(ws, rs, quad, r, lane, w STAMP_PASS);
        STAMP(12);
        tag = make_tag(epoch);
        const unsigned fo = br ? OFF_F1 + ((quad * 2 + (w >> 1)) * 32 + 2 * r + (w & 1)) * 8
                               : OFF_FA + ((4 * (quad >> 1) + (w >> 1) + 2 * lane) * 64 + (quad & 1) * 32 + 2 * r + (w & 1)) * 8;
        WAITV();
        asm volatile("" :: "v"(pf), "v"(pg));
        if (lane < 2 - br) __hip_atomic_store((gu64*)(size_t)(ws + fo), tag | pay, RLX_AGENT);
        STAMP(13);
    }
    {
        const unsigned vinit = rho == 18 ? ONES : 0u;
        asm volatile(".set r_, 120\n .rept 20\n v_mov_b64 v[r_:r_+1], 0\n .set r_, r_+2\n .endr\n"
                     ".set r_, 176\n .rept 8\n v_mov_b32 v[r_], %4\n .set r_, r_+1\n .endr\n"
                     "v_mov_b32 v175, 0x3f803f80\n v_mov_b32 v184, 0xdddddddd\n"
                     : "={v[64:95]}"(A.S0), "={v[96:127]}"(A.S1), "={v[128:159]}"(A.O), "=&{v[170:185]}"(A.F) : "{v192}"(vinit));
    }
    const unsigned bK = kh * 4096 + lane * 16, bK2 = 8192 + kh * 512 + rho * 4, bV = 9216 + kh * 4608 + (rho < 16 ? hh * 256 + rho * 16 : 768 + hh * 32 + rho * 16);
    const unsigned dV = rho < 8 ? 640 : rho < 16 ? 384 : 64;
    A.ad = (u32x4){bK, bK2, bV, bV + dV};
    A.kvsrc = kvsrc; A.m1 = w * 1024; A.m2 = w * 2048; A.m3 = 16384 + (w & 1) * 1024; A.mr = L_W3 + (32 + w + 8 * (w & 4)) * 1024;
    A.vc = (u32x2){lane * 16 + A.m2, lane * 16 + A.m3};
    asm volatile("" : "+{v[186:189]}"(A.ad), "+{v[190:191]}"(A.vc));
    W3Src ws3 = w3src(cb, lane, w);
    OPAQUE(ws3.om); OPAQUE(ws3.orm); OPAQUE(ws3.ob);
    STAMP(16);
    WAITV_BAR(0);
    STAMP(17);
    u64 seen;
    if (!young) {
        seen = peek_flags<31>(ws + OFF_F1 + (h >> 1) * 256, lane);
        const u32x4 q1a = lds<u32x4>(L_EX)[qg * 128 + lane], q1b = lds<u32x4>(L_EX)[qg * 128 + 64 + lane];
        A.q1 = __builtin_shufflevector(q1a, q1b, 0, 1, 2, 3, 4, 5, 6, 7); A.q2 = lds<u32x2>(L_EX + 8192)[qg * 64 + lane];
    }
    att_block<0, 0, 0>(A, WoB, 0, 0, 0);
    WAITV();
    u32x4 kvp[8];
    if (!young) {
        wait_flags<31, 4>(ws + OFF_F1 + (h >> 1) * 256, tag, seen, lane);
        const unsigned o = rho < 22 ? OFF_KVP + (h * 16 + hh) * 1408 + (qg * 22 + rho) * 16 : WS_USED;
#pragma unroll
        for (int i = 0; i < 8; i++) kvp[i] = ld128(rs, o + i * 2816);
        if (w == 0) w3_last_piece(WoB, cb, lane);
    }
    BAR();
    STAMP(18);
    if (young) {
        kvp[0] = ld128(rs, OFF_Q1 + (32 + h) * 65536 + qt0 * 1024 + lane * 16);
        kvp[1] = ld128(rs, OFF_Q1 + (32 + h) * 65536 + qt0 * 1024 + 1024 + lane * 16);
        kvp[2][0] = ld32(rs, OFF_Q2 + (32 + h) * 8192 + qt0 * 128 + lane * 4);
    }
    att_block<1, 1, 0>(A, WoB, ws3.om, 0, 0);
    WAITV();
    if (!young) {
        f32x4 s4 = __builtin_bit_cast(f32x4, kvp[0]);
#pragma unroll
        for (int i = 1; i < 8; i++) s4 += __builtin_bit_cast(f32x4, kvp[i]);
        const float lo = swap32_add(s4[0], s4[2]), hi = swap32_add(s4[1], s4[3]);
        const int u = qg * 22 + rho;
        if (rho < 22) *lds<unsigned>(L_KVT + u * 8 + hh * 4 + (u < 76 ? 0 : 32)) = pk(lo, hi);
        asm volatile("s_waitcnt lgkmcnt(0)" ::: "memory");
    }
    BAR();
    STAMP(19);
    if (young) {
        __builtin_amdgcn_s_setprio(3);
        const int e = rho < 19 ? rho : 18;
        const bf16x8 a1 = ldsr128(L_KVT + e * 32 + hh * 16);
        const u32x2 a2 = {*lds<unsigned>(L_KVT + 640 + e * 4), 0u};
        const u32x2 b2 = {kvp[2][0], 0u};
        f32x32 o = {};
        o = __builtin_amdgcn_mfma_f32_32x32x4bf16_1k(__builtin_bit_cast(s16x4, a2), __builtin_bit_cast(s16x4, b2), o, 1, 0, 0);
        f32x16 oa = __builtin_shufflevector(o, o, 0, 1, 2, 3, 4, 5, 6, 7, 8, 9, 10, 11, 12, 13, 14, 15);
        f32x16 ob = __builtin_shufflevector(o, o, 16, 17, 18, 19, 20, 21, 22, 23, 24, 25, 26, 27, 28, 29, 30, 31);
        oa = __builtin_amdgcn_mfma_f32_32x32x16_bf16(a1, __builtin_bit_cast(bf16x8, kvp[0]), oa, 0, 0, 0);
        ob = __builtin_amdgcn_mfma_f32_32x32x16_bf16(a1, __builtin_bit_cast(bf16x8, kvp[1]), ob, 0, 0, 0);
        store_o(rs, 1, oa, 1e-6f, h, qt0, lane);
        store_o(rs, 1, ob, 1e-6f, h, qt0 + 1, lane);
        __builtin_amdgcn_s_setprio(0);
    }
    att_block<2, 2, 1>(A, WoB, ws3.om, ws3.orm, ws3.ob);
    WAITV();
    if (young && lane < 2) __hip_atomic_store((gu64*)(size_t)(ws + OFF_FB) + (qt0 + lane) * 32 + h, tag, RLX_AGENT);
    BAR();
    STAMP(20);
    const int rt = w & 3, qt3 = 2 * rb + (rt >> 1);
    att_block<3, 0, 0>(A, WoB, 0, 0, 0);
    WAITV();
    u32x4 af[9];
    float gate;
    if (!young) {
        wait_flags<31, 4>(ws + OFF_FB + qt3 * 256, tag, ~tag, lane);
        load_af(af, rs, 1, rb * 4 + rt, kh, lane);
        gate = __uint_as_float(ld32(rs, OFF_GATE + (rb * 64 + rt * 16 + m) * 4));
        if (b == 0 && tid == 0) __hip_atomic_store((gu64*)(size_t)(ws + OFF_EPOCH), epoch + 1, RLX_AGENT);
    }
    BAR();
    STAMP(21);
    if (young) {
        load_af(af, rs, 1, rb * 4 + rt, kh, lane);
        gate = __uint_as_float(ld32(rs, OFF_GATE + (rb * 64 + rt * 16 + m) * 4));
    }
    att_block<4, 0, 0>(A, WoB, 0, 0, 0);
    WAITV();
    f32x4 accb[5];
    if (!young) gemm3(accb, af, kh, lane);
    BAR();
    STAMP(22);
    if (young) gemm3(accb, af, kh, lane);
    att_block<5, 0, 1>(A, WoB, 0, 0, 0);
    WAITV();
    BAR();
    STAMP(23);
    ws3 = w3src(cb, lane, w);
    if (w == 4) w3_last_piece(WoA, cb, lane);
    att_block<6, 1, 1>(A, WoA, ws3.om, 0, 0);
    asm volatile("s_waitcnt vmcnt(5)\n\ts_barrier" ::: "memory");
    STAMP(24);
    att_block<7, 2, 1>(A, WoA, ws3.om, ws3.orm, ws3.ob);
    att_block<8, 0, 0>(A, WoA, 0, 0, 0);
    STAMP(27);
    f32x16 Oa = __builtin_shufflevector(A.O, A.O, 0, 1, 2, 3, 4, 5, 6, 7, 8, 9, 10, 11, 12, 13, 14, 15);
    f32x16 Ob = __builtin_shufflevector(A.O, A.O, 16, 17, 18, 19, 20, 21, 22, 23, 24, 25, 26, 27, 28, 29, 30, 31);
    LAS f32x4* ex = lds<f32x4>(L_EX);
    {
        const float thr = 9.094947e-13f;
        const bool bad = hh == 0 && !(Oa[10] >= thr && Oa[10] < 1e30f && Ob[10] >= thr && Ob[10] < 1e30f);
        if (__any(bad) && lane == 0) *(volatile LAS unsigned*)lds<unsigned>(L_FLAG) = 1;
#pragma unroll
        for (int i = 0; i < 3; i++) ex[(w * 3 + i) * 64 + lane] = kh ? sub4(Oa, i) : sub4(Ob, i);
        WAITV_BAR(0);
        if (*(volatile LAS unsigned*)lds<unsigned>(L_FLAG)) {
            const u32x2 b2 = {A.q2[0], 0u};
            const u32x4 q1a = __builtin_shufflevector(A.q1, A.q1, 0, 1, 2, 3), q1b = __builtin_shufflevector(A.q1, A.q1, 4, 5, 6, 7);
            float xa = -INFINITY, xb = -INFINITY;
            for (int pass = 0; pass < 2; pass++) {
                for (int c = 0; c < 8; c++) {
                    LBAR();
                    if (young) load_chunk(kvsrc, c, 0, lane, qg);
                    WAITV_BAR(0);
                    for (int t = 0; t < 4; t++) {
                        const bf16x8 kf = ldsr128(bK + t * 1024);
                        const u32x2 k2 = {*lds<unsigned>(bK2 + t * 128), 0u};
                        f32x32 s = {};
                        s = __builtin_amdgcn_mfma_f32_32x32x4bf16_1k(__builtin_bit_cast(s16x4, k2), __builtin_bit_cast(s16x4, b2), s, 1, 0, 0);
                        f32x16 sa = __builtin_shufflevector(s, s, 0, 1, 2, 3, 4, 5, 6, 7, 8, 9, 10, 11, 12, 13, 14, 15);
                        f32x16 sb = __builtin_shufflevector(s, s, 16, 17, 18, 19, 20, 21, 22, 23, 24, 25, 26, 27, 28, 29, 30, 31);
                        sa = __builtin_amdgcn_mfma_f32_32x32x16_bf16(kf, __builtin_bit_cast(bf16x8, q1a), sa, 0, 0, 0);
                        sb = __builtin_amdgcn_mfma_f32_32x32x16_bf16(kf, __builtin_bit_cast(bf16x8, q1b), sb, 0, 0, 0);
                        if (pass == 0) {
#pragma unroll
                            for (int i = 0; i < 16; i++) { xa = fmaxf(xa, sa[i]); xb = fmaxf(xb, sb[i]); }
                        } else {
#pragma unroll
                            for (int i = 0; i < 16; i++) { sa[i] = __builtin_amdgcn_exp2f(sa[i] - xa); sb[i] = __builtin_amdgcn_exp2f(sb[i] - xb); }
#pragma unroll
                            for (int st = 0; st < 2; st++) {
                                u32x4 vf = {ONES, ONES, ONES, ONES};
                                if (rho < 18) vf = __builtin_bit_cast(u32x4, ldsr128(bV + t * 1152 + st * dV));
                                f32x8 pa, pb;
#pragma unroll
                                for (int i = 0; i < 8; i++) { pa[i] = sa[8 * st + i]; pb[i] = sb[8 * st + i]; }
                                Oa = __builtin_amdgcn_smfmac_f32_32x32x32_bf16(__builtin_bit_cast(bf16x8, vf), __builtin_bit_cast(bf16x16, pa), Oa, (int)0xdddddddd, 0, 0);
                                Ob = __builtin_amdgcn_smfmac_f32_32x32x32_bf16(__builtin_bit_cast(bf16x8, vf), __builtin_bit_cast(bf16x16, pb), Ob, (int)0xdddddddd, 0, 0);
                            }
                        }
                    }
                }
                if (pass == 0) {
                    xa = swap32_max(xa); xb = swap32_max(xb);
                    LAS float* mx = lds<float>(L_EX);
                    mx[w * 64 + lane] = hh ? xb : xa;
                    LBAR();
                    xa = fmaxf(xa, mx[(w ^ 4) * 64 + rho]); xb = fmaxf(xb, mx[(w ^ 4) * 64 + 32 + rho]);
#pragma unroll
                    for (int i = 0; i < 16; i++) { Oa[i] = 0.f; Ob[i] = 0.f; }
                }
            }
#pragma unroll
            for (int i = 0; i < 3; i++) ex[(w * 3 + i) * 64 + lane] = kh ? sub4(Oa, i) : sub4(Ob, i);
            LBAR();
        }
    }
    {
        f32x16 mine = kh ? Ob : Oa;
#pragma unroll
        for (int i = 0; i < 3; i++) {
            const f32x4 v = ex[((w ^ 4) * 3 + i) * 64 + lane];
#pragma unroll
            for (int k = 0; k < 4; k++) mine[4 * i + k] += v[k];
        }
        store_o(rs, 0, mine, 0.f, h, qtf, lane);
    }
    STAMP(28);
    const W3Addr WA = w3addr(kh, lane);
    bf16x8 wf[7][5];
#pragma unroll
    for (int i = 0; i < 3; i++)
#pragma unroll
        for (int t = 0; t < 5; t++) wf[i][t] = w3frag(WA, i, t);
    post_flag(ws + OFF_F2 + (qtf * 32 + h) * 8, tag, lane);
    STAMP(29);
#pragma unroll
    for (int i = 3; i < 7; i++)
#pragma unroll
        for (int t = 0; t < 5; t++) wf[i][t] = w3frag(WA, i, t);
    wait_flags<15, 4>(ws + OFF_F2 + qt3 * 256 + kh * 128, tag, ~tag, lane);
    STAMP(30);
    load_af(af, rs, 0, rb * 4 + rt, kh, lane);
    __builtin_amdgcn_sched_barrier(0);
    f32x4 acc[5];
#pragma unroll
    for (int i = 0; i < 9; i++) {
#pragma unroll
        for (int t = 0; t < 5; t++) acc[t] = mfma16(wf[i % 7][t], __builtin_bit_cast(bf16x8, af[i]), i ? acc[t] : (f32x4){0, 0, 0, 0});
        if (i < 2) {
#pragma unroll
            for (int t = 0; t < 5; t++) wf[i][t] = w3frag(WA, 7 + i, t);
        }
        __builtin_amdgcn_sched_barrier(0);
    }
    STAMP(31);
    LAS f32x4* red = lds<f32x4>(0);
#pragma unroll
    for (int t = 0; t < 5; t++) acc[t] = acc[t] * gate + accb[t] * (1.f - gate);
#pragma unroll
    for (int t = 0; t < 5; t++) if ((t < 3) == (kh == 1)) red[(rt * 5 + t) * 64 + lane] = acc[t];
    LBAR();
    char* orow = out + (size_t)(rb * 64 + rt * 16 + m) * 1152 + cb * 144 + g * 8;
#pragma unroll
    for (int t = 0; t < 5; t++)
        if ((t < 3) == (kh == 0)) {
            const f32x4 v = acc[t] + red[(rt * 5 + t) * 64 + lane];
            if (t < 4 || g < 2) *(u32x2*)(orow + t * 32) = (u32x2){pk(v[0], v[1]), pk(v[2], v[3])};
        }
    STAMP(32);
    STAMP_OUT;
}

extern "C" void kernel_launch(void* const* d_in, const int* in_sizes, int n_in, void* d_out, int out_size, void* d_ws, size_t ws_size,
                              hipStream_t stream) {
    (void)hipFuncSetAttribute((const void*)fused, hipFuncAttributeMaxDynamicSharedMemorySize, LDS_BYTES);
    fused<<<256, 512, LDS_BYTES, stream>>>((const char*)d_in[0], (const char*)d_in[1], (const char*)d_in[4], (const char*)d_in[2],
                                          (const char*)d_in[5], (const unsigned*)d_in[6], (char*)d_ws, (char*)d_out);
}
```

```cpp
#include <hip/hip_runtime.h>
#include <stdint.h>

typedef __attribute__((ext_vector_type(8))) __bf16 bf16x8;
typedef __attribute__((ext_vector_type(16))) __bf16 bf16x16;
typedef __attribute__((ext_vector_type(2))) __bf16 bf16x2;
typedef __attribute__((ext_vector_type(4))) short s16x4;
typedef __attribute__((ext_vector_type(2))) float f32x2;
typedef __attribute__((ext_vector_type(4))) float f32x4;
typedef __attribute__((ext_vector_type(8))) float f32x8;
typedef __attribute__((ext_vector_type(16))) float f32x16;
typedef __attribute__((ext_vector_type(32))) float f32x32;
typedef __attribute__((ext_vector_type(4))) unsigned u32x4;
typedef __attribute__((ext_vector_type(2))) unsigned u32x2;
typedef __attribute__((ext_vector_type(8))) unsigned u32x8;
typedef unsigned long long u64;
typedef __attribute__((address_space(1))) u64 gu64;
#define LAS __attribute__((address_space(3)))
#define DI __device__ __forceinline__
#define RLX_AGENT __ATOMIC_RELAXED, __HIP_MEMORY_SCOPE_AGENT

constexpr unsigned CHUNK = 18432;
constexpr unsigned KV_HEAD = 8 * CHUNK;
constexpr unsigned OFF_EPOCH = 0;
constexpr unsigned OFF_FA = 256;
constexpr unsigned OFF_F1 = OFF_FA + 8192;
constexpr unsigned OFF_FB = OFF_F1 + 4096;
constexpr unsigned OFF_GATE = OFF_FB + 16384;
constexpr unsigned OFF_KVP = OFF_GATE + 8192;
constexpr unsigned OFF_Q2 = OFF_KVP + 32 * 16 * 1408;
constexpr unsigned OFF_Q1 = OFF_Q2 + 2 * 32 * 8192;
constexpr unsigned OFF_KV = OFF_Q1 + 2 * 32 * 65536;
constexpr unsigned OFF_O = OFF_KV + 32 * KV_HEAD;
constexpr unsigned OFF_OR = OFF_O + 2 * 128 * 16384;
constexpr unsigned WS_USED = OFF_OR + 2 * 64 * 4096;

constexpr unsigned ST1 = 44032;
constexpr unsigned L_RED1 = 57344;
constexpr unsigned L_W3 = 3 * CHUNK;
constexpr unsigned L_EX = L_W3 + 82944;
constexpr unsigned L_EX3 = L_EX + 18432;
constexpr unsigned L_KVT = L_EX + 24576;
constexpr unsigned L_FLAG = L_KVT + 768;
constexpr unsigned LDS_BYTES = 163840;
static_assert(L_FLAG + 16 == 163600, "the generated streams know this address");

constexpr float QSCALE = 0.23570226039551584f * 1.4426950408889634f;
#define CODE_BYTES 44448u
constexpr unsigned ONES = 0x3f803f80u, UNSET = 0xffffffffu;

DI unsigned pk(float a, float b) { bf16x2 v = __builtin_convertvector((f32x2){a, b}, bf16x2); return __builtin_bit_cast(unsigned, v); }
DI f32x4 phi4(f32x4 a) {
    const f32x4 s = a * 1.4426950408889634f;
    f32x4 e, x;
#pragma unroll
    for (int i = 0; i < 4; i++) {
        e[i] = __builtin_amdgcn_fmed3f(__builtin_amdgcn_exp2f(s[i]), 0.f, 1.f);
        x[i] = __builtin_amdgcn_fmed3f(a[i], 0.f, 3.4028235e38f);
    }
    return (x + 1e-6f) + e;
}
DI void glds16(unsigned voff, const void* sbase, unsigned lds_dst) {
    unsigned keep;
    asm volatile("s_mov_b32 %0, m0\n\ts_mov_b32 m0, %3\n\ts_nop 0\n\tglobal_load_lds_dwordx4 %1, %2\n\ts_mov_b32 m0, %0"
                 : "=&s"(keep) : "v"(voff), "s"(sbase), "s"(lds_dst) : "memory");
}
DI void glds16_dev(unsigned voff, const void* sbase, unsigned lds_dst) {
    unsigned keep;
    asm volatile("s_mov_b32 %0, m0\n\ts_mov_b32 m0, %3\n\ts_nop 0\n\tglobal_load_lds_dwordx4 %1, %2 sc1\n\ts_mov_b32 m0, %0"
                 : "=&s"(keep) : "v"(voff), "s"(sbase), "s"(lds_dst) : "memory");
}
#define WAITV_BAR(n) asm volatile("s_waitcnt vmcnt(" #n ") lgkmcnt(0)\n\ts_barrier" ::: "memory")
#define LBAR() asm volatile("s_waitcnt lgkmcnt(0)\n\ts_barrier" ::: "memory")
#define WAITV() do { asm volatile("s_waitcnt vmcnt(0)" ::: "memory"); __builtin_amdgcn_s_waitcnt(0x0F70); } while (0)
#define OPAQUE(x) asm volatile("" : "+v"(x))
DI int fresh_lane() { int l; asm volatile("v_mbcnt_lo_u32_b32 %0, -1, 0\n\tv_mbcnt_hi_u32_b32 %0, -1, %0" : "=v"(l)); return l; }
template <class T> DI LAS T* lds(unsigned a) { return (LAS T*)(size_t)a; }
DI bf16x8 ldsr128(unsigned a) { return *(const LAS bf16x8*)(size_t)a; }
DI s16x4 trr(unsigned a) { return __builtin_amdgcn_ds_read_tr16_b64_v4i16((LAS s16x4*)(size_t)a); }
DI bf16x8 trfrag(unsigned a, unsigned b) {
    s16x4 lo = trr(a), hi = trr(b);
    return __builtin_bit_cast(bf16x8, __builtin_shufflevector(lo, hi, 0, 1, 2, 3, 4, 5, 6, 7));
}
DI f32x4 mfma16(bf16x8 a, bf16x8 b, f32x4 c) { return __builtin_amdgcn_mfma_f32_16x16x32_bf16(a, b, c, 0, 0, 0); }
template <int CTRL> DI float dpp(float v) { return __int_as_float(__builtin_amdgcn_mov_dpp(__float_as_int(v), CTRL, 0xf, 0xf, true)); }
DI float swap16_add(float a, float b) { auto r = __builtin_amdgcn_permlane16_swap(__float_as_uint(a), __float_as_uint(b), false, false); return __uint_as_float(r[0]) + __uint_as_float(r[1]); }
DI float swap32_add(float a, float b) { auto r = __builtin_amdgcn_permlane32_swap(__float_as_uint(a), __float_as_uint(b), false, false); return __uint_as_float(r[0]) + __uint_as_float(r[1]); }
DI unsigned umax(unsigned a, unsigned b) { return a > b ? a : b; }
template <int CTRL> DI unsigned dppu(unsigned v) { return __builtin_amdgcn_mov_dpp(v, CTRL, 0xf, 0xf, true); }
DI unsigned swap16_umax(unsigned a) { auto r = __builtin_amdgcn_permlane16_swap(a, a, false, false); return umax(r[0], r[1]); }
DI unsigned swap32_umax(unsigned a) { auto r = __builtin_amdgcn_permlane32_swap(a, a, false, false); return umax(r[0], r[1]); }
DI unsigned row_umax(unsigned x) { x = umax(x, dppu<0xB1>(x)); x = umax(x, dppu<0x4E>(x)); x = umax(x, dppu<0x141>(x)); x = umax(x, dppu<0x140>(x)); return x; }
DI float swap32_max(float a) { auto r = __builtin_amdgcn_permlane32_swap(__float_as_uint(a), __float_as_uint(a), false, false); return fmaxf(__uint_as_float(r[0]), __uint_as_float(r[1])); }
DI float row_sum(float x) {
    x += dpp<0xB1>(x); x += dpp<0x4E>(x); x += dpp<0x141>(x); x += dpp<0x140>(x); return x; }

typedef __amdgpu_buffer_rsrc_t rsrc_t;
DI void st128(rsrc_t r, unsigned off, u32x4 v) { __builtin_amdgcn_raw_buffer_store_b128(v, r, off, 0, 16); }
DI void st64(rsrc_t r, unsigned off, u32x2 v) { __builtin_amdgcn_raw_buffer_store_b64(v, r, off, 0, 16); }
DI void st32(rsrc_t r, unsigned off, unsigned v) { __builtin_amdgcn_raw_buffer_store_b32(v, r, off, 0, 16); }
DI u32x4 ld128(rsrc_t r, unsigned off) { return __builtin_amdgcn_raw_buffer_load_b128(r, off, 0, 16); }
DI unsigned ld32(rsrc_t r, unsigned off) { return __builtin_amdgcn_raw_buffer_load_b32(r, off, 0, 16); }

template <int MASK, int SLEEP>
DI u64 wait_flags(const char* words, u64 tag, u64 seen, int lane) {
    gu64* p = (gu64*)(size_t)words + (lane & MASK);
    if (!__all((seen ^ tag) < 0x1000000ull)) {
        const u64 t0 = __builtin_amdgcn_s_memrealtime();
        for (;;) {
            seen = __hip_atomic_load(p, RLX_AGENT);
            if (__all((seen ^ tag) < 0x1000000ull)) break;
            __builtin_amdgcn_s_sleep(SLEEP);
            if (__builtin_amdgcn_s_memrealtime() - t0 > 10000000ull) break;
        }
    }
    asm volatile("" ::: "memory");
    return seen;
}
DI void post_flag(char* word, u64 val, int lane) {
    WAITV();
    if (lane == 0) __hip_atomic_store((gu64*)(size_t)word, val, RLX_AGENT);
}
DI u64 read_epoch() {
    const u32x2 e = *(volatile LAS u32x2*)lds<u32x2>(L_FLAG + 32);
    return (u64)(unsigned)__builtin_amdgcn_readfirstlane(e[0]) | ((u64)(unsigned)__builtin_amdgcn_readfirstlane(e[1]) << 32);
}
DI u64 make_tag(u64 epoch) { return ((epoch + 1) * 0x9E3779B97F4A7C15ull) << 24; }

#define P1_PRO \
    "ds_read_b128 v[152:155], v212 offset:0\n" \
    "ds_read_b128 v[156:159], v212 offset:2048\n" \
    "ds_read_b128 v[160:163], v212 offset:4096\n" \
    "ds_read_b128 v[164:167], v212 offset:6144\n" \
    "ds_read_b64_tr_b16 v[184:185], v214 offset:0\n" \
    "ds_read_b64_tr_b16 v[186:187], v214 offset:432\n" \
    "ds_read_b64_tr_b16 v[188:189], v214 offset:40\n" \
    "ds_read_b64_tr_b16 v[190:191], v214 offset:472\n" \
    "ds_read_b64_tr_b16 v[192:193], v214 offset:144\n" \
    "ds_read_b64_tr_b16 v[194:195], v214 offset:576\n" \
    "ds_read_b64_tr_b16 v[196:197], v214 offset:184\n" \
    "ds_read_b64_tr_b16 v[198:199], v214 offset:616\n" \
    "ds_read_b64_tr_b16 v[200:201], v214 offset:288\n" \
    "ds_read_b64_tr_b16 v[202:203], v214 offset:720\n" \
    "ds_read_b64_tr_b16 v[204:205], v214 offset:328\n" \
    "ds_read_b64_tr_b16 v[206:207], v214 offset:760\n" \
    "ds_read_b64_tr_b16 v[208:209], v215 offset:0\n" \
    "ds_read_b64_tr_b16 v[210:211], v215 offset:432\n" \
    "s_waitcnt lgkmcnt(0)\n"
#define P1_BODY \
    "s_waitcnt lgkmcnt(12)\n" \
    "v_mfma_f32_16x16x32_bf16 v[40:43], v[184:187], v[152:155], v[40:43]\n" \
    "ds_read_b128 v[168:171], v213 offset:0\n" \
    "v_mfma_f32_16x16x32_bf16 v[44:47], v[184:187], v[156:159], v[44:47]\n" \
    "ds_read_b128 v[172:175], v213 offset:2048\n" \
    "v_mfma_f32_16x16x32_bf16 v[48:51], v[184:187], v[160:163], v[48:51]\n" \
    "ds_read_b128 v[176:179], v213 offset:4096\n" \
    "v_mfma_f32_16x16x32_bf16 v[52:55], v[184:187], v[164:167], v[52:55]\n" \
    "ds_read_b128 v[180:183], v213 offset:6144\n" \
    "s_waitcnt lgkmcnt(14)\n" \
    "v_mfma_f32_16x16x32_bf16 v[56:59], v[188:191], v[152:155], v[56:59]\n" \
    "ds_read_b64_tr_b16 v[184:185], v214 offset:13824\n" \
    "v_mfma_f32_16x16x32_bf16 v[60:63], v[188:191], v[156:159], v[60:63]\n" \
    "ds_read_b64_tr_b16 v[186:187], v214 offset:14256\n" \
    "v_mfma_f32_16x16x32_bf16 v[64:67], v[188:191], v[160:163], v[64:67]\n" \
    "v_mfma_f32_16x16x32_bf16 v[68:71], v[188:191], v[164:167], v[68:71]\n" \
    "s_waitcnt lgkmcnt(14)\n" \
    "MFK 72, 192, 152\n" \
    "ds_read_b64_tr_b16 v[188:189], v214 offset:13864\n" \
    "MFK 76, 192, 156\n" \
    "ds_read_b64_tr_b16 v[190:191], v214 offset:14296\n" \
    "MFK 80, 192, 160\n" \
    "MFK 84, 192, 164\n" \
    "s_waitcnt lgkmcnt(14)\n" \
    "MFK 88, 196, 152\n" \
    "ds_read_b64_tr_b16 v[192:193], v214 offset:13968\n" \
    "MFK 92, 196, 156\n" \
    "ds_read_b64_tr_b16 v[194:195], v214 offset:14400\n" \
    "MFK 96, 196, 160\n" \
    "MFK 100, 196, 164\n" \
    "s_waitcnt lgkmcnt(14)\n" \
    "v_mfma_f32_16x16x32_bf16 v[104:107], v[152:155], v[200:203], v[104:107]\n" \
    "ds_read_b64_tr_b16 v[196:197], v214 offset:14008\n" \
    "v_mfma_f32_16x16x32_bf16 v[108:111], v[156:159], v[200:203], v[108:111]\n" \
    "ds_read_b64_tr_b16 v[198:199], v214 offset:14440\n" \
    "v_mfma_f32_16x16x32_bf16 v[112:115], v[160:163], v[200:203], v[112:115]\n" \
    "v_mfma_f32_16x16x32_bf16 v[116:119], v[164:167], v[200:203], v[116:119]\n" \
    "s_waitcnt lgkmcnt(14)\n" \
    "v_mfma_f32_16x16x32_bf16 v[120:123], v[152:155], v[204:207], v[120:123]\n" \
    "ds_read_b64_tr_b16 v[200:201], v214 offset:14112\n" \
    "v_mfma_f32_16x16x32_bf16 v[124:127], v[156:159], v[204:207], v[124:127]\n" \
    "ds_read_b64_tr_b16 v[202:203], v214 offset:14544\n" \
    "v_mfma_f32_16x16x32_bf16 v[128:131], v[160:163], v[204:207], v[128:131]\n" \
    "v_mfma_f32_16x16x32_bf16 v[132:135], v[164:167], v[204:207], v[132:135]\n" \
    "s_waitcnt lgkmcnt(14)\n" \
    "v_mfma_f32_16x16x32_bf16 v[136:139], v[152:155], v[208:211], v[136:139]\n" \
    "ds_read_b64_tr_b16 v[204:205], v214 offset:14152\n" \
    "v_mfma_f32_16x16x32_bf16 v[140:143], v[156:159], v[208:211], v[140:143]\n" \
    "ds_read_b64_tr_b16 v[206:207], v214 offset:14584\n" \
    "v_mfma_f32_16x16x32_bf16 v[144:147], v[160:163], v[208:211], v[144:147]\n" \
    "v_mfma_f32_16x16x32_bf16 v[148:151], v[164:167], v[208:211], v[148:151]\n" \
    "s_waitcnt lgkmcnt(10)\n" \
    "v_mfma_f32_16x16x32_bf16 v[40:43], v[184:187], v[168:171], v[40:43]\n" \
    "ds_read_b64_tr_b16 v[208:209], v215 offset:13824\n" \
    "v_mfma_f32_16x16x32_bf16 v[44:47], v[184:187], v[172:175], v[44:47]\n" \
    "ds_read_b64_tr_b16 v[210:211], v215 offset:14256\n" \
    "v_mfma_f32_16x16x32_bf16 v[48:51], v[184:187], v[176:179], v[48:51]\n" \
    "v_mfma_f32_16x16x32_bf16 v[52:55], v[184:187], v[180:183], v[52:55]\n" \
    "s_waitcnt lgkmcnt(10)\n" \
    "v_mfma_f32_16x16x32_bf16 v[56:59], v[188:191], v[168:171], v[56:59]\n" \
    "s_cmp_eq_u32 %[p], 2\n" \
    "v_mfma_f32_16x16x32_bf16 v[60:63], v[188:191], v[172:175], v[60:63]\n" \
    "s_cselect_b32 %[d], %[neg], 44032\n" \
    "v_mfma_f32_16x16x32_bf16 v[64:67], v[188:191], v[176:179], v[64:67]\n" \
    "s_cselect_b32 %[p], -1, %[p]\n" \
    "v_mfma_f32_16x16x32_bf16 v[68:71], v[188:191], v[180:183], v[68:71]\n" \
    "s_add_u32 %[p], %[p], 1\n" \
    "s_waitcnt lgkmcnt(8)\n" \
    "MFK 72, 192, 168\n" \
    "s_waitcnt lgkmcnt(0)\n" \
    "s_barrier\n" \
    "v_add_u32 v212, %[d], v212\n" \
    "MFK 76, 192, 172\n" \
    "v_add_u32 v214, %[d], v214\n" \
    "ds_read_b128 v[152:155], v212 offset:0\n" \
    "MFK 80, 192, 176\n" \
    "v_add_u32 v213, %[d], v213\n" \
    "ds_read_b128 v[156:159], v212 offset:2048\n" \
    "MFK 84, 192, 180\n" \
    "v_add_u32 v215, %[d], v215\n" \
    "ds_read_b128 v[160:163], v212 offset:4096\n" \
    "MFK 88, 196, 168\n" \
    "ds_read_b128 v[164:167], v212 offset:6144\n" \
    "MFK 92, 196, 172\n" \
    "ds_read_b64_tr_b16 v[184:185], v214 offset:0\n" \
    "MFK 96, 196, 176\n" \
    "ds_read_b64_tr_b16 v[186:187], v214 offset:432\n" \
    "MFK 100, 196, 180\n" \
    "ds_read_b64_tr_b16 v[188:189], v214 offset:40\n" \
    "v_mfma_f32_16x16x32_bf16 v[104:107], v[168:171], v[200:203], v[104:107]\n" \
    "ds_read_b64_tr_b16 v[190:191], v214 offset:472\n" \
    "v_mfma_f32_16x16x32_bf16 v[108:111], v[172:175], v[200:203], v[108:111]\n" \
    "ds_read_b64_tr_b16 v[192:193], v214 offset:144\n" \
    "v_mfma_f32_16x16x32_bf16 v[112:115], v[176:179], v[200:203], v[112:115]\n" \
    "ds_read_b64_tr_b16 v[194:195], v214 offset:576\n" \
    "v_mfma_f32_16x16x32_bf16 v[116:119], v[180:183], v[200:203], v[116:119]\n" \
    "ds_read_b64_tr_b16 v[196:197], v214 offset:184\n" \
    "v_mfma_f32_16x16x32_bf16 v[120:123], v[168:171], v[204:207], v[120:123]\n" \
    "ds_read_b64_tr_b16 v[198:199], v214 offset:616\n" \
    "v_mfma_f32_16x16x32_bf16 v[124:127], v[172:175], v[204:207], v[124:127]\n" \
    "ds_read_b64_tr_b16 v[200:201], v214 offset:288\n" \
    "v_mfma_f32_16x16x32_bf16 v[128:131], v[176:179], v[204:207], v[128:131]\n" \
    "ds_read_b64_tr_b16 v[202:203], v214 offset:720\n" \
    "v_mfma_f32_16x16x32_bf16 v[132:135], v[180:183], v[204:207], v[132:135]\n" \
    "v_mfma_f32_16x16x32_bf16 v[136:139], v[168:171], v[208:211], v[136:139]\n" \
    "ds_read_b64_tr_b16 v[204:205], v214 offset:328\n" \
    "v_mfma_f32_16x16x32_bf16 v[140:143], v[172:175], v[208:211], v[140:143]\n" \
    "ds_read_b64_tr_b16 v[206:207], v214 offset:760\n" \
    "v_mfma_f32_16x16x32_bf16 v[144:147], v[176:179], v[208:211], v[144:147]\n" \
    "v_mfma_f32_16x16x32_bf16 v[148:151], v[180:183], v[208:211], v[148:151]\n" \
    "ds_read_b64_tr_b16 v[208:209], v215 offset:0\n" \
    "ds_read_b64_tr_b16 v[210:211], v215 offset:432\n"
#define P1_TAIL \
    "s_waitcnt lgkmcnt(12)\n" \
    "v_mfma_f32_16x16x32_bf16 v[40:43], v[184:187], v[152:155], v[40:43]\n" \
    "ds_read_b128 v[168:171], v213 offset:0\n" \
    "v_mfma_f32_16x16x32_bf16 v[44:47], v[184:187], v[156:159], v[44:47]\n" \
    "ds_read_b128 v[172:175], v213 offset:2048\n" \
    "v_mfma_f32_16x16x32_bf16 v[48:51], v[184:187], v[160:163], v[48:51]\n" \
    "ds_read_b128 v[176:179], v213 offset:4096\n" \
    "v_mfma_f32_16x16x32_bf16 v[52:55], v[184:187], v[164:167], v[52:55]\n" \
    "ds_read_b128 v[180:183], v213 offset:6144\n" \
    "s_waitcnt lgkmcnt(14)\n" \
    "v_mfma_f32_16x16x32_bf16 v[56:59], v[188:191], v[152:155], v[56:59]\n" \
    "ds_read_b64_tr_b16 v[184:185], v214 offset:13824\n" \
    "v_mfma_f32_16x16x32_bf16 v[60:63], v[188:191], v[156:159], v[60:63]\n" \
    "ds_read_b64_tr_b16 v[186:187], v214 offset:14256\n" \
    "v_mfma_f32_16x16x32_bf16 v[64:67], v[188:191], v[160:163], v[64:67]\n" \
    "v_mfma_f32_16x16x32_bf16 v[68:71], v[188:191], v[164:167], v[68:71]\n" \
    "s_waitcnt lgkmcnt(14)\n" \
    "MFK 72, 192, 152\n" \
    "ds_read_b64_tr_b16 v[188:189], v214 offset:13864\n" \
    "MFK 76, 192, 156\n" \
    "ds_read_b64_tr_b16 v[190:191], v214 offset:14296\n" \
    "MFK 80, 192, 160\n" \
    "MFK 84, 192, 164\n" \
    "s_waitcnt lgkmcnt(14)\n" \
    "MFK 88, 196, 152\n" \
    "ds_read_b64_tr_b16 v[192:193], v214 offset:13968\n" \
    "MFK 92, 196, 156\n" \
    "ds_read_b64_tr_b16 v[194:195], v214 offset:14400\n" \
    "MFK 96, 196, 160\n" \
    "MFK 100, 196, 164\n" \
    "s_waitcnt lgkmcnt(14)\n" \
    "v_mfma_f32_16x16x32_bf16 v[104:107], v[152:155], v[200:203], v[104:107]\n" \
    "ds_read_b64_tr_b16 v[196:197], v214 offset:14008\n" \
    "v_mfma_f32_16x16x32_bf16 v[108:111], v[156:159], v[200:203], v[108:111]\n" \
    "ds_read_b64_tr_b16 v[198:199], v214 offset:14440\n" \
    "v_mfma_f32_16x16x32_bf16 v[112:115], v[160:163], v[200:203], v[112:115]\n" \
    "v_mfma_f32_16x16x32_bf16 v[116:119], v[164:167], v[200:203], v[116:119]\n" \
    "s_waitcnt lgkmcnt(14)\n" \
    "v_mfma_f32_16x16x32_bf16 v[120:123], v[152:155], v[204:207], v[120:123]\n" \
    "ds_read_b64_tr_b16 v[200:201], v214 offset:14112\n" \
    "v_mfma_f32_16x16x32_bf16 v[124:127], v[156:159], v[204:207], v[124:127]\n" \
    "ds_read_b64_tr_b16 v[202:203], v214 offset:14544\n" \
    "v_mfma_f32_16x16x32_bf16 v[128:131], v[160:163], v[204:207], v[128:131]\n" \
    "v_mfma_f32_16x16x32_bf16 v[132:135], v[164:167], v[204:207], v[132:135]\n" \
    "s_waitcnt lgkmcnt(14)\n" \
    "v_mfma_f32_16x16x32_bf16 v[136:139], v[152:155], v[208:211], v[136:139]\n" \
    "ds_read_b64_tr_b16 v[204:205], v214 offset:14152\n" \
    "v_mfma_f32_16x16x32_bf16 v[140:143], v[156:159], v[208:211], v[140:143]\n" \
    "ds_read_b64_tr_b16 v[206:207], v214 offset:14584\n" \
    "v_mfma_f32_16x16x32_bf16 v[144:147], v[160:163], v[208:211], v[144:147]\n" \
    "v_mfma_f32_16x16x32_bf16 v[148:151], v[164:167], v[208:211], v[148:151]\n" \
    "s_waitcnt lgkmcnt(10)\n" \
    "v_mfma_f32_16x16x32_bf16 v[40:43], v[184:187], v[168:171], v[40:43]\n" \
    "ds_read_b64_tr_b16 v[208:209], v215 offset:13824\n" \
    "v_mfma_f32_16x16x32_bf16 v[44:47], v[184:187], v[172:175], v[44:47]\n" \
    "ds_read_b64_tr_b16 v[210:211], v215 offset:14256\n" \
    "v_mfma_f32_16x16x32_bf16 v[48:51], v[184:187], v[176:179], v[48:51]\n" \
    "v_mfma_f32_16x16x32_bf16 v[52:55], v[184:187], v[180:183], v[52:55]\n" \
    "s_waitcnt lgkmcnt(10)\n" \
    "v_mfma_f32_16x16x32_bf16 v[56:59], v[188:191], v[168:171], v[56:59]\n" \
    "v_mfma_f32_16x16x32_bf16 v[60:63], v[188:191], v[172:175], v[60:63]\n" \
    "v_mfma_f32_16x16x32_bf16 v[64:67], v[188:191], v[176:179], v[64:67]\n" \
    "v_mfma_f32_16x16x32_bf16 v[68:71], v[188:191], v[180:183], v[68:71]\n" \
    "s_waitcnt lgkmcnt(8)\n" \
    "MFK 72, 192, 168\n" \
    "MFK 76, 192, 172\n" \
    "MFK 80, 192, 176\n" \
    "MFK 84, 192, 180\n" \
    "s_waitcnt lgkmcnt(6)\n" \
    "MFK 88, 196, 168\n" \
    "MFK 92, 196, 172\n" \
    "MFK 96, 196, 176\n" \
    "MFK 100, 196, 180\n" \
    "s_waitcnt lgkmcnt(4)\n" \
    "v_mfma_f32_16x16x32_bf16 v[104:107], v[168:171], v[200:203], v[104:107]\n" \
    "v_mfma_f32_16x16x32_bf16 v[108:111], v[172:175], v[200:203], v[108:111]\n" \
    "v_mfma_f32_16x16x32_bf16 v[112:115], v[176:179], v[200:203], v[112:115]\n" \
    "v_mfma_f32_16x16x32_bf16 v[116:119], v[180:183], v[200:203], v[116:119]\n" \
    "s_waitcnt lgkmcnt(2)\n" \
    "v_mfma_f32_16x16x32_bf16 v[120:123], v[168:171], v[204:207], v[120:123]\n" \
    "v_mfma_f32_16x16x32_bf16 v[124:127], v[172:175], v[204:207], v[124:127]\n" \
    "v_mfma_f32_16x16x32_bf16 v[128:131], v[176:179], v[204:207], v[128:131]\n" \
    "v_mfma_f32_16x16x32_bf16 v[132:135], v[180:183], v[204:207], v[132:135]\n" \
    "s_waitcnt lgkmcnt(0)\n" \
    "v_mfma_f32_16x16x32_bf16 v[136:139], v[168:171], v[208:211], v[136:139]\n" \
    "v_mfma_f32_16x16x32_bf16 v[140:143], v[172:175], v[208:211], v[140:143]\n" \
    "v_mfma_f32_16x16x32_bf16 v[144:147], v[176:179], v[208:211], v[144:147]\n" \
    "v_mfma_f32_16x16x32_bf16 v[148:151], v[180:183], v[208:211], v[148:151]\n"
#define P1_TAIL_Q \
    "s_waitcnt lgkmcnt(12)\n" \
    "v_mfma_f32_16x16x32_bf16 v[40:43], v[184:187], v[152:155], v[40:43]\n" \
    "ds_read_b128 v[168:171], v213 offset:0\n" \
    "ds_read_b128 v[172:175], v213 offset:2048\n" \
    "v_mfma_f32_16x16x32_bf16 v[44:47], v[184:187], v[156:159], v[44:47]\n" \
    "ds_read_b128 v[176:179], v213 offset:4096\n" \
    "ds_read_b128 v[180:183], v213 offset:6144\n" \
    "v_mfma_f32_16x16x32_bf16 v[48:51], v[184:187], v[160:163], v[48:51]\n" \
    "ds_read_b64_tr_b16 v[216:217], v215 offset:13824\n" \
    "ds_read_b64_tr_b16 v[218:219], v215 offset:14256\n" \
    "v_mfma_f32_16x16x32_bf16 v[52:55], v[184:187], v[164:167], v[52:55]\n" \
    "ds_read_b64_tr_b16 v[220:221], v214 offset:13824\n" \
    "s_waitcnt lgkmcnt(7)\n" \
    "v_mfma_f32_16x16x32_bf16 v[136:139], v[152:155], v[208:211], v[136:139]\n" \
    "ds_read_b64_tr_b16 v[222:223], v214 offset:14256\n" \
    "v_mfma_f32_16x16x32_bf16 v[140:143], v[156:159], v[208:211], v[140:143]\n" \
    "v_mfma_f32_16x16x32_bf16 v[144:147], v[160:163], v[208:211], v[144:147]\n" \
    "v_mfma_f32_16x16x32_bf16 v[148:151], v[164:167], v[208:211], v[148:151]\n" \
    "s_waitcnt lgkmcnt(2)\n" \
    "v_mfma_f32_16x16x32_bf16 v[136:139], v[168:171], v[216:219], v[136:139]\n" \
    "v_mfma_f32_16x16x32_bf16 v[140:143], v[172:175], v[216:219], v[140:143]\n" \
    "v_mfma_f32_16x16x32_bf16 v[144:147], v[176:179], v[216:219], v[144:147]\n" \
    "v_mfma_f32_16x16x32_bf16 v[148:151], v[180:183], v[216:219], v[148:151]\n" \
    "s_waitcnt lgkmcnt(0)\n" \
    "v_mfma_f32_16x16x32_bf16 v[40:43], v[220:223], v[168:171], v[40:43]\n" \
    "ds_read_b64_tr_b16 v[216:217], v214 offset:13864\n" \
    "v_mfma_f32_16x16x32_bf16 v[44:47], v[220:223], v[172:175], v[44:47]\n" \
    "ds_read_b64_tr_b16 v[218:219], v214 offset:14296\n" \
    "v_pk_mul_f32 v[136:137], v[136:137], v[238:239]\n" \
    "v_pk_mul_f32 v[138:139], v[138:139], v[238:239]\n" \
    "v_mfma_f32_16x16x32_bf16 v[48:51], v[220:223], v[176:179], v[48:51]\n" \
    "v_pk_mul_f32 v[140:141], v[140:141], v[238:239]\n" \
    "v_pk_mul_f32 v[142:143], v[142:143], v[238:239]\n" \
    "v_mfma_f32_16x16x32_bf16 v[52:55], v[220:223], v[180:183], v[52:55]\n" \
    "v_cvt_pk_bf16_f32 v136, v136, v137\n" \
    "v_cvt_pk_bf16_f32 v137, v138, v139\n" \
    "v_mfma_f32_16x16x32_bf16 v[56:59], v[188:191], v[152:155], v[56:59]\n" \
    "ds_read_b64_tr_b16 v[220:221], v214 offset:13968\n" \
    "v_cvt_pk_bf16_f32 v138, v140, v141\n" \
    "v_cvt_pk_bf16_f32 v139, v142, v143\n" \
    "v_mfma_f32_16x16x32_bf16 v[60:63], v[188:191], v[156:159], v[60:63]\n" \
    "ds_read_b64_tr_b16 v[222:223], v214 offset:14400\n" \
    "v_cndmask_b32 v230, v138, v136, %[mo]\n" \
    "v_cndmask_b32 v231, v139, v137, %[mo]\n" \
    "v_mfma_f32_16x16x32_bf16 v[64:67], v[188:191], v[160:163], v[64:67]\n" \
    "v_cndmask_b32 v228, v136, v138, %[mo]\n" \
    "v_cndmask_b32 v229, v137, v139, %[mo]\n" \
    "v_mfma_f32_16x16x32_bf16 v[68:71], v[188:191], v[164:167], v[68:71]\n" \
    "v_mov_b32_dpp v230, v230 quad_perm:[1,0,3,2] row_mask:0xf bank_mask:0xf\n" \
    "v_mov_b32_dpp v231, v231 quad_perm:[1,0,3,2] row_mask:0xf bank_mask:0xf\n" \
    "s_waitcnt lgkmcnt(2)\n" \
    "v_mfma_f32_16x16x32_bf16 v[56:59], v[216:219], v[168:171], v[56:59]\n" \
    "v_perm_b32 v140, v228, v230, v236\n" \
    "v_perm_b32 v141, v228, v230, v237\n" \
    "v_mfma_f32_16x16x32_bf16 v[60:63], v[216:219], v[172:175], v[60:63]\n" \
    "v_perm_b32 v142, v229, v231, v236\n" \
    "v_perm_b32 v143, v229, v231, v237\n" \
    "v_mfma_f32_16x16x32_bf16 v[64:67], v[216:219], v[176:179], v[64:67]\n" \
    "v_cndmask_b32 v136, v140, v136, %[m8]\n" \
    "v_cndmask_b32 v137, v141, v137, %[m8]\n" \
    "v_mfma_f32_16x16x32_bf16 v[68:71], v[216:219], v[180:183], v[68:71]\n" \
    "v_cndmask_b32 v138, v142, v138, %[m8]\n" \
    "v_cndmask_b32 v139, v143, v139, %[m8]\n" \
    "v_mfma_f32_16x16x32_bf16 v[72:75], v[192:195], v[152:155], v[72:75]\n" \
    "ds_read_b64_tr_b16 v[216:217], v214 offset:14008\n" \
    "buffer_store_dwordx4 v[136:139], v234, %[rs], 0 offen sc1\n" \
    "v_pk_mul_f32 v[144:145], v[144:145], v[238:239]\n" \
    "v_pk_mul_f32 v[146:147], v[146:147], v[238:239]\n" \
    "v_mfma_f32_16x16x32_bf16 v[76:79], v[192:195], v[156:159], v[76:79]\n" \
    "ds_read_b64_tr_b16 v[218:219], v214 offset:14440\n" \
    "v_pk_mul_f32 v[148:149], v[148:149], v[238:239]\n" \
    "v_pk_mul_f32 v[150:151], v[150:151], v[238:239]\n" \
    "v_mfma_f32_16x16x32_bf16 v[80:83], v[192:195], v[160:163], v[80:83]\n" \
    "v_cvt_pk_bf16_f32 v144, v144, v145\n" \
    "v_cvt_pk_bf16_f32 v145, v146, v147\n" \
    "v_mfma_f32_16x16x32_bf16 v[84:87], v[192:195], v[164:167], v[84:87]\n" \
    "v_cvt_pk_bf16_f32 v146, v148, v149\n" \
    "v_cvt_pk_bf16_f32 v147, v150, v151\n" \
    "s_waitcnt lgkmcnt(2)\n" \
    "v_mfma_f32_16x16x32_bf16 v[72:75], v[220:223], v[168:171], v[72:75]\n" \
    "v_cndmask_b32 v230, v146, v144, %[mo]\n" \
    "v_cndmask_b32 v231, v147, v145, %[mo]\n" \
    "v_mfma_f32_16x16x32_bf16 v[76:79], v[220:223], v[172:175], v[76:79]\n" \
    "v_cndmask_b32 v228, v144, v146, %[mo]\n" \
    "v_cndmask_b32 v229, v145, v147, %[mo]\n" \
    "v_mfma_f32_16x16x32_bf16 v[80:83], v[220:223], v[176:179], v[80:83]\n" \
    "v_mov_b32_dpp v230, v230 quad_perm:[1,0,3,2] row_mask:0xf bank_mask:0xf\n" \
    "v_mov_b32_dpp v231, v231 quad_perm:[1,0,3,2] row_mask:0xf bank_mask:0xf\n" \
    "v_mfma_f32_16x16x32_bf16 v[84:87], v[220:223], v[180:183], v[84:87]\n" \
    "v_perm_b32 v148, v228, v230, v236\n" \
    "v_perm_b32 v149, v228, v230, v237\n" \
    "v_mfma_f32_16x16x32_bf16 v[88:91], v[196:199], v[152:155], v[88:91]\n" \
    "ds_read_b64_tr_b16 v[220:221], v214 offset:14112\n" \
    "v_perm_b32 v150, v229, v231, v236\n" \
    "v_perm_b32 v151, v229, v231, v237\n" \
    "v_mfma_f32_16x16x32_bf16 v[92:95], v[196:199], v[156:159], v[92:95]\n" \
    "ds_read_b64_tr_b16 v[222:223], v214 offset:14544\n" \
    "v_cndmask_b32 v144, v148, v144, %[m8]\n" \
    "v_cndmask_b32 v145, v149, v145, %[m8]\n" \
    "v_mfma_f32_16x16x32_bf16 v[96:99], v[196:199], v[160:163], v[96:99]\n" \
    "v_cndmask_b32 v146, v150, v146, %[m8]\n" \
    "v_cndmask_b32 v147, v151, v147, %[m8]\n" \
    "v_mfma_f32_16x16x32_bf16 v[100:103], v[196:199], v[164:167], v[100:103]\n" \
    "buffer_store_dwordx4 v[144:147], v235, %[rs], 0 offen sc1\n" \
    "v_pk_mul_f32 v[40:41], v[40:41], %[qs]\n" \
    "v_pk_mul_f32 v[42:43], v[42:43], %[qs]\n" \
    "s_waitcnt lgkmcnt(2)\n" \
    "v_mfma_f32_16x16x32_bf16 v[88:91], v[216:219], v[168:171], v[88:91]\n" \
    "v_pk_mul_f32 v[44:45], v[44:45], %[qs]\n" \
    "v_pk_mul_f32 v[46:47], v[46:47], %[qs]\n" \
    "v_mfma_f32_16x16x32_bf16 v[92:95], v[216:219], v[172:175], v[92:95]\n" \
    "v_cvt_pk_bf16_f32 v40, v40, v41\n" \
    "v_cvt_pk_bf16_f32 v41, v42, v43\n" \
    "v_mfma_f32_16x16x32_bf16 v[96:99], v[216:219], v[176:179], v[96:99]\n" \
    "v_cvt_pk_bf16_f32 v42, v44, v45\n" \
    "v_cvt_pk_bf16_f32 v43, v46, v47\n" \
    "v_mfma_f32_16x16x32_bf16 v[100:103], v[216:219], v[180:183], v[100:103]\n" \
    "v_permlane16_swap_b32 v40, v42\n" \
    "v_permlane16_swap_b32 v41, v43\n" \
    "v_mfma_f32_16x16x32_bf16 v[104:107], v[152:155], v[200:203], v[104:107]\n" \
    "ds_read_b64_tr_b16 v[216:217], v214 offset:14152\n" \
    "buffer_store_dwordx4 v[40:43], v232, %[rs], 0 offen offset:0 sc1\n" \
    "v_pk_mul_f32 v[48:49], v[48:49], %[qs]\n" \
    "v_pk_mul_f32 v[50:51], v[50:51], %[qs]\n" \
    "v_mfma_f32_16x16x32_bf16 v[108:111], v[156:159], v[200:203], v[108:111]\n" \
    "ds_read_b64_tr_b16 v[218:219], v214 offset:14584\n" \
    "v_pk_mul_f32 v[52:53], v[52:53], %[qs]\n" \
    "v_pk_mul_f32 v[54:55], v[54:55], %[qs]\n" \
    "v_mfma_f32_16x16x32_bf16 v[112:115], v[160:163], v[200:203], v[112:115]\n" \
    "v_cvt_pk_bf16_f32 v44, v48, v49\n" \
    "v_cvt_pk_bf16_f32 v45, v50, v51\n" \
    "v_mfma_f32_16x16x32_bf16 v[116:119], v[164:167], v[200:203], v[116:119]\n" \
    "v_cvt_pk_bf16_f32 v46, v52, v53\n" \
    "v_cvt_pk_bf16_f32 v47, v54, v55\n" \
    "s_waitcnt lgkmcnt(2)\n" \
    "v_mfma_f32_16x16x32_bf16 v[104:107], v[168:171], v[220:223], v[104:107]\n" \
    "v_permlane16_swap_b32 v44, v46\n" \
    "v_permlane16_swap_b32 v45, v47\n" \
    "v_mfma_f32_16x16x32_bf16 v[108:111], v[172:175], v[220:223], v[108:111]\n" \
    "buffer_store_dwordx4 v[44:47], v232, %[rs], 0 offen offset:1024 sc1\n" \
    "v_add_u32 v225, 65536, v232\n" \
    "v_pk_mul_f32 v[56:57], v[56:57], %[qs]\n" \
    "v_mfma_f32_16x16x32_bf16 v[112:115], v[176:179], v[220:223], v[112:115]\n" \
    "v_pk_mul_f32 v[58:59], v[58:59], %[qs]\n" \
    "v_pk_mul_f32 v[60:61], v[60:61], %[qs]\n" \
    "v_mfma_f32_16x16x32_bf16 v[116:119], v[180:183], v[220:223], v[116:119]\n" \
    "v_pk_mul_f32 v[62:63], v[62:63], %[qs]\n" \
    "v_cvt_pk_bf16_f32 v56, v56, v57\n" \
    "v_mfma_f32_16x16x32_bf16 v[120:123], v[152:155], v[204:207], v[120:123]\n" \
    "v_cvt_pk_bf16_f32 v57, v58, v59\n" \
    "v_cvt_pk_bf16_f32 v58, v60, v61\n" \
    "v_mfma_f32_16x16x32_bf16 v[124:127], v[156:159], v[204:207], v[124:127]\n" \
    "v_cvt_pk_bf16_f32 v59, v62, v63\n" \
    "v_permlane16_swap_b32 v56, v58\n" \
    "v_mfma_f32_16x16x32_bf16 v[128:131], v[160:163], v[204:207], v[128:131]\n" \
    "v_permlane16_swap_b32 v57, v59\n" \
    "buffer_store_dwordx4 v[56:59], v225, %[rs], 0 offen offset:0 sc1\n" \
    "v_pk_mul_f32 v[64:65], v[64:65], %[qs]\n" \
    "v_mfma_f32_16x16x32_bf16 v[132:135], v[164:167], v[204:207], v[132:135]\n" \
    "v_pk_mul_f32 v[66:67], v[66:67], %[qs]\n" \
    "v_pk_mul_f32 v[68:69], v[68:69], %[qs]\n" \
    "s_waitcnt lgkmcnt(0)\n" \
    "v_mfma_f32_16x16x32_bf16 v[120:123], v[168:171], v[216:219], v[120:123]\n" \
    "v_pk_mul_f32 v[70:71], v[70:71], %[qs]\n" \
    "v_cvt_pk_bf16_f32 v60, v64, v65\n" \
    "v_mfma_f32_16x16x32_bf16 v[124:127], v[172:175], v[216:219], v[124:127]\n" \
    "v_cvt_pk_bf16_f32 v61, v66, v67\n" \
    "v_cvt_pk_bf16_f32 v62, v68, v69\n" \
    "v_mfma_f32_16x16x32_bf16 v[128:131], v[176:179], v[216:219], v[128:131]\n" \
    "v_cvt_pk_bf16_f32 v63, v70, v71\n" \
    "v_permlane16_swap_b32 v60, v62\n" \
    "v_mfma_f32_16x16x32_bf16 v[132:135], v[180:183], v[216:219], v[132:135]\n" \
    "v_permlane16_swap_b32 v61, v63\n" \
    "buffer_store_dwordx4 v[60:63], v225, %[rs], 0 offen offset:1024 sc1\n" \
    "v_add_u32 v226, 262144, v232\n" \
    "v_pk_mul_f32 v[72:73], v[72:73], %[qs]\n" \
    "v_pk_mul_f32 v[74:75], v[74:75], %[qs]\n" \
    "v_pk_mul_f32 v[76:77], v[76:77], %[qs]\n" \
    "v_pk_mul_f32 v[78:79], v[78:79], %[qs]\n" \
    "v_cvt_pk_bf16_f32 v72, v72, v73\n" \
    "v_cvt_pk_bf16_f32 v73, v74, v75\n" \
    "v_cvt_pk_bf16_f32 v74, v76, v77\n" \
    "v_cvt_pk_bf16_f32 v75, v78, v79\n" \
    "s_nop 0\n" \
    "v_permlane16_swap_b32 v72, v74\n" \
    "v_permlane16_swap_b32 v73, v75\n" \
    "buffer_store_dwordx4 v[72:75], v226, %[rs], 0 offen offset:0 sc1\n" \
    "v_pk_mul_f32 v[80:81], v[80:81], %[qs]\n" \
    "v_pk_mul_f32 v[82:83], v[82:83], %[qs]\n" \
    "v_pk_mul_f32 v[84:85], v[84:85], %[qs]\n" \
    "v_pk_mul_f32 v[86:87], v[86:87], %[qs]\n" \
    "v_cvt_pk_bf16_f32 v76, v80, v81\n" \
    "v_cvt_pk_bf16_f32 v77, v82, v83\n" \
    "v_cvt_pk_bf16_f32 v78, v84, v85\n" \
    "v_cvt_pk_bf16_f32 v79, v86, v87\n" \
    "s_nop 0\n" \
    "v_permlane16_swap_b32 v76, v78\n" \
    "v_permlane16_swap_b32 v77, v79\n" \
    "buffer_store_dwordx4 v[76:79], v226, %[rs], 0 offen offset:1024 sc1\n" \
    "v_add_u32 v227, 327680, v232\n" \
    "v_pk_mul_f32 v[88:89], v[88:89], %[qs]\n" \
    "v_pk_mul_f32 v[90:91], v[90:91], %[qs]\n" \
    "v_pk_mul_f32 v[92:93], v[92:93], %[qs]\n" \
    "v_pk_mul_f32 v[94:95], v[94:95], %[qs]\n" \
    "v_cvt_pk_bf16_f32 v88, v88, v89\n" \
    "v_cvt_pk_bf16_f32 v89, v90, v91\n" \
    "v_cvt_pk_bf16_f32 v90, v92, v93\n" \
    "v_cvt_pk_bf16_f32 v91, v94, v95\n" \
    "s_nop 0\n" \
    "v_permlane16_swap_b32 v88, v90\n" \
    "v_permlane16_swap_b32 v89, v91\n" \
    "buffer_store_dwordx4 v[88:91], v227, %[rs], 0 offen offset:0 sc1\n" \
    "v_pk_mul_f32 v[96:97], v[96:97], %[qs]\n" \
    "v_pk_mul_f32 v[98:99], v[98:99], %[qs]\n" \
    "v_pk_mul_f32 v[100:101], v[100:101], %[qs]\n" \
    "v_pk_mul_f32 v[102:103], v[102:103], %[qs]\n" \
    "v_cvt_pk_bf16_f32 v92, v96, v97\n" \
    "v_cvt_pk_bf16_f32 v93, v98, v99\n" \
    "v_cvt_pk_bf16_f32 v94, v100, v101\n" \
    "v_cvt_pk_bf16_f32 v95, v102, v103\n" \
    "s_nop 0\n" \
    "v_permlane16_swap_b32 v92, v94\n" \
    "v_permlane16_swap_b32 v93, v95\n" \
    "buffer_store_dwordx4 v[92:95], v227, %[rs], 0 offen offset:1024 sc1\n" \
    "v_cvt_pk_bf16_f32 v104, v104, v105\n" \
    "v_cvt_pk_bf16_f32 v105, v106, v107\n" \
    "v_cvt_pk_bf16_f32 v106, v108, v109\n" \
    "v_cvt_pk_bf16_f32 v107, v110, v111\n" \
    "buffer_store_dwordx4 v[104:107], v233, %[rs], 0 offen offset:0 sc1\n" \
    "v_cvt_pk_bf16_f32 v108, v112, v113\n" \
    "v_cvt_pk_bf16_f32 v109, v114, v115\n" \
    "v_cvt_pk_bf16_f32 v110, v116, v117\n" \
    "v_cvt_pk_bf16_f32 v111, v118, v119\n" \
    "buffer_store_dwordx4 v[108:111], v233, %[rs], 0 offen offset:1152 sc1\n" \
    "v_add_u32 v224, 147456, v233\n" \
    "v_cvt_pk_bf16_f32 v120, v120, v121\n" \
    "v_cvt_pk_bf16_f32 v121, v122, v123\n" \
    "v_cvt_pk_bf16_f32 v122, v124, v125\n" \
    "v_cvt_pk_bf16_f32 v123, v126, v127\n" \
    "buffer_store_dwordx4 v[120:123], v224, %[rs], 0 offen offset:0 sc1\n" \
    "v_cvt_pk_bf16_f32 v124, v128, v129\n" \
    "v_cvt_pk_bf16_f32 v125, v130, v131\n" \
    "v_cvt_pk_bf16_f32 v126, v132, v133\n" \
    "v_cvt_pk_bf16_f32 v127, v134, v135\n" \
    "buffer_store_dwordx4 v[124:127], v224, %[rs], 0 offen offset:1152 sc1\n"
#define P1_TAIL_K \
    "s_waitcnt lgkmcnt(12)\n" \
    "v_mfma_f32_16x16x32_bf16 v[40:43], v[184:187], v[152:155], v[40:43]\n" \
    "ds_read_b128 v[168:171], v213 offset:0\n" \
    "ds_read_b128 v[172:175], v213 offset:2048\n" \
    "v_mfma_f32_16x16x32_bf16 v[44:47], v[184:187], v[156:159], v[44:47]\n" \
    "ds_read_b128 v[176:179], v213 offset:4096\n" \
    "ds_read_b128 v[180:183], v213 offset:6144\n" \
    "v_mfma_f32_16x16x32_bf16 v[48:51], v[184:187], v[160:163], v[48:51]\n" \
    "ds_read_b64_tr_b16 v[216:217], v215 offset:13824\n" \
    "ds_read_b64_tr_b16 v[218:219], v215 offset:14256\n" \
    "v_mfma_f32_16x16x32_bf16 v[52:55], v[184:187], v[164:167], v[52:55]\n" \
    "ds_read_b64_tr_b16 v[220:221], v214 offset:13824\n" \
    "s_waitcnt lgkmcnt(7)\n" \
    "v_mfma_f32_16x16x32_bf16 v[136:139], v[152:155], v[208:211], v[136:139]\n" \
    "ds_read_b64_tr_b16 v[222:223], v214 offset:14256\n" \
    "v_mfma_f32_16x16x32_bf16 v[140:143], v[156:159], v[208:211], v[140:143]\n" \
    "v_mfma_f32_16x16x32_bf16 v[144:147], v[160:163], v[208:211], v[144:147]\n" \
    "v_mfma_f32_16x16x32_bf16 v[148:151], v[164:167], v[208:211], v[148:151]\n" \
    "s_waitcnt lgkmcnt(2)\n" \
    "v_mfma_f32_16x16x32_bf16 v[136:139], v[168:171], v[216:219], v[136:139]\n" \
    "v_mfma_f32_16x16x32_bf16 v[140:143], v[172:175], v[216:219], v[140:143]\n" \
    "v_mfma_f32_16x16x32_bf16 v[144:147], v[176:179], v[216:219], v[144:147]\n" \
    "v_mfma_f32_16x16x32_bf16 v[148:151], v[180:183], v[216:219], v[148:151]\n" \
    "s_waitcnt lgkmcnt(0)\n" \
    "v_mfma_f32_16x16x32_bf16 v[40:43], v[220:223], v[168:171], v[40:43]\n" \
    "ds_read_b64_tr_b16 v[216:217], v214 offset:13864\n" \
    "v_mfma_f32_16x16x32_bf16 v[44:47], v[220:223], v[172:175], v[44:47]\n" \
    "ds_read_b64_tr_b16 v[218:219], v214 offset:14296\n" \
    "v_cvt_pk_bf16_f32 v136, v136, v137\n" \
    "v_cvt_pk_bf16_f32 v137, v138, v139\n" \
    "v_mfma_f32_16x16x32_bf16 v[48:51], v[220:223], v[176:179], v[48:51]\n" \
    "v_cvt_pk_bf16_f32 v138, v140, v141\n" \
    "v_cvt_pk_bf16_f32 v139, v142, v143\n" \
    "v_mfma_f32_16x16x32_bf16 v[52:55], v[220:223], v[180:183], v[52:55]\n" \
    "v_cndmask_b32 v230, v138, v136, %[mo]\n" \
    "v_cndmask_b32 v231, v139, v137, %[mo]\n" \
    "v_mfma_f32_16x16x32_bf16 v[56:59], v[188:191], v[152:155], v[56:59]\n" \
    "ds_read_b64_tr_b16 v[220:221], v214 offset:13968\n" \
    "v_cndmask_b32 v228, v136, v138, %[mo]\n" \
    "v_cndmask_b32 v229, v137, v139, %[mo]\n" \
    "v_mfma_f32_16x16x32_bf16 v[60:63], v[188:191], v[156:159], v[60:63]\n" \
    "ds_read_b64_tr_b16 v[222:223], v214 offset:14400\n" \
    "v_mov_b32_dpp v230, v230 quad_perm:[1,0,3,2] row_mask:0xf bank_mask:0xf\n" \
    "v_mov_b32_dpp v231, v231 quad_perm:[1,0,3,2] row_mask:0xf bank_mask:0xf\n" \
    "v_mfma_f32_16x16x32_bf16 v[64:67], v[188:191], v[160:163], v[64:67]\n" \
    "v_perm_b32 v140, v228, v230, v236\n" \
    "v_perm_b32 v141, v228, v230, v237\n" \
    "v_mfma_f32_16x16x32_bf16 v[68:71], v[188:191], v[164:167], v[68:71]\n" \
    "v_perm_b32 v142, v229, v231, v236\n" \
    "v_perm_b32 v143, v229, v231, v237\n" \
    "s_waitcnt lgkmcnt(2)\n" \
    "v_mfma_f32_16x16x32_bf16 v[56:59], v[216:219], v[168:171], v[56:59]\n" \
    "v_cndmask_b32 v136, v140, v136, %[m8]\n" \
    "v_cndmask_b32 v137, v141, v137, %[m8]\n" \
    "v_mfma_f32_16x16x32_bf16 v[60:63], v[216:219], v[172:175], v[60:63]\n" \
    "v_cndmask_b32 v138, v142, v138, %[m8]\n" \
    "v_cndmask_b32 v139, v143, v139, %[m8]\n" \
    "v_mfma_f32_16x16x32_bf16 v[64:67], v[216:219], v[176:179], v[64:67]\n" \
    "buffer_store_dwordx4 v[136:139], v234, %[rs], 0 offen sc1\n" \
    "v_cvt_pk_bf16_f32 v144, v144, v145\n" \
    "v_cvt_pk_bf16_f32 v145, v146, v147\n" \
    "v_mfma_f32_16x16x32_bf16 v[68:71], v[216:219], v[180:183], v[68:71]\n" \
    "v_cvt_pk_bf16_f32 v146, v148, v149\n" \
    "v_cvt_pk_bf16_f32 v147, v150, v151\n" \
    "v_mfma_f32_16x16x32_bf16 v[72:75], v[192:195], v[152:155], v[72:75]\n" \
    "ds_read_b64_tr_b16 v[216:217], v214 offset:14008\n" \
    "v_cndmask_b32 v230, v146, v144, %[mo]\n" \
    "v_cndmask_b32 v231, v147, v145, %[mo]\n" \
    "v_mfma_f32_16x16x32_bf16 v[76:79], v[192:195], v[156:159], v[76:79]\n" \
    "ds_read_b64_tr_b16 v[218:219], v214 offset:14440\n" \
    "v_cndmask_b32 v228, v144, v146, %[mo]\n" \
    "v_cndmask_b32 v229, v145, v147, %[mo]\n" \
    "v_mfma_f32_16x16x32_bf16 v[80:83], v[192:195], v[160:163], v[80:83]\n" \
    "v_mov_b32_dpp v230, v230 quad_perm:[1,0,3,2] row_mask:0xf bank_mask:0xf\n" \
    "v_mov_b32_dpp v231, v231 quad_perm:[1,0,3,2] row_mask:0xf bank_mask:0xf\n" \
    "v_mfma_f32_16x16x32_bf16 v[84:87], v[192:195], v[164:167], v[84:87]\n" \
    "v_perm_b32 v148, v228, v230, v236\n" \
    "v_perm_b32 v149, v228, v230, v237\n" \
    "s_waitcnt lgkmcnt(2)\n" \
    "v_mfma_f32_16x16x32_bf16 v[72:75], v[220:223], v[168:171], v[72:75]\n" \
    "v_perm_b32 v150, v229, v231, v236\n" \
    "v_perm_b32 v151, v229, v231, v237\n" \
    "v_mfma_f32_16x16x32_bf16 v[76:79], v[220:223], v[172:175], v[76:79]\n" \
    "v_cndmask_b32 v144, v148, v144, %[m8]\n" \
    "v_cndmask_b32 v145, v149, v145, %[m8]\n" \
    "v_mfma_f32_16x16x32_bf16 v[80:83], v[220:223], v[176:179], v[80:83]\n" \
    "v_cndmask_b32 v146, v150, v146, %[m8]\n" \
    "v_cndmask_b32 v147, v151, v147, %[m8]\n" \
    "v_mfma_f32_16x16x32_bf16 v[84:87], v[220:223], v[180:183], v[84:87]\n" \
    "buffer_store_dwordx4 v[144:147], v235, %[rs], 0 offen sc1\n" \
    "v_cvt_pk_bf16_f32 v40, v40, v41\n" \
    "v_cvt_pk_bf16_f32 v41, v42, v43\n" \
    "v_mfma_f32_16x16x32_bf16 v[88:91], v[196:199], v[152:155], v[88:91]\n" \
    "ds_read_b64_tr_b16 v[220:221], v214 offset:14112\n" \
    "v_cvt_pk_bf16_f32 v42, v44, v45\n" \
    "v_cvt_pk_bf16_f32 v43, v46, v47\n" \
    "v_mfma_f32_16x16x32_bf16 v[92:95], v[196:199], v[156:159], v[92:95]\n" \
    "ds_read_b64_tr_b16 v[222:223], v214 offset:14544\n" \
    "v_permlane16_swap_b32 v40, v42\n" \
    "v_permlane16_swap_b32 v41, v43\n" \
    "v_mfma_f32_16x16x32_bf16 v[96:99], v[196:199], v[160:163], v[96:99]\n" \
    "buffer_store_dwordx4 v[40:43], v232, %[rs], 0 offen offset:0 sc1\n" \
    "v_cvt_pk_bf16_f32 v44, v48, v49\n" \
    "v_cvt_pk_bf16_f32 v45, v50, v51\n" \
    "v_mfma_f32_16x16x32_bf16 v[100:103], v[196:199], v[164:167], v[100:103]\n" \
    "v_cvt_pk_bf16_f32 v46, v52, v53\n" \
    "v_cvt_pk_bf16_f32 v47, v54, v55\n" \
    "s_waitcnt lgkmcnt(2)\n" \
    "v_mfma_f32_16x16x32_bf16 v[88:91], v[216:219], v[168:171], v[88:91]\n" \
    "v_permlane16_swap_b32 v44, v46\n" \
    "v_permlane16_swap_b32 v45, v47\n" \
    "v_mfma_f32_16x16x32_bf16 v[92:95], v[216:219], v[172:175], v[92:95]\n" \
    "buffer_store_dwordx4 v[44:47], v232, %[rs], 0 offen offset:1024 sc1\n" \
    "v_add_u32 v225, 147456, v232\n" \
    "v_cvt_pk_bf16_f32 v56, v56, v57\n" \
    "v_mfma_f32_16x16x32_bf16 v[96:99], v[216:219], v[176:179], v[96:99]\n" \
    "v_cvt_pk_bf16_f32 v57, v58, v59\n" \
    "v_cvt_pk_bf16_f32 v58, v60, v61\n" \
    "v_mfma_f32_16x16x32_bf16 v[100:103], v[216:219], v[180:183], v[100:103]\n" \
    "v_cvt_pk_bf16_f32 v59, v62, v63\n" \
    "v_permlane16_swap_b32 v56, v58\n" \
    "v_mfma_f32_16x16x32_bf16 v[104:107], v[152:155], v[200:203], v[104:107]\n" \
    "ds_read_b64_tr_b16 v[216:217], v214 offset:14152\n" \
    "v_permlane16_swap_b32 v57, v59\n" \
    "buffer_store_dwordx4 v[56:59], v225, %[rs], 0 offen offset:0 sc1\n" \
    "v_cvt_pk_bf16_f32 v60, v64, v65\n" \
    "v_mfma_f32_16x16x32_bf16 v[108:111], v[156:159], v[200:203], v[108:111]\n" \
    "ds_read_b64_tr_b16 v[218:219], v214 offset:14584\n" \
    "v_cvt_pk_bf16_f32 v61, v66, v67\n" \
    "v_cvt_pk_bf16_f32 v62, v68, v69\n" \
    "v_mfma_f32_16x16x32_bf16 v[112:115], v[160:163], v[200:203], v[112:115]\n" \
    "v_cvt_pk_bf16_f32 v63, v70, v71\n" \
    "v_permlane16_swap_b32 v60, v62\n" \
    "v_mfma_f32_16x16x32_bf16 v[116:119], v[164:167], v[200:203], v[116:119]\n" \
    "v_permlane16_swap_b32 v61, v63\n" \
    "buffer_store_dwordx4 v[60:63], v225, %[rs], 0 offen offset:1024 sc1\n" \
    "v_add_u32 v226, 589824, v232\n" \
    "s_waitcnt lgkmcnt(2)\n" \
    "v_mfma_f32_16x16x32_bf16 v[104:107], v[168:171], v[220:223], v[104:107]\n" \
    "v_cvt_pk_bf16_f32 v72, v72, v73\n" \
    "v_cvt_pk_bf16_f32 v73, v74, v75\n" \
    "v_mfma_f32_16x16x32_bf16 v[108:111], v[172:175], v[220:223], v[108:111]\n" \
    "v_cvt_pk_bf16_f32 v74, v76, v77\n" \
    "v_cvt_pk_bf16_f32 v75, v78, v79\n" \
    "v_mfma_f32_16x16x32_bf16 v[112:115], v[176:179], v[220:223], v[112:115]\n" \
    "v_permlane16_swap_b32 v72, v74\n" \
    "v_permlane16_swap_b32 v73, v75\n" \
    "v_mfma_f32_16x16x32_bf16 v[116:119], v[180:183], v[220:223], v[116:119]\n" \
    "buffer_store_dwordx4 v[72:75], v226, %[rs], 0 offen offset:0 sc1\n" \
    "v_cvt_pk_bf16_f32 v76, v80, v81\n" \
    "v_cvt_pk_bf16_f32 v77, v82, v83\n" \
    "v_mfma_f32_16x16x32_bf16 v[120:123], v[152:155], v[204:207], v[120:123]\n" \
    "v_cvt_pk_bf16_f32 v78, v84, v85\n" \
    "v_cvt_pk_bf16_f32 v79, v86, v87\n" \
    "v_mfma_f32_16x16x32_bf16 v[124:127], v[156:159], v[204:207], v[124:127]\n" \
    "v_permlane16_swap_b32 v76, v78\n" \
    "v_permlane16_swap_b32 v77, v79\n" \
    "v_mfma_f32_16x16x32_bf16 v[128:131], v[160:163], v[204:207], v[128:131]\n" \
    "buffer_store_dwordx4 v[76:79], v226, %[rs], 0 offen offset:1024 sc1\n" \
    "v_add_u32 v227, 737280, v232\n" \
    "v_cvt_pk_bf16_f32 v88, v88, v89\n" \
    "v_mfma_f32_16x16x32_bf16 v[132:135], v[164:167], v[204:207], v[132:135]\n" \
    "v_cvt_pk_bf16_f32 v89, v90, v91\n" \
    "v_cvt_pk_bf16_f32 v90, v92, v93\n" \
    "s_waitcnt lgkmcnt(0)\n" \
    "v_mfma_f32_16x16x32_bf16 v[120:123], v[168:171], v[216:219], v[120:123]\n" \
    "v_cvt_pk_bf16_f32 v91, v94, v95\n" \
    "v_permlane16_swap_b32 v88, v90\n" \
    "v_mfma_f32_16x16x32_bf16 v[124:127], v[172:175], v[216:219], v[124:127]\n" \
    "v_permlane16_swap_b32 v89, v91\n" \
    "buffer_store_dwordx4 v[88:91], v227, %[rs], 0 offen offset:0 sc1\n" \
    "v_cvt_pk_bf16_f32 v92, v96, v97\n" \
    "v_mfma_f32_16x16x32_bf16 v[128:131], v[176:179], v[216:219], v[128:131]\n" \
    "v_cvt_pk_bf16_f32 v93, v98, v99\n" \
    "v_cvt_pk_bf16_f32 v94, v100, v101\n" \
    "v_mfma_f32_16x16x32_bf16 v[132:135], v[180:183], v[216:219], v[132:135]\n" \
    "v_cvt_pk_bf16_f32 v95, v102, v103\n" \
    "v_permlane16_swap_b32 v92, v94\n" \
    "s_nop 0\n" \
    "v_permlane16_swap_b32 v93, v95\n" \
    "buffer_store_dwordx4 v[92:95], v227, %[rs], 0 offen offset:1024 sc1\n" \
    "v_cvt_pk_bf16_f32 v104, v104, v105\n" \
    "v_cvt_pk_bf16_f32 v105, v106, v107\n" \
    "v_cvt_pk_bf16_f32 v106, v108, v109\n" \
    "v_cvt_pk_bf16_f32 v107, v110, v111\n" \
    "buffer_store_dwordx4 v[104:107], v233, %[rs], 0 offen offset:0 sc1\n" \
    "v_cvt_pk_bf16_f32 v108, v112, v113\n" \
    "v_cvt_pk_bf16_f32 v109, v114, v115\n" \
    "v_cvt_pk_bf16_f32 v110, v116, v117\n" \
    "v_cvt_pk_bf16_f32 v111, v118, v119\n" \
    "buffer_store_dwordx4 v[108:111], v233, %[rs], 0 offen offset:1152 sc1\n" \
    "v_add_u32 v224, 147456, v233\n" \
    "v_cvt_pk_bf16_f32 v120, v120, v121\n" \
    "v_cvt_pk_bf16_f32 v121, v122, v123\n" \
    "v_cvt_pk_bf16_f32 v122, v124, v125\n" \
    "v_cvt_pk_bf16_f32 v123, v126, v127\n" \
    "buffer_store_dwordx4 v[120:123], v224, %[rs], 0 offen offset:0 sc1\n" \
    "v_cvt_pk_bf16_f32 v124, v128, v129\n" \
    "v_cvt_pk_bf16_f32 v125, v130, v131\n" \
    "v_cvt_pk_bf16_f32 v126, v132, v133\n" \
    "v_cvt_pk_bf16_f32 v127, v134, v135\n" \
    "buffer_store_dwordx4 v[124:127], v224, %[rs], 0 offen offset:1152 sc1\n"

DI u32x4 fm_pack(f32x4 t0, f32x4 t1) {
    unsigned a0 = pk(t0[0], t0[1]), a1 = pk(t0[2], t0[3]), b0 = pk(t1[0], t1[1]), b1 = pk(t1[2], t1[3]);
    auto r0 = __builtin_amdgcn_permlane16_swap(a0, b0, false, false);
    auto r1 = __builtin_amdgcn_permlane16_swap(a1, b1, false, false);
    return (u32x4){r0[0], r1[0], r0[1], r1[1]};
}
DI u32x4 tm_pack(f32x4 t0, f32x4 t1) { return (u32x4){pk(t0[0], t0[1]), pk(t0[2], t0[3]), pk(t1[0], t1[1]), pk(t1[2], t1[3])}; }
DI f32x4 sub4(const f32x16& a, int r) { return (f32x4){a[4 * r], a[4 * r + 1], a[4 * r + 2], a[4 * r + 3]}; }

DI float dot2(unsigned a, unsigned b, float c) { return __builtin_amdgcn_fdot2_f32_bf16(__builtin_bit_cast(bf16x2, a), __builtin_bit_cast(bf16x2, b), c, false); }
DI float nrm8(u32x4 v) { return dot2(v[3], v[3], dot2(v[2], v[2], dot2(v[1], v[1], dot2(v[0], v[0], 0.f)))); }
DI u32x4 pair_up(u32x4 vr, bool odd, unsigned selLo, unsigned selHi) {
    const unsigned a = odd ? vr[2] : vr[0], b = odd ? vr[3] : vr[1];
    const unsigned c = dppu<0xB1>(odd ? vr[0] : vr[2]), d = dppu<0xB1>(odd ? vr[1] : vr[3]);
    return (u32x4){__builtin_amdgcn_perm(a, c, selLo), __builtin_amdgcn_perm(a, c, selHi), __builtin_amdgcn_perm(b, d, selLo), __builtin_amdgcn_perm(b, d, selHi)};
}

DI void p1_loader(const char* ws, const char* X, const char* W, unsigned c0, unsigned c1, unsigned c2, int r, u64 kord, int rw, int rx, int lane, int lw) {
    int rot = (int)kord & 15;
    unsigned xo, wo[7], wd[7], xd[4];
    { const int row = lw * 8 + (lane >> 3); xo = (unsigned)(r * 128 + row) * 1152u + (((lane & 7) ^ (row & 7)) << 4); }
#pragma unroll
    for (int i = 0; i < 4; i++) { xd[i] = (i + rx) & 3; glds16(xo, X + rot * 128 + xd[i] * 32 * 1152, (lw + 4 * xd[i]) * 1024); }
#pragma unroll
    for (int i = 0; i < 7; i++) {
        int pi = i + rw; pi = pi >= 7 ? pi - 7 : pi; pi = lw + 4 * pi; pi = pi == 27 ? 26 : pi;
        const unsigned u = pi * 64u + lane;
        const unsigned lr = (u * 2428u) >> 16, pc = u - lr * 27u;
        const unsigned k = (lr & ~7u) | ((lr & 1u) << 2) | ((lr >> 1) & 3u);
        wo[i] = k * 3456u + pc * 16u + (pc < 9 ? c0 : pc < 18 ? c1 : c2);
        wd[i] = 16384 + pi * 1024;
        glds16(wo[i], W + rot * 64 * 3456, wd[i]);
    }
    u64 ep = 0;
    if (lw == 0) asm volatile("global_load_dwordx2 %0, %1, %2 sc1" : "+v"(ep) : "v"(OFF_EPOCH), "s"(ws) : "memory");
    unsigned dst = ST1;
#pragma unroll 1
    for (int s = 1; s < 9; s++) {
        rot = (int)(kord >> (4 * s)) & 15;
        const char* xb = X + rot * 128; const char* wb = W + rot * 64 * 3456;
#pragma unroll
        for (int i = 0; i < 7; i++) {
            glds16(wo[i], wb, dst + wd[i]);
            if (i == 2 && s == 1) asm volatile("s_waitcnt vmcnt(5)\n\ts_barrier" ::: "memory");
            if (i < 4) glds16(xo, xb + xd[i] * 32 * 1152, dst + (lw + 4 * xd[i]) * 1024);
        }
        dst = dst >= 2 * ST1 ? 0 : dst + ST1;
        if (s > 1) asm volatile("s_waitcnt vmcnt(11)\n\ts_barrier" ::: "memory");
    }
    if (lw == 0) asm volatile("ds_write_b64 %0, %1" :: "v"(L_FLAG + 32), "v"(ep) : "memory");
    WAITV_BAR(0);
}

template <int TY>
DI unsigned p1_consumer(rsrc_t rs, int quad, int r, int lane, int w) {
    constexpr bool ISB = TY == 2;
    const int rh = w & 1, pr = w >> 1, g = lane >> 4, m = lane & 15, q4 = m >> 2, p = m & 3;
    unsigned aW = 16384 + (g * 8 + q4 * 2) * 432 + pr * 72 + 8 * p;
    unsigned aR = 16384 + (g * 8 + q4 * 2) * 432 + pr * 72 + (p < 2 ? p : 2) * 144 + 32;
    unsigned aX0 = (64 * rh + m) * 128 + ((g ^ (m & 7)) << 4), aX1 = aX0 ^ 64;
    const int h0 = 4 * quad + 2 * pr, hA = 8 * (quad >> 1) + 2 * pr, T0 = 4 * r + 2 * rh;
    const int rho = ((m >> 2) << 3) + ((g & 1) << 2) + (m & 3);
    const unsigned fmoff = (g >> 1) * 512 + rho * 16, kvo = OFF_KV + (T0 >> 3) * CHUNK, u0 = T0 & 7, pofs = (8 * g + 4 * (m & 1)) * 4;
    const bool odd = lane & 1;
    unsigned selLo = odd ? 0x05040100u : 0x01000504u, selHi = odd ? 0x07060302u : 0x03020706u;
    unsigned oF, oV = 0, oR, oR1;
    if (ISB) {
        oF = OFF_Q1 + (32 + h0) * 65536 + T0 * 1024 + fmoff;
        oR = m < 4 ? OFF_Q2 + (32 + h0 + (m >> 1)) * 8192 + T0 * 128 + pofs : WS_USED; oR1 = oR + 128;
    } else {
        const int hr = hA + ((m >> 1) & 1) + (m & 4);
        oF = (TY == 0 ? OFF_Q1 + hA * 65536 + T0 * 1024 : kvo + hA * KV_HEAD + u0 * 1024) + fmoff;
        oV = kvo + h0 * KV_HEAD + 9216 + u0 * 1152 + g * 256 + ((m ^ ((g >> 1) << 3)) << 4);
        oR = (TY == 0 ? OFF_Q2 + hr * 8192 + T0 * 128 : kvo + hr * KV_HEAD + 8192 + u0 * 128) + pofs; oR1 = oR + 128;
        if (m >= 8) { oR = kvo + (h0 + ((m >> 1) & 1)) * KV_HEAD + 9216 + u0 * 1152 + 1024 + g * 32 + (m & 1) * 16; oR1 = oR + 1152; }
        if (m >= 12) { oR = WS_USED; oR1 = WS_USED; }
    }
    const unsigned rsc = __float_as_uint(m < 8 ? QSCALE : 1.f);
    u32x8 ea = {oF, oV, oR, oR1, selLo, selHi, rsc, rsc};
    f32x16 acc[7];
    unsigned cnt, ph, dl;
    asm volatile(
        ".macro MFK a, w, x\n"
        ".if %[isb]\n v_mfma_f32_16x16x32_bf16 v[\\a:\\a+3], v[\\x:\\x+3], v[\\w:\\w+3], v[\\a:\\a+3]\n"
        ".else\n v_mfma_f32_16x16x32_bf16 v[\\a:\\a+3], v[\\w:\\w+3], v[\\x:\\x+3], v[\\a:\\a+3]\n .endif\n .endm\n"
        ".set r_, 40\n .rept 56\n v_mov_b64 v[r_:r_+1], 0\n .set r_, r_+2\n .endr\n"
        "s_mov_b32 %[c], 8\n s_mov_b32 %[p], 0\n"
        "s_waitcnt lgkmcnt(0)\n s_barrier\n"
        P1_PRO
        "1:\n"
        P1_BODY
        "s_sub_u32 %[c], %[c], 1\n s_cmp_lg_u32 %[c], 0\n s_cbranch_scc1 1b\n"
        ".if %[ty] == 0\n" P1_TAIL_Q ".elseif %[ty] == 1\n" P1_TAIL_K ".else\n" P1_TAIL "s_nop 5\n .endif\n s_nop 1\n"
        ".purgem MFK\n"
        : "={v[40:55]}"(acc[0]), "={v[56:71]}"(acc[1]), "={v[72:87]}"(acc[2]), "={v[88:103]}"(acc[3]), "={v[104:119]}"(acc[4]),
          "={v[120:135]}"(acc[5]), "={v[136:151]}"(acc[6]), "+{v212}"(aX0), "+{v213}"(aX1), "+{v214}"(aW), "+{v215}"(aR), "+{v[232:239]}"(ea),
          [c] "=&s"(cnt), [p] "=&s"(ph), [d] "=&s"(dl)
        : [neg] "s"(-2 * (int)ST1), [isb] "i"(ISB ? 1 : 0), [ty] "i"(TY), [rs] "s"(rs), [qs] "s"((f32x2){QSCALE, QSCALE}),
          [mo] "s"(0xaaaaaaaaaaaaaaaaull), [m8] "s"(0xff00ff00ff00ff00ull)
        : "memory", "scc", "v152", "v153", "v154", "v155", "v156", "v157", "v158", "v159", "v160", "v161", "v162", "v163", "v164", "v165",
          "v166", "v167", "v168", "v169", "v170", "v171", "v172", "v173", "v174", "v175", "v176", "v177", "v178", "v179", "v180", "v181",
          "v182", "v183", "v184", "v185", "v186", "v187", "v188", "v189", "v190", "v191", "v192", "v193", "v194", "v195", "v196", "v197",
          "v198", "v199", "v200", "v201", "v202", "v203", "v204", "v205", "v206", "v207", "v208", "v209", "v210", "v211", "v216", "v217",
          "v218", "v219", "v220", "v221", "v222", "v223", "v224", "v225", "v226", "v227", "v228", "v229", "v230", "v231");
    if (!ISB) {
        if (TY == 0) return 0;
        unsigned kn0 = 0, kn1 = 0, rm = 0;
#pragma unroll
        for (int tl = 0; tl < 2; tl++) {
            float nn[4];
#pragma unroll
            for (int jj = 0; jj < 4; jj++) nn[jj] = nrm8(__builtin_bit_cast(u32x4, sub4(acc[jj], tl)));
            kn0 = umax(kn0, __float_as_uint(swap32_add(nn[0], nn[1]))); kn1 = umax(kn1, __float_as_uint(swap32_add(nn[2], nn[3])));
#pragma unroll
            for (int i = 0; i < 4; i++) {
                const unsigned pr2 = __float_as_uint(acc[6][8 * tl + 4 + i]);
                rm = umax(rm, __float_as_uint(dot2(pr2, pr2, 0.f)));
            }
        }
        kn0 = swap16_umax(row_umax(kn0)); kn1 = swap16_umax(row_umax(kn1));
        rm = swap32_umax(swap16_umax(umax(rm, dppu<0xB1>(rm))));
        unsigned kb = (__float_as_uint(__uint_as_float(m & 4 ? kn1 : kn0) + __uint_as_float(rm)) + 0x7ffffu) >> 19;
        kb = kb > 0xfffu ? 0xfffu : kb;
        const unsigned p0 = __builtin_amdgcn_readlane(kb, 0) | (__builtin_amdgcn_readlane(kb, 34) << 12);
        const unsigned p1 = __builtin_amdgcn_readlane(kb, 4) | (__builtin_amdgcn_readlane(kb, 38) << 12);
        return odd ? p1 : p0;
    } else {
        oF = ea[0]; oR = ea[2]; oR1 = ea[3]; selLo = ea[4]; selHi = ea[5];
        f32x4 M0 = {0, 0, 0, 0}, M1 = M0, M2 = M0, M3 = M0, M4 = M0, M5 = M0, M6 = M0;
#pragma unroll
        for (int tl = 0; tl < 2; tl++) {
            const f32x4 r0 = sub4(acc[6], 2 * tl), r1 = sub4(acc[6], 2 * tl + 1), p0 = phi4(r0), p1 = phi4(r1);
            const bf16x8 k0 = __builtin_bit_cast(bf16x8, tm_pack(phi4(sub4(acc[2], 2 * tl)), phi4(sub4(acc[2], 2 * tl + 1))));
            const bf16x8 k1 = __builtin_bit_cast(bf16x8, tm_pack(phi4(sub4(acc[3], 2 * tl)), phi4(sub4(acc[3], 2 * tl + 1))));
            const u32x4 kru = tm_pack(p0, p1);
            const bf16x8 kr = __builtin_bit_cast(bf16x8, kru);
            const bf16x8 v0 = __builtin_bit_cast(bf16x8, tm_pack(sub4(acc[4], 2 * tl), sub4(acc[4], 2 * tl + 1)));
            const bf16x8 v1 = __builtin_bit_cast(bf16x8, tm_pack(sub4(acc[5], 2 * tl), sub4(acc[5], 2 * tl + 1)));
            u32x4 vru = tm_pack(r0, r1);
            st128(rs, tl ? oR1 : oR, pair_up(kru, odd, selLo, selHi));
            if (m >= 12) vru = (u32x4){ONES, ONES, ONES, ONES};
            const bf16x8 vr = __builtin_bit_cast(bf16x8, vru);
            M0 = mfma16(k0, v0, M0); M4 = mfma16(k1, v1, M4); M1 = mfma16(k0, vr, M1); M5 = mfma16(k1, vr, M5);
            M2 = mfma16(kr, v0, M2); M6 = mfma16(kr, v1, M6); M3 = mfma16(kr, vr, M3);
        }
        const bool h1c = m == 10 || m == 11 || m == 13;
        f32x4 A2 = h1c ? M5 : M1, A3 = {M2[0], M2[1], M6[2], M6[3]};
        LAS f32x4* red = lds<f32x4>(L_RED1) + pr * 320 + lane;
        volatile LAS unsigned* hs = lds<unsigned>(L_FLAG + 4 + 4 * pr);
        if (rh) {
            red[0] = M0; red[64] = M4; red[128] = A2; red[192] = A3; red[256] = M3;
            asm volatile("s_waitcnt lgkmcnt(0)" ::: "memory");
            if (lane == 0) *hs = 1;
        }
#pragma unroll
        for (int tl = 0; tl < 2; tl++)
#pragma unroll
            for (int jj = 0; jj < 2; jj++) st128(rs, oF + tl * 1024 + jj * 65536, fm_pack(phi4(sub4(acc[jj], 2 * tl)), phi4(sub4(acc[jj], 2 * tl + 1))));
        if (!rh) {
            while (*hs == 0) __builtin_amdgcn_s_sleep(1);
            asm volatile("" ::: "memory");
            M0 += red[0]; M4 += red[64]; A2 += red[128]; A3 += red[192]; M3 += red[256];
            const unsigned kb = OFF_KVP + (h0 * 16 + r) * 1408, e = m >= 12 ? 18 : 16 + (m & 1);
            const bool ex = m >= 8 && m < 14;
            const unsigned kbx = kb + (h1c ? 16 * 1408 : 0);
            st128(rs, kb + m * 64 + g * 16, __builtin_bit_cast(u32x4, M0));
            st128(rs, kb + 16 * 1408 + m * 64 + g * 16, __builtin_bit_cast(u32x4, M4));
            st128(rs, ex ? kbx + e * 64 + g * 16 : WS_USED, __builtin_bit_cast(u32x4, A2));
            const u32x4 a3 = __builtin_bit_cast(u32x4, A3), m3 = __builtin_bit_cast(u32x4, M3);
            st64(rs, g == 1 ? kb + 1216 + m * 8 : WS_USED, (u32x2){a3[0], a3[1]});
            st64(rs, g == 1 ? kb + 16 * 1408 + 1216 + m * 8 : WS_USED, (u32x2){a3[2], a3[3]});
            st64(rs, g == 1 && ex ? kbx + 1216 + e * 8 : WS_USED, h1c ? (u32x2){m3[2], m3[3]} : (u32x2){m3[0], m3[1]});
        }
        return 0;
    }
}

#define ATT_O0 \
    "ds_read_b32 v174, v187\nds_read_b128 v[170:173], v186\ns_waitcnt lgkmcnt(1)\nv_mfma_f32_32x32x4_2b_bf16 v[64:95], v[174:175], v[168:169], 0 cbsz:1\n" \
    "ds_read_b32 v174, v187 offset:128\ns_waitcnt lgkmcnt(1)\ns_add_u32 m0, %[m2], 18432\ns_nop 0\nglobal_load_lds_dwordx4 v190, %[sc] sc1\n" \
    "global_load_lds_dwordx4 v190, %[sc] offset:1024 sc1\nglobal_load_lds_dwordx4 v190, %[sc] offset:2048 sc1\ns_nop 10\n" \
    "v_mfma_f32_32x32x16_bf16 v[64:79], v[170:173], v[160:163], v[64:79]\nv_mfma_f32_32x32x16_bf16 v[80:95], v[170:173], v[164:167], v[80:95]\n" \
    "ds_read_b128 v[170:173], v186 offset:1024\ns_mov_b64 exec, %[vm]\nds_read_b128 v[176:179], v188\ns_mov_b64 exec, -1\ns_nop 7\ns_setprio 1\n" \
    "v_exp_f32 v64, v64\nv_exp_f32 v65, v65\nv_smfmac_f32_32x32x32_bf16 v[144:159], v[180:183], v[120:127], v184\ns_mov_b64 exec, %[vm]\n" \
    "ds_read_b128 v[180:183], v189 offset:0\ns_mov_b64 exec, -1\nv_exp_f32 v66, v66\nv_exp_f32 v67, v67\nv_exp_f32 v68, v68\ns_waitcnt lgkmcnt(3)\n" \
    "v_mfma_f32_32x32x4_2b_bf16 v[96:127], v[174:175], v[168:169], 0 cbsz:1\nds_read_b32 v174, v187 offset:256\nv_exp_f32 v69, v69\nv_exp_f32 v70, v70\n" \
    "v_exp_f32 v71, v71\nv_exp_f32 v72, v72\nv_exp_f32 v73, v73\nv_exp_f32 v74, v74\nv_exp_f32 v75, v75\nglobal_load_dwordx2 v[192:193], %[pa], %[pb] sc1\n" \
    "v_exp_f32 v76, v76\ns_waitcnt lgkmcnt(2)\nv_smfmac_f32_32x32x32_bf16 v[128:143], v[176:179], v[64:71], v184\nv_exp_f32 v77, v77\nv_exp_f32 v78, v78\n" \
    "v_exp_f32 v79, v79\nv_exp_f32 v80, v80\nv_exp_f32 v81, v81\ns_waitcnt lgkmcnt(1)\nv_smfmac_f32_32x32x32_bf16 v[128:143], v[180:183], v[72:79], v184\n" \
    "v_exp_f32 v82, v82\nv_exp_f32 v83, v83\nv_exp_f32 v84, v84\nv_mfma_f32_32x32x16_bf16 v[96:111], v[170:173], v[160:163], v[96:111]\nv_exp_f32 v85, v85\n" \
    "v_exp_f32 v86, v86\nv_exp_f32 v87, v87\nv_exp_f32 v88, v88\nv_mfma_f32_32x32x16_bf16 v[112:127], v[170:173], v[164:167], v[112:127]\n" \
    "ds_read_b128 v[170:173], v186 offset:2048\nv_exp_f32 v89, v89\nv_exp_f32 v90, v90\nv_exp_f32 v91, v91\n" \
    "v_smfmac_f32_32x32x32_bf16 v[144:159], v[176:179], v[80:87], v184\ns_mov_b64 exec, %[vm]\nds_read_b128 v[176:179], v188 offset:1152\n" \
    "s_mov_b64 exec, -1\nv_exp_f32 v92, v92\nv_exp_f32 v93, v93\nv_exp_f32 v94, v94\nv_exp_f32 v95, v95\ns_setprio 0\nv_exp_f32 v96, v96\n" \
    "v_exp_f32 v97, v97\nv_smfmac_f32_32x32x32_bf16 v[144:159], v[180:183], v[88:95], v184\ns_mov_b64 exec, %[vm]\n" \
    "ds_read_b128 v[180:183], v189 offset:1152\ns_mov_b64 exec, -1\nv_exp_f32 v98, v98\nv_exp_f32 v99, v99\nv_exp_f32 v100, v100\ns_waitcnt lgkmcnt(3)\n" \
    "v_mfma_f32_32x32x4_2b_bf16 v[64:95], v[174:175], v[168:169], 0 cbsz:1\nds_read_b32 v174, v187 offset:384\nv_exp_f32 v101, v101\nv_exp_f32 v102, v102\n" \
    "v_exp_f32 v103, v103\nv_exp_f32 v104, v104\nv_exp_f32 v105, v105\nv_exp_f32 v106, v106\nv_exp_f32 v107, v107\ns_waitcnt vmcnt(1)\ns_mov_b64 exec, 1\n" \
    "ds_add_u32 v191, v191\ns_mov_b64 exec, -1\nv_exp_f32 v108, v108\ns_waitcnt lgkmcnt(3)\n" \
    "v_smfmac_f32_32x32x32_bf16 v[128:143], v[176:179], v[96:103], v184\nv_exp_f32 v109, v109\nv_exp_f32 v110, v110\nv_exp_f32 v111, v111\n" \
    "v_exp_f32 v112, v112\nv_exp_f32 v113, v113\ns_waitcnt lgkmcnt(2)\nv_smfmac_f32_32x32x32_bf16 v[128:143], v[180:183], v[104:111], v184\n" \
    "v_exp_f32 v114, v114\nv_exp_f32 v115, v115\nv_exp_f32 v116, v116\nv_mfma_f32_32x32x16_bf16 v[64:79], v[170:173], v[160:163], v[64:79]\n" \
    "ds_read_b32 v185, v191\nv_exp_f32 v117, v117\nv_exp_f32 v118, v118\nv_exp_f32 v119, v119\nv_exp_f32 v120, v120\n" \
    "v_mfma_f32_32x32x16_bf16 v[80:95], v[170:173], v[164:167], v[80:95]\nds_read_b128 v[170:173], v186 offset:3072\nv_exp_f32 v121, v121\n" \
    "v_exp_f32 v122, v122\nv_exp_f32 v123, v123\nv_smfmac_f32_32x32x32_bf16 v[144:159], v[176:179], v[112:119], v184\ns_mov_b64 exec, %[vm]\n" \
    "ds_read_b128 v[176:179], v188 offset:2304\ns_mov_b64 exec, -1\nv_exp_f32 v124, v124\nv_exp_f32 v125, v125\nglobal_load_dword v255, %[to], %[tw]\n" \
    "v_exp_f32 v126, v126\nv_exp_f32 v127, v127\ns_waitcnt lgkmcnt(2)\nv_readfirstlane_b32 s94, v185\ns_cmp_ge_u32 s94, 1308800\ns_cbranch_scc1 4f\n3:\n" \
    "s_sleep 1\nds_read_b32 v185, v191\ns_waitcnt lgkmcnt(0)\nv_readfirstlane_b32 s94, v185\ns_cmp_lt_u32 s94, 1308800\ns_cbranch_scc1 3b\n4:\n" \
    "s_waitcnt vmcnt(1)\ns_setprio 3\n"
#define ATT_O1 \
    "s_setprio 3\nv_exp_f32 v64, v64\nv_exp_f32 v65, v65\nv_smfmac_f32_32x32x32_bf16 v[144:159], v[180:183], v[120:127], v184\ns_mov_b64 exec, %[vm]\n" \
    "ds_read_b128 v[180:183], v189 offset:2304\ns_mov_b64 exec, -1\nv_exp_f32 v66, v66\nv_exp_f32 v67, v67\nv_exp_f32 v68, v68\n" \
    "v_mfma_f32_32x32x4_2b_bf16 v[96:127], v[174:175], v[168:169], 0 cbsz:1\nds_read_b32 v174, v187 offset:18432\nv_exp_f32 v69, v69\nv_exp_f32 v70, v70\n" \
    "buffer_load_dwordx4 v[192:195], v224, %[rs], 0 offen offset:0 sc1\nbuffer_load_dwordx4 v[196:199], v224, %[rs], 0 offen offset:2816 sc1\n" \
    "v_exp_f32 v71, v71\nv_exp_f32 v72, v72\nv_exp_f32 v73, v73\nv_exp_f32 v74, v74\nv_exp_f32 v75, v75\n" \
    "buffer_load_dwordx4 v[200:203], v225, %[rs], 0 offen offset:0 sc1\nbuffer_load_dwordx4 v[204:207], v225, %[rs], 0 offen offset:2816 sc1\n" \
    "v_exp_f32 v76, v76\ns_waitcnt lgkmcnt(2)\nv_smfmac_f32_32x32x32_bf16 v[128:143], v[176:179], v[64:71], v184\nv_exp_f32 v77, v77\nv_exp_f32 v78, v78\n" \
    "v_exp_f32 v79, v79\nv_exp_f32 v80, v80\nv_exp_f32 v81, v81\ns_waitcnt lgkmcnt(1)\nv_smfmac_f32_32x32x32_bf16 v[128:143], v[180:183], v[72:79], v184\n" \
    "v_exp_f32 v82, v82\nv_exp_f32 v83, v83\nbuffer_load_dwordx4 v[208:211], v226, %[rs], 0 offen offset:0 sc1\n" \
    "buffer_load_dwordx4 v[212:215], v226, %[rs], 0 offen offset:2816 sc1\nv_exp_f32 v84, v84\n" \
    "v_mfma_f32_32x32x16_bf16 v[96:111], v[170:173], v[160:163], v[96:111]\nv_exp_f32 v85, v85\nv_exp_f32 v86, v86\nv_exp_f32 v87, v87\nv_exp_f32 v88, v88\n" \
    "v_mfma_f32_32x32x16_bf16 v[112:127], v[170:173], v[164:167], v[112:127]\nds_read_b128 v[170:173], v186 offset:18432\nv_exp_f32 v89, v89\n" \
    "v_exp_f32 v90, v90\nv_exp_f32 v91, v91\nv_smfmac_f32_32x32x32_bf16 v[144:159], v[176:179], v[80:87], v184\ns_mov_b64 exec, %[vm]\n" \
    "ds_read_b128 v[176:179], v188 offset:3456\ns_mov_b64 exec, -1\nv_exp_f32 v92, v92\nv_exp_f32 v93, v93\n" \
    "buffer_load_dwordx4 v[216:219], v227, %[rs], 0 offen offset:0 sc1\nbuffer_load_dwordx4 v[220:223], v227, %[rs], 0 offen offset:2816 sc1\n" \
    "v_exp_f32 v94, v94\nv_exp_f32 v95, v95\ns_setprio 2\nv_exp_f32 v96, v96\nv_exp_f32 v97, v97\n" \
    "v_smfmac_f32_32x32x32_bf16 v[144:159], v[180:183], v[88:95], v184\ns_mov_b64 exec, %[vm]\nds_read_b128 v[180:183], v189 offset:3456\n" \
    "s_mov_b64 exec, -1\nv_exp_f32 v98, v98\nv_exp_f32 v99, v99\nv_exp_f32 v100, v100\ns_waitcnt lgkmcnt(3)\n" \
    "v_mfma_f32_32x32x4_2b_bf16 v[64:95], v[174:175], v[168:169], 0 cbsz:1\nds_read_b32 v174, v187 offset:18560\nv_exp_f32 v101, v101\n" \
    "s_add_u32 m0, %[m2], 36864\nv_exp_f32 v102, v102\nglobal_load_lds_dwordx4 v190, %[sc] sc1\nv_exp_f32 v103, v103\nv_exp_f32 v104, v104\n" \
    "v_exp_f32 v105, v105\nv_exp_f32 v106, v106\nv_exp_f32 v107, v107\nglobal_load_lds_dwordx4 v190, %[sc] offset:1024 sc1\nv_exp_f32 v108, v108\n" \
    "s_waitcnt lgkmcnt(2)\nv_smfmac_f32_32x32x32_bf16 v[128:143], v[176:179], v[96:103], v184\nv_exp_f32 v109, v109\nv_exp_f32 v110, v110\n" \
    "v_exp_f32 v111, v111\nv_exp_f32 v112, v112\nv_exp_f32 v113, v113\ns_waitcnt lgkmcnt(1)\n" \
    "v_smfmac_f32_32x32x32_bf16 v[128:143], v[180:183], v[104:111], v184\nv_exp_f32 v114, v114\nv_exp_f32 v115, v115\n" \
    "global_load_lds_dwordx4 v190, %[sc] offset:2048 sc1\nv_exp_f32 v116, v116\nv_mfma_f32_32x32x16_bf16 v[64:79], v[170:173], v[160:163], v[64:79]\n" \
    "v_exp_f32 v117, v117\nv_exp_f32 v118, v118\nv_exp_f32 v119, v119\nv_exp_f32 v120, v120\n" \
    "v_mfma_f32_32x32x16_bf16 v[80:95], v[170:173], v[164:167], v[80:95]\nds_read_b128 v[170:173], v186 offset:19456\nv_exp_f32 v121, v121\n" \
    "v_exp_f32 v122, v122\nv_exp_f32 v123, v123\nv_smfmac_f32_32x32x32_bf16 v[144:159], v[176:179], v[112:119], v184\ns_mov_b64 exec, %[vm]\n" \
    "ds_read_b128 v[176:179], v188 offset:18432\ns_mov_b64 exec, -1\nv_exp_f32 v124, v124\nv_exp_f32 v125, v125\nv_exp_f32 v126, v126\nv_exp_f32 v127, v127\n" \
    "s_setprio 1\nv_exp_f32 v64, v64\nv_exp_f32 v65, v65\nv_smfmac_f32_32x32x32_bf16 v[144:159], v[180:183], v[120:127], v184\ns_mov_b64 exec, %[vm]\n" \
    "ds_read_b128 v[180:183], v189 offset:18432\ns_mov_b64 exec, -1\nv_exp_f32 v66, v66\nv_exp_f32 v67, v67\nv_exp_f32 v68, v68\ns_waitcnt lgkmcnt(3)\n" \
    "v_mfma_f32_32x32x4_2b_bf16 v[96:127], v[174:175], v[168:169], 0 cbsz:1\nds_read_b32 v174, v187 offset:18688\nv_exp_f32 v69, v69\ns_waitcnt vmcnt(3)\n" \
    "v_pk_add_f32 v[192:193], v[192:193], v[196:197]\nv_exp_f32 v70, v70\nv_pk_add_f32 v[194:195], v[194:195], v[198:199]\nv_exp_f32 v71, v71\n" \
    "v_pk_add_f32 v[200:201], v[200:201], v[204:205]\nv_exp_f32 v72, v72\nv_pk_add_f32 v[202:203], v[202:203], v[206:207]\nv_exp_f32 v73, v73\n" \
    "v_pk_add_f32 v[208:209], v[208:209], v[212:213]\nv_exp_f32 v74, v74\nv_pk_add_f32 v[210:211], v[210:211], v[214:215]\nv_exp_f32 v75, v75\n" \
    "v_pk_add_f32 v[216:217], v[216:217], v[220:221]\nv_exp_f32 v76, v76\ns_waitcnt lgkmcnt(2)\n" \
    "v_smfmac_f32_32x32x32_bf16 v[128:143], v[176:179], v[64:71], v184\nv_pk_add_f32 v[218:219], v[218:219], v[222:223]\nv_exp_f32 v77, v77\n" \
    "v_pk_add_f32 v[192:193], v[192:193], v[200:201]\nv_exp_f32 v78, v78\nv_pk_add_f32 v[194:195], v[194:195], v[202:203]\nv_exp_f32 v79, v79\n" \
    "v_pk_add_f32 v[208:209], v[208:209], v[216:217]\nv_exp_f32 v80, v80\nv_pk_add_f32 v[210:211], v[210:211], v[218:219]\nv_exp_f32 v81, v81\n" \
    "s_waitcnt lgkmcnt(1)\nv_smfmac_f32_32x32x32_bf16 v[128:143], v[180:183], v[72:79], v184\nv_pk_add_f32 v[192:193], v[192:193], v[208:209]\n" \
    "v_exp_f32 v82, v82\nv_pk_add_f32 v[194:195], v[194:195], v[210:211]\nv_exp_f32 v83, v83\nv_exp_f32 v84, v84\n" \
    "v_mfma_f32_32x32x16_bf16 v[96:111], v[170:173], v[160:163], v[96:111]\nv_exp_f32 v85, v85\nv_permlane32_swap_b32 v192, v194\nv_exp_f32 v86, v86\n" \
    "v_permlane32_swap_b32 v193, v195\nv_exp_f32 v87, v87\nv_pk_add_f32 v[192:193], v[192:193], v[194:195]\nv_exp_f32 v88, v88\n" \
    "v_mfma_f32_32x32x16_bf16 v[112:127], v[170:173], v[164:167], v[112:127]\nds_read_b128 v[170:173], v186 offset:20480\n" \
    "v_cvt_pk_bf16_f32 v192, v192, v193\nv_exp_f32 v89, v89\ns_mov_b64 exec, %[km]\nds_write_b32 v228, v192\ns_mov_b64 exec, -1\nv_exp_f32 v90, v90\n" \
    "v_exp_f32 v91, v91\nv_smfmac_f32_32x32x32_bf16 v[144:159], v[176:179], v[80:87], v184\ns_mov_b64 exec, %[vm]\n" \
    "ds_read_b128 v[176:179], v188 offset:19584\ns_mov_b64 exec, -1\nv_exp_f32 v92, v92\nv_exp_f32 v93, v93\nv_exp_f32 v94, v94\nv_exp_f32 v95, v95\n" \
    "s_waitcnt vmcnt(0)\ns_mov_b64 exec, 1\nds_add_u32 v191, v191\ns_mov_b64 exec, -1\ns_setprio 0\nv_exp_f32 v96, v96\nv_exp_f32 v97, v97\n" \
    "v_smfmac_f32_32x32x32_bf16 v[144:159], v[180:183], v[88:95], v184\ns_mov_b64 exec, %[vm]\nds_read_b128 v[180:183], v189 offset:19584\n" \
    "s_mov_b64 exec, -1\nv_exp_f32 v98, v98\nv_exp_f32 v99, v99\nv_exp_f32 v100, v100\ns_waitcnt lgkmcnt(5)\n" \
    "v_mfma_f32_32x32x4_2b_bf16 v[64:95], v[174:175], v[168:169], 0 cbsz:1\nds_read_b32 v174, v187 offset:18816\nv_exp_f32 v101, v101\nv_exp_f32 v102, v102\n" \
    "v_exp_f32 v103, v103\nv_exp_f32 v104, v104\nv_exp_f32 v105, v105\nv_exp_f32 v106, v106\nv_exp_f32 v107, v107\nv_exp_f32 v108, v108\n" \
    "s_waitcnt lgkmcnt(3)\nv_smfmac_f32_32x32x32_bf16 v[128:143], v[176:179], v[96:103], v184\nv_exp_f32 v109, v109\nv_exp_f32 v110, v110\n" \
    "v_exp_f32 v111, v111\nv_exp_f32 v112, v112\nv_exp_f32 v113, v113\ns_waitcnt lgkmcnt(1)\n" \
    "v_smfmac_f32_32x32x32_bf16 v[128:143], v[180:183], v[104:111], v184\nv_exp_f32 v114, v114\nv_exp_f32 v115, v115\nv_exp_f32 v116, v116\n" \
    "v_mfma_f32_32x32x16_bf16 v[64:79], v[170:173], v[160:163], v[64:79]\nds_read_b32 v185, v191\nv_exp_f32 v117, v117\nv_exp_f32 v118, v118\n" \
    "v_exp_f32 v119, v119\nv_exp_f32 v120, v120\nv_mfma_f32_32x32x16_bf16 v[80:95], v[170:173], v[164:167], v[80:95]\n" \
    "ds_read_b128 v[170:173], v186 offset:21504\nv_exp_f32 v121, v121\nv_exp_f32 v122, v122\nv_exp_f32 v123, v123\n" \
    "v_smfmac_f32_32x32x32_bf16 v[144:159], v[176:179], v[112:119], v184\ns_mov_b64 exec, %[vm]\nds_read_b128 v[176:179], v188 offset:20736\n" \
    "s_mov_b64 exec, -1\nv_exp_f32 v124, v124\nv_exp_f32 v125, v125\nv_exp_f32 v126, v126\nv_exp_f32 v127, v127\ns_waitcnt lgkmcnt(2)\n" \
    "v_readfirstlane_b32 s94, v185\ns_cmp_ge_u32 s94, 2617600\ns_cbranch_scc1 4f\n3:\ns_sleep 1\nds_read_b32 v185, v191\ns_waitcnt lgkmcnt(0)\n" \
    "v_readfirstlane_b32 s94, v185\ns_cmp_lt_u32 s94, 2617600\ns_cbranch_scc1 3b\n4:\n"
#define ATT_O2 \
    "s_setprio 3\nv_exp_f32 v64, v64\nv_exp_f32 v65, v65\nv_smfmac_f32_32x32x32_bf16 v[144:159], v[180:183], v[120:127], v184\ns_mov_b64 exec, %[vm]\n" \
    "ds_read_b128 v[180:183], v189 offset:20736\ns_mov_b64 exec, -1\nv_exp_f32 v66, v66\nv_exp_f32 v67, v67\nv_exp_f32 v68, v68\n" \
    "v_mfma_f32_32x32x4_2b_bf16 v[96:127], v[174:175], v[168:169], 0 cbsz:1\nds_read_b32 v174, v187 offset:36864\nv_exp_f32 v69, v69\n" \
    "s_add_u32 m0, %[m2], 0\nv_exp_f32 v70, v70\nglobal_load_lds_dwordx4 v190, %[sc] sc1\nv_exp_f32 v71, v71\nv_exp_f32 v72, v72\nv_exp_f32 v73, v73\n" \
    "v_exp_f32 v74, v74\nv_exp_f32 v75, v75\nglobal_load_lds_dwordx4 v190, %[sc] offset:1024 sc1\nv_exp_f32 v76, v76\ns_waitcnt lgkmcnt(2)\n" \
    "v_smfmac_f32_32x32x32_bf16 v[128:143], v[176:179], v[64:71], v184\nv_exp_f32 v77, v77\nv_exp_f32 v78, v78\nv_exp_f32 v79, v79\nv_exp_f32 v80, v80\n" \
    "v_exp_f32 v81, v81\ns_waitcnt lgkmcnt(1)\nv_smfmac_f32_32x32x32_bf16 v[128:143], v[180:183], v[72:79], v184\nv_exp_f32 v82, v82\nv_exp_f32 v83, v83\n" \
    "global_load_lds_dwordx4 v190, %[sc] offset:2048 sc1\nv_exp_f32 v84, v84\nv_mfma_f32_32x32x16_bf16 v[96:111], v[170:173], v[160:163], v[96:111]\n" \
    "v_exp_f32 v85, v85\nv_exp_f32 v86, v86\nv_exp_f32 v87, v87\nv_exp_f32 v88, v88\n" \
    "v_mfma_f32_32x32x16_bf16 v[112:127], v[170:173], v[164:167], v[112:127]\nds_read_b128 v[170:173], v186 offset:36864\nv_exp_f32 v89, v89\n" \
    "v_exp_f32 v90, v90\nv_exp_f32 v91, v91\nv_smfmac_f32_32x32x32_bf16 v[144:159], v[176:179], v[80:87], v184\ns_mov_b64 exec, %[vm]\n" \
    "ds_read_b128 v[176:179], v188 offset:21888\ns_mov_b64 exec, -1\nv_exp_f32 v92, v92\nv_exp_f32 v93, v93\nv_exp_f32 v94, v94\nv_exp_f32 v95, v95\n" \
    "s_setprio 2\nv_exp_f32 v96, v96\nv_exp_f32 v97, v97\nv_smfmac_f32_32x32x32_bf16 v[144:159], v[180:183], v[88:95], v184\ns_mov_b64 exec, %[vm]\n" \
    "ds_read_b128 v[180:183], v189 offset:21888\ns_mov_b64 exec, -1\nv_exp_f32 v98, v98\nv_exp_f32 v99, v99\nv_exp_f32 v100, v100\ns_waitcnt lgkmcnt(3)\n" \
    "v_mfma_f32_32x32x4_2b_bf16 v[64:95], v[174:175], v[168:169], 0 cbsz:1\nds_read_b32 v174, v187 offset:36992\nv_exp_f32 v101, v101\n" \
    "s_add_u32 s98, s96, 0\ns_addc_u32 s99, s97, 0\ns_add_u32 m0, %[m1], 55296\nv_exp_f32 v102, v102\nglobal_load_lds_dwordx4 %[w0], s[98:99]\n" \
    "v_exp_f32 v103, v103\nv_exp_f32 v104, v104\nv_exp_f32 v105, v105\nv_exp_f32 v106, v106\ns_add_u32 s98, s96, 82944\ns_addc_u32 s99, s97, 0\n" \
    "s_add_u32 m0, %[m1], 64512\nv_exp_f32 v107, v107\nglobal_load_lds_dwordx4 %[w0], s[98:99]\nv_exp_f32 v108, v108\ns_waitcnt lgkmcnt(2)\n" \
    "v_smfmac_f32_32x32x32_bf16 v[128:143], v[176:179], v[96:103], v184\nv_exp_f32 v109, v109\nv_exp_f32 v110, v110\nv_exp_f32 v111, v111\n" \
    "v_exp_f32 v112, v112\nv_exp_f32 v113, v113\ns_waitcnt lgkmcnt(1)\nv_smfmac_f32_32x32x32_bf16 v[128:143], v[180:183], v[104:111], v184\n" \
    "v_exp_f32 v114, v114\ns_add_u32 s98, s96, 165888\ns_addc_u32 s99, s97, 0\ns_add_u32 m0, %[m1], 73728\nv_exp_f32 v115, v115\n" \
    "global_load_lds_dwordx4 %[w0], s[98:99]\nv_exp_f32 v116, v116\nv_mfma_f32_32x32x16_bf16 v[64:79], v[170:173], v[160:163], v[64:79]\n" \
    "v_exp_f32 v117, v117\nv_exp_f32 v118, v118\nv_exp_f32 v119, v119\nv_exp_f32 v120, v120\n" \
    "v_mfma_f32_32x32x16_bf16 v[80:95], v[170:173], v[164:167], v[80:95]\nds_read_b128 v[170:173], v186 offset:37888\nv_exp_f32 v121, v121\n" \
    "v_exp_f32 v122, v122\nv_exp_f32 v123, v123\nv_smfmac_f32_32x32x32_bf16 v[144:159], v[176:179], v[112:119], v184\ns_mov_b64 exec, %[vm]\n" \
    "ds_read_b128 v[176:179], v188 offset:36864\ns_mov_b64 exec, -1\nv_exp_f32 v124, v124\ns_add_u32 s98, s96, 248832\ns_addc_u32 s99, s97, 0\n" \
    "s_add_u32 m0, %[m1], 82944\nv_exp_f32 v125, v125\nglobal_load_lds_dwordx4 %[w0], s[98:99]\nv_exp_f32 v126, v126\nv_exp_f32 v127, v127\ns_setprio 1\n" \
    "v_exp_f32 v64, v64\nv_exp_f32 v65, v65\nv_smfmac_f32_32x32x32_bf16 v[144:159], v[180:183], v[120:127], v184\ns_mov_b64 exec, %[vm]\n" \
    "ds_read_b128 v[180:183], v189 offset:36864\ns_mov_b64 exec, -1\nv_exp_f32 v66, v66\nv_exp_f32 v67, v67\nv_exp_f32 v68, v68\ns_waitcnt lgkmcnt(3)\n" \
    "v_mfma_f32_32x32x4_2b_bf16 v[96:127], v[174:175], v[168:169], 0 cbsz:1\nds_read_b32 v174, v187 offset:37120\nv_exp_f32 v69, v69\n" \
    "s_add_u32 s98, s96, 331776\ns_addc_u32 s99, s97, 0\ns_add_u32 m0, %[m1], 92160\nv_exp_f32 v70, v70\nglobal_load_lds_dwordx4 %[w0], s[98:99]\n" \
    "v_exp_f32 v71, v71\nv_exp_f32 v72, v72\nv_exp_f32 v73, v73\nv_exp_f32 v74, v74\nv_exp_f32 v75, v75\nv_exp_f32 v76, v76\ns_waitcnt lgkmcnt(2)\n" \
    "v_smfmac_f32_32x32x32_bf16 v[128:143], v[176:179], v[64:71], v184\nv_exp_f32 v77, v77\nv_exp_f32 v78, v78\nv_exp_f32 v79, v79\nv_exp_f32 v80, v80\n" \
    "v_exp_f32 v81, v81\ns_waitcnt lgkmcnt(1)\nv_smfmac_f32_32x32x32_bf16 v[128:143], v[180:183], v[72:79], v184\nv_exp_f32 v82, v82\ns_mov_b32 m0, %[mr]\n" \
    "v_exp_f32 v83, v83\nglobal_load_lds_dwordx4 %[w2], s[96:97]\nv_exp_f32 v84, v84\nv_mfma_f32_32x32x16_bf16 v[96:111], v[170:173], v[160:163], v[96:111]\n" \
    "v_exp_f32 v85, v85\nv_exp_f32 v86, v86\nv_exp_f32 v87, v87\nv_exp_f32 v88, v88\n" \
    "v_mfma_f32_32x32x16_bf16 v[112:127], v[170:173], v[164:167], v[112:127]\nds_read_b128 v[170:173], v186 offset:38912\nv_exp_f32 v89, v89\n" \
    "v_exp_f32 v90, v90\nv_exp_f32 v91, v91\nv_smfmac_f32_32x32x32_bf16 v[144:159], v[176:179], v[80:87], v184\ns_mov_b64 exec, %[vm]\n" \
    "ds_read_b128 v[176:179], v188 offset:38016\ns_mov_b64 exec, -1\nv_exp_f32 v92, v92\nv_exp_f32 v93, v93\nv_exp_f32 v94, v94\nv_exp_f32 v95, v95\n" \
    "s_waitcnt vmcnt(6)\ns_mov_b64 exec, 1\nds_add_u32 v191, v191\ns_mov_b64 exec, -1\ns_setprio 0\nv_exp_f32 v96, v96\nv_exp_f32 v97, v97\n" \
    "v_smfmac_f32_32x32x32_bf16 v[144:159], v[180:183], v[88:95], v184\ns_mov_b64 exec, %[vm]\nds_read_b128 v[180:183], v189 offset:38016\n" \
    "s_mov_b64 exec, -1\nv_exp_f32 v98, v98\nv_exp_f32 v99, v99\nv_exp_f32 v100, v100\ns_waitcnt lgkmcnt(4)\n" \
    "v_mfma_f32_32x32x4_2b_bf16 v[64:95], v[174:175], v[168:169], 0 cbsz:1\nds_read_b32 v174, v187 offset:37248\nv_exp_f32 v101, v101\nv_exp_f32 v102, v102\n" \
    "v_exp_f32 v103, v103\nv_exp_f32 v104, v104\nv_exp_f32 v105, v105\nv_exp_f32 v106, v106\nv_exp_f32 v107, v107\nv_exp_f32 v108, v108\n" \
    "s_waitcnt lgkmcnt(3)\nv_smfmac_f32_32x32x32_bf16 v[128:143], v[176:179], v[96:103], v184\nv_exp_f32 v109, v109\nv_exp_f32 v110, v110\n" \
    "v_exp_f32 v111, v111\nv_exp_f32 v112, v112\nv_exp_f32 v113, v113\ns_waitcnt lgkmcnt(1)\n" \
    "v_smfmac_f32_32x32x32_bf16 v[128:143], v[180:183], v[104:111], v184\nv_exp_f32 v114, v114\nv_exp_f32 v115, v115\nv_exp_f32 v116, v116\n" \
    "v_mfma_f32_32x32x16_bf16 v[64:79], v[170:173], v[160:163], v[64:79]\nds_read_b32 v185, v191\nv_exp_f32 v117, v117\nv_exp_f32 v118, v118\n" \
    "v_exp_f32 v119, v119\nv_exp_f32 v120, v120\nv_mfma_f32_32x32x16_bf16 v[80:95], v[170:173], v[164:167], v[80:95]\n" \
    "ds_read_b128 v[170:173], v186 offset:39936\nv_exp_f32 v121, v121\nv_exp_f32 v122, v122\nv_exp_f32 v123, v123\n" \
    "v_smfmac_f32_32x32x32_bf16 v[144:159], v[176:179], v[112:119], v184\ns_mov_b64 exec, %[vm]\nds_read_b128 v[176:179], v188 offset:39168\n" \
    "s_mov_b64 exec, -1\nv_exp_f32 v124, v124\nv_exp_f32 v125, v125\nv_exp_f32 v126, v126\nv_exp_f32 v127, v127\ns_waitcnt lgkmcnt(2)\n" \
    "v_readfirstlane_b32 s94, v185\ns_cmp_ge_u32 s94, 3926400\ns_cbranch_scc1 4f\n3:\ns_sleep 1\nds_read_b32 v185, v191\ns_waitcnt lgkmcnt(0)\n" \
    "v_readfirstlane_b32 s94, v185\ns_cmp_lt_u32 s94, 3926400\ns_cbranch_scc1 3b\n4:\n"
#define ATT_O3 \
    "s_setprio 3\nv_exp_f32 v64, v64\nv_exp_f32 v65, v65\nv_smfmac_f32_32x32x32_bf16 v[144:159], v[180:183], v[120:127], v184\ns_mov_b64 exec, %[vm]\n" \
    "ds_read_b128 v[180:183], v189 offset:39168\ns_mov_b64 exec, -1\nv_exp_f32 v66, v66\nv_exp_f32 v67, v67\nv_exp_f32 v68, v68\n" \
    "v_mfma_f32_32x32x4_2b_bf16 v[96:127], v[174:175], v[168:169], 0 cbsz:1\nds_read_b32 v174, v187 offset:0\nv_exp_f32 v69, v69\n" \
    "s_add_u32 m0, %[m2], 18432\nv_exp_f32 v70, v70\nglobal_load_lds_dwordx4 v190, %[sc] sc1\nv_exp_f32 v71, v71\nv_exp_f32 v72, v72\nv_exp_f32 v73, v73\n" \
    "v_exp_f32 v74, v74\nv_exp_f32 v75, v75\nglobal_load_lds_dwordx4 v190, %[sc] offset:1024 sc1\nv_exp_f32 v76, v76\ns_waitcnt lgkmcnt(2)\n" \
    "v_smfmac_f32_32x32x32_bf16 v[128:143], v[176:179], v[64:71], v184\nv_exp_f32 v77, v77\nv_exp_f32 v78, v78\nv_exp_f32 v79, v79\nv_exp_f32 v80, v80\n" \
    "v_exp_f32 v81, v81\ns_waitcnt lgkmcnt(1)\nv_smfmac_f32_32x32x32_bf16 v[128:143], v[180:183], v[72:79], v184\nv_exp_f32 v82, v82\nv_exp_f32 v83, v83\n" \
    "global_load_lds_dwordx4 v190, %[sc] offset:2048 sc1\nv_exp_f32 v84, v84\nv_mfma_f32_32x32x16_bf16 v[96:111], v[170:173], v[160:163], v[96:111]\n" \
    "v_exp_f32 v85, v85\nv_exp_f32 v86, v86\nv_exp_f32 v87, v87\nv_exp_f32 v88, v88\n" \
    "v_mfma_f32_32x32x16_bf16 v[112:127], v[170:173], v[164:167], v[112:127]\nds_read_b128 v[170:173], v186 offset:0\nv_exp_f32 v89, v89\n" \
    "v_exp_f32 v90, v90\nv_exp_f32 v91, v91\nv_smfmac_f32_32x32x32_bf16 v[144:159], v[176:179], v[80:87], v184\ns_mov_b64 exec, %[vm]\n" \
    "ds_read_b128 v[176:179], v188 offset:40320\ns_mov_b64 exec, -1\nv_exp_f32 v92, v92\nv_exp_f32 v93, v93\nv_exp_f32 v94, v94\nv_exp_f32 v95, v95\n" \
    "s_setprio 2\nv_exp_f32 v96, v96\nv_exp_f32 v97, v97\nv_smfmac_f32_32x32x32_bf16 v[144:159], v[180:183], v[88:95], v184\ns_mov_b64 exec, %[vm]\n" \
    "ds_read_b128 v[180:183], v189 offset:40320\ns_mov_b64 exec, -1\nv_exp_f32 v98, v98\nv_exp_f32 v99, v99\nv_exp_f32 v100, v100\ns_waitcnt lgkmcnt(3)\n" \
    "v_mfma_f32_32x32x4_2b_bf16 v[64:95], v[174:175], v[168:169], 0 cbsz:1\nds_read_b32 v174, v187 offset:128\nv_exp_f32 v101, v101\n" \
    "s_add_u32 s98, s96, 414720\ns_addc_u32 s99, s97, 0\ns_add_u32 m0, %[m1], 101376\nv_exp_f32 v102, v102\nglobal_load_lds_dwordx4 %[w0], s[98:99]\n" \
    "v_exp_f32 v103, v103\nv_exp_f32 v104, v104\nv_exp_f32 v105, v105\nv_exp_f32 v106, v106\ns_add_u32 s98, s96, 497664\ns_addc_u32 s99, s97, 0\n" \
    "s_add_u32 m0, %[m1], 110592\nv_exp_f32 v107, v107\nglobal_load_lds_dwordx4 %[w0], s[98:99]\nv_exp_f32 v108, v108\ns_waitcnt lgkmcnt(2)\n" \
    "v_smfmac_f32_32x32x32_bf16 v[128:143], v[176:179], v[96:103], v184\nv_exp_f32 v109, v109\nv_exp_f32 v110, v110\nv_exp_f32 v111, v111\n" \
    "v_exp_f32 v112, v112\nv_exp_f32 v113, v113\ns_waitcnt lgkmcnt(1)\nv_smfmac_f32_32x32x32_bf16 v[128:143], v[180:183], v[104:111], v184\n" \
    "v_exp_f32 v114, v114\ns_add_u32 s98, s96, 580608\ns_addc_u32 s99, s97, 0\ns_add_u32 m0, %[m1], 119808\nv_exp_f32 v115, v115\n" \
    "global_load_lds_dwordx4 %[w0], s[98:99]\nv_exp_f32 v116, v116\nv_mfma_f32_32x32x16_bf16 v[64:79], v[170:173], v[160:163], v[64:79]\n" \
    "v_exp_f32 v117, v117\nv_exp_f32 v118, v118\nv_exp_f32 v119, v119\nv_exp_f32 v120, v120\n" \
    "v_mfma_f32_32x32x16_bf16 v[80:95], v[170:173], v[164:167], v[80:95]\nds_read_b128 v[170:173], v186 offset:1024\nv_exp_f32 v121, v121\n" \
    "v_exp_f32 v122, v122\nv_exp_f32 v123, v123\nv_smfmac_f32_32x32x32_bf16 v[144:159], v[176:179], v[112:119], v184\ns_mov_b64 exec, %[vm]\n" \
    "ds_read_b128 v[176:179], v188 offset:0\ns_mov_b64 exec, -1\nv_exp_f32 v124, v124\ns_add_u32 m0, %[m1], 129024\nv_exp_f32 v125, v125\n" \
    "global_load_lds_dwordx4 %[w1], s[96:97]\nv_exp_f32 v126, v126\nv_exp_f32 v127, v127\ns_setprio 1\nv_exp_f32 v64, v64\nv_exp_f32 v65, v65\n" \
    "v_smfmac_f32_32x32x32_bf16 v[144:159], v[180:183], v[120:127], v184\ns_mov_b64 exec, %[vm]\nds_read_b128 v[180:183], v189 offset:0\ns_mov_b64 exec, -1\n" \
    "v_exp_f32 v66, v66\nv_exp_f32 v67, v67\nv_exp_f32 v68, v68\ns_waitcnt lgkmcnt(3)\n" \
    "v_mfma_f32_32x32x4_2b_bf16 v[96:127], v[174:175], v[168:169], 0 cbsz:1\nds_read_b32 v174, v187 offset:256\nv_exp_f32 v69, v69\nv_exp_f32 v70, v70\n" \
    "v_exp_f32 v71, v71\nv_exp_f32 v72, v72\nv_exp_f32 v73, v73\nv_exp_f32 v74, v74\nv_exp_f32 v75, v75\nv_exp_f32 v76, v76\ns_waitcnt lgkmcnt(2)\n" \
    "v_smfmac_f32_32x32x32_bf16 v[128:143], v[176:179], v[64:71], v184\nv_exp_f32 v77, v77\nv_exp_f32 v78, v78\nv_exp_f32 v79, v79\nv_exp_f32 v80, v80\n" \
    "v_exp_f32 v81, v81\ns_waitcnt lgkmcnt(1)\nv_smfmac_f32_32x32x32_bf16 v[128:143], v[180:183], v[72:79], v184\nv_exp_f32 v82, v82\nv_exp_f32 v83, v83\n" \
    "global_load_dwordx2 v[192:193], %[pa], %[pb] sc1\nv_exp_f32 v84, v84\nv_mfma_f32_32x32x16_bf16 v[96:111], v[170:173], v[160:163], v[96:111]\n" \
    "v_exp_f32 v85, v85\nv_exp_f32 v86, v86\nv_exp_f32 v87, v87\nv_exp_f32 v88, v88\n" \
    "v_mfma_f32_32x32x16_bf16 v[112:127], v[170:173], v[164:167], v[112:127]\nds_read_b128 v[170:173], v186 offset:2048\nv_exp_f32 v89, v89\n" \
    "v_exp_f32 v90, v90\nv_exp_f32 v91, v91\nv_smfmac_f32_32x32x32_bf16 v[144:159], v[176:179], v[80:87], v184\ns_mov_b64 exec, %[vm]\n" \
    "ds_read_b128 v[176:179], v188 offset:1152\ns_mov_b64 exec, -1\nv_exp_f32 v92, v92\nv_exp_f32 v93, v93\nv_exp_f32 v94, v94\nv_exp_f32 v95, v95\n" \
    "s_waitcnt vmcnt(5)\ns_mov_b64 exec, 1\nds_add_u32 v191, v191\ns_mov_b64 exec, -1\ns_setprio 0\nv_exp_f32 v96, v96\nv_exp_f32 v97, v97\n" \
    "v_smfmac_f32_32x32x32_bf16 v[144:159], v[180:183], v[88:95], v184\ns_mov_b64 exec, %[vm]\nds_read_b128 v[180:183], v189 offset:1152\n" \
    "s_mov_b64 exec, -1\nv_exp_f32 v98, v98\nv_exp_f32 v99, v99\nv_exp_f32 v100, v100\ns_waitcnt lgkmcnt(4)\n" \
    "v_mfma_f32_32x32x4_2b_bf16 v[64:95], v[174:175], v[168:169], 0 cbsz:1\nds_read_b32 v174, v187 offset:384\nv_exp_f32 v101, v101\nv_exp_f32 v102, v102\n" \
    "v_exp_f32 v103, v103\nv_exp_f32 v104, v104\nv_exp_f32 v105, v105\nv_exp_f32 v106, v106\nv_exp_f32 v107, v107\nv_exp_f32 v108, v108\n" \
    "s_waitcnt lgkmcnt(3)\nv_smfmac_f32_32x32x32_bf16 v[128:143], v[176:179], v[96:103], v184\nv_exp_f32 v109, v109\nv_exp_f32 v110, v110\n" \
    "v_exp_f32 v111, v111\nv_exp_f32 v112, v112\nv_exp_f32 v113, v113\ns_waitcnt lgkmcnt(1)\n" \
    "v_smfmac_f32_32x32x32_bf16 v[128:143], v[180:183], v[104:111], v184\nv_exp_f32 v114, v114\nv_exp_f32 v115, v115\nv_exp_f32 v116, v116\n" \
    "v_mfma_f32_32x32x16_bf16 v[64:79], v[170:173], v[160:163], v[64:79]\nds_read_b32 v185, v191\nv_exp_f32 v117, v117\nv_exp_f32 v118, v118\n" \
    "v_exp_f32 v119, v119\nv_exp_f32 v120, v120\nv_mfma_f32_32x32x16_bf16 v[80:95], v[170:173], v[164:167], v[80:95]\n" \
    "ds_read_b128 v[170:173], v186 offset:3072\nv_exp_f32 v121, v121\nv_exp_f32 v122, v122\nv_exp_f32 v123, v123\n" \
    "v_smfmac_f32_32x32x32_bf16 v[144:159], v[176:179], v[112:119], v184\ns_mov_b64 exec, %[vm]\nds_read_b128 v[176:179], v188 offset:2304\n" \
    "s_mov_b64 exec, -1\nv_exp_f32 v124, v124\nv_exp_f32 v125, v125\nv_exp_f32 v126, v126\nv_exp_f32 v127, v127\ns_waitcnt lgkmcnt(2)\n" \
    "v_readfirstlane_b32 s94, v185\ns_cmp_ge_u32 s94, 5235200\ns_cbranch_scc1 4f\n3:\ns_sleep 1\nds_read_b32 v185, v191\ns_waitcnt lgkmcnt(0)\n" \
    "v_readfirstlane_b32 s94, v185\ns_cmp_lt_u32 s94, 5235200\ns_cbranch_scc1 3b\n4:\ns_waitcnt vmcnt(0)\ns_setprio 3\n"
#define ATT_O4 \
    "s_setprio 3\nv_exp_f32 v64, v64\nv_exp_f32 v65, v65\nv_smfmac_f32_32x32x32_bf16 v[144:159], v[180:183], v[120:127], v184\ns_mov_b64 exec, %[vm]\n" \
    "ds_read_b128 v[180:183], v189 offset:2304\ns_mov_b64 exec, -1\nv_exp_f32 v66, v66\nv_exp_f32 v67, v67\nv_exp_f32 v68, v68\n" \
    "v_mfma_f32_32x32x4_2b_bf16 v[96:127], v[174:175], v[168:169], 0 cbsz:1\nds_read_b32 v174, v187 offset:18432\nv_exp_f32 v69, v69\n" \
    "s_add_u32 m0, %[m2], 36864\nv_exp_f32 v70, v70\nglobal_load_lds_dwordx4 v190, %[sc] sc1\nv_exp_f32 v71, v71\nv_exp_f32 v72, v72\nv_exp_f32 v73, v73\n" \
    "v_exp_f32 v74, v74\nv_exp_f32 v75, v75\nglobal_load_lds_dwordx4 v190, %[sc] offset:1024 sc1\nv_exp_f32 v76, v76\ns_waitcnt lgkmcnt(2)\n" \
    "v_smfmac_f32_32x32x32_bf16 v[128:143], v[176:179], v[64:71], v184\nv_exp_f32 v77, v77\nv_exp_f32 v78, v78\nv_exp_f32 v79, v79\nv_exp_f32 v80, v80\n" \
    "v_exp_f32 v81, v81\ns_waitcnt lgkmcnt(1)\nv_smfmac_f32_32x32x32_bf16 v[128:143], v[180:183], v[72:79], v184\nv_exp_f32 v82, v82\nv_exp_f32 v83, v83\n" \
    "global_load_lds_dwordx4 v190, %[sc] offset:2048 sc1\nv_exp_f32 v84, v84\nv_mfma_f32_32x32x16_bf16 v[96:111], v[170:173], v[160:163], v[96:111]\n" \
    "v_exp_f32 v85, v85\nv_exp_f32 v86, v86\nv_exp_f32 v87, v87\nv_exp_f32 v88, v88\n" \
    "v_mfma_f32_32x32x16_bf16 v[112:127], v[170:173], v[164:167], v[112:127]\nds_read_b128 v[170:173], v186 offset:18432\nv_exp_f32 v89, v89\n" \
    "v_exp_f32 v90, v90\nv_exp_f32 v91, v91\nv_smfmac_f32_32x32x32_bf16 v[144:159], v[176:179], v[80:87], v184\ns_mov_b64 exec, %[vm]\n" \
    "ds_read_b128 v[176:179], v188 offset:3456\ns_mov_b64 exec, -1\nv_exp_f32 v92, v92\nv_exp_f32 v93, v93\n" \
    "buffer_load_dwordx4 v[192:195], %[a0], %[rs], 0 offen offset:0 sc1\nbuffer_load_dwordx4 v[196:199], %[a0], %[rs], 0 offen offset:1024 sc1\n" \
    "v_exp_f32 v94, v94\nv_exp_f32 v95, v95\ns_setprio 2\nv_exp_f32 v96, v96\nv_exp_f32 v97, v97\n" \
    "v_smfmac_f32_32x32x32_bf16 v[144:159], v[180:183], v[88:95], v184\ns_mov_b64 exec, %[vm]\nds_read_b128 v[180:183], v189 offset:3456\n" \
    "s_mov_b64 exec, -1\nv_exp_f32 v98, v98\nv_exp_f32 v99, v99\nv_exp_f32 v100, v100\ns_waitcnt lgkmcnt(3)\n" \
    "v_mfma_f32_32x32x4_2b_bf16 v[64:95], v[174:175], v[168:169], 0 cbsz:1\nds_read_b32 v174, v187 offset:18560\nv_exp_f32 v101, v101\nv_exp_f32 v102, v102\n" \
    "buffer_load_dwordx4 v[200:203], %[a0], %[rs], 0 offen offset:2048 sc1\nbuffer_load_dwordx4 v[204:207], %[a0], %[rs], 0 offen offset:3072 sc1\n" \
    "v_exp_f32 v103, v103\nv_exp_f32 v104, v104\nv_exp_f32 v105, v105\nv_exp_f32 v106, v106\nv_exp_f32 v107, v107\n" \
    "buffer_load_dwordx4 v[208:211], %[a1], %[rs], 0 offen offset:0 sc1\nbuffer_load_dwordx4 v[212:215], %[a1], %[rs], 0 offen offset:1024 sc1\n" \
    "v_exp_f32 v108, v108\ns_waitcnt lgkmcnt(2)\nv_smfmac_f32_32x32x32_bf16 v[128:143], v[176:179], v[96:103], v184\nv_exp_f32 v109, v109\n" \
    "v_exp_f32 v110, v110\nv_exp_f32 v111, v111\nv_exp_f32 v112, v112\nv_exp_f32 v113, v113\ns_waitcnt lgkmcnt(1)\n" \
    "v_smfmac_f32_32x32x32_bf16 v[128:143], v[180:183], v[104:111], v184\nv_exp_f32 v114, v114\nv_exp_f32 v115, v115\n" \
    "buffer_load_dwordx4 v[216:219], %[a1], %[rs], 0 offen offset:2048 sc1\nbuffer_load_dwordx4 v[220:223], %[a1], %[rs], 0 offen offset:3072 sc1\n" \
    "v_exp_f32 v116, v116\nv_mfma_f32_32x32x16_bf16 v[64:79], v[170:173], v[160:163], v[64:79]\nv_exp_f32 v117, v117\nv_exp_f32 v118, v118\n" \
    "v_exp_f32 v119, v119\nv_exp_f32 v120, v120\nv_mfma_f32_32x32x16_bf16 v[80:95], v[170:173], v[164:167], v[80:95]\n" \
    "ds_read_b128 v[170:173], v186 offset:19456\nv_exp_f32 v121, v121\nv_exp_f32 v122, v122\nv_exp_f32 v123, v123\n" \
    "v_smfmac_f32_32x32x32_bf16 v[144:159], v[176:179], v[112:119], v184\ns_mov_b64 exec, %[vm]\nds_read_b128 v[176:179], v188 offset:18432\n" \
    "s_mov_b64 exec, -1\nv_exp_f32 v124, v124\nv_exp_f32 v125, v125\nbuffer_load_dword v224, %[ar], %[rs], 0 offen offset:0 sc1\n" \
    "buffer_load_dword v225, %[ar], %[rs], 0 offen offset:128 sc1\nv_exp_f32 v126, v126\nv_exp_f32 v127, v127\ns_setprio 1\nv_exp_f32 v64, v64\n" \
    "v_exp_f32 v65, v65\nv_smfmac_f32_32x32x32_bf16 v[144:159], v[180:183], v[120:127], v184\ns_mov_b64 exec, %[vm]\n" \
    "ds_read_b128 v[180:183], v189 offset:18432\ns_mov_b64 exec, -1\nv_exp_f32 v66, v66\nv_exp_f32 v67, v67\nv_exp_f32 v68, v68\ns_waitcnt lgkmcnt(3)\n" \
    "v_mfma_f32_32x32x4_2b_bf16 v[96:127], v[174:175], v[168:169], 0 cbsz:1\nds_read_b32 v174, v187 offset:18688\nv_exp_f32 v69, v69\nv_exp_f32 v70, v70\n" \
    "buffer_load_dword v226, %[ar], %[rs], 0 offen offset:256 sc1\nbuffer_load_dword v227, %[ar], %[rs], 0 offen offset:384 sc1\nv_exp_f32 v71, v71\n" \
    "v_exp_f32 v72, v72\nv_exp_f32 v73, v73\nv_exp_f32 v74, v74\nv_exp_f32 v75, v75\nbuffer_load_dword v254, %[ag], %[rs], 0 offen sc1\nv_exp_f32 v76, v76\n" \
    "s_waitcnt lgkmcnt(2)\nv_smfmac_f32_32x32x32_bf16 v[128:143], v[176:179], v[64:71], v184\nv_exp_f32 v77, v77\nv_exp_f32 v78, v78\nv_exp_f32 v79, v79\n" \
    "v_exp_f32 v80, v80\nv_exp_f32 v81, v81\ns_waitcnt lgkmcnt(1)\nv_smfmac_f32_32x32x32_bf16 v[128:143], v[180:183], v[72:79], v184\nv_exp_f32 v82, v82\n" \
    "v_exp_f32 v83, v83\nv_exp_f32 v84, v84\nv_mfma_f32_32x32x16_bf16 v[96:111], v[170:173], v[160:163], v[96:111]\nv_exp_f32 v85, v85\nv_exp_f32 v86, v86\n" \
    "v_exp_f32 v87, v87\nv_exp_f32 v88, v88\nv_mfma_f32_32x32x16_bf16 v[112:127], v[170:173], v[164:167], v[112:127]\n" \
    "ds_read_b128 v[170:173], v186 offset:20480\nv_exp_f32 v89, v89\nv_exp_f32 v90, v90\nv_exp_f32 v91, v91\n" \
    "v_smfmac_f32_32x32x32_bf16 v[144:159], v[176:179], v[80:87], v184\ns_mov_b64 exec, %[vm]\nds_read_b128 v[176:179], v188 offset:19584\n" \
    "s_mov_b64 exec, -1\nv_exp_f32 v92, v92\nv_exp_f32 v93, v93\nv_exp_f32 v94, v94\nv_exp_f32 v95, v95\ns_waitcnt vmcnt(13)\ns_mov_b64 exec, 1\n" \
    "ds_add_u32 v191, v191\ns_mov_b64 exec, -1\ns_setprio 0\nv_exp_f32 v96, v96\nv_exp_f32 v97, v97\n" \
    "v_smfmac_f32_32x32x32_bf16 v[144:159], v[180:183], v[88:95], v184\ns_mov_b64 exec, %[vm]\nds_read_b128 v[180:183], v189 offset:19584\n" \
    "s_mov_b64 exec, -1\nv_exp_f32 v98, v98\nv_exp_f32 v99, v99\nv_exp_f32 v100, v100\ns_waitcnt lgkmcnt(4)\n" \
    "v_mfma_f32_32x32x4_2b_bf16 v[64:95], v[174:175], v[168:169], 0 cbsz:1\nds_read_b32 v174, v187 offset:18816\nv_exp_f32 v101, v101\nv_exp_f32 v102, v102\n" \
    "v_exp_f32 v103, v103\nv_exp_f32 v104, v104\nv_exp_f32 v105, v105\nv_exp_f32 v106, v106\nv_exp_f32 v107, v107\nv_exp_f32 v108, v108\n" \
    "s_waitcnt lgkmcnt(3)\nv_smfmac_f32_32x32x32_bf16 v[128:143], v[176:179], v[96:103], v184\nv_exp_f32 v109, v109\nv_exp_f32 v110, v110\n" \
    "v_exp_f32 v111, v111\nv_exp_f32 v112, v112\nv_exp_f32 v113, v113\ns_waitcnt lgkmcnt(1)\n" \
    "v_smfmac_f32_32x32x32_bf16 v[128:143], v[180:183], v[104:111], v184\nv_exp_f32 v114, v114\nv_exp_f32 v115, v115\nv_exp_f32 v116, v116\n" \
    "v_mfma_f32_32x32x16_bf16 v[64:79], v[170:173], v[160:163], v[64:79]\nds_read_b32 v185, v191\nv_exp_f32 v117, v117\nv_exp_f32 v118, v118\n" \
    "v_exp_f32 v119, v119\nv_exp_f32 v120, v120\nv_mfma_f32_32x32x16_bf16 v[80:95], v[170:173], v[164:167], v[80:95]\n" \
    "ds_read_b128 v[170:173], v186 offset:21504\nv_exp_f32 v121, v121\nv_exp_f32 v122, v122\nv_exp_f32 v123, v123\n" \
    "v_smfmac_f32_32x32x32_bf16 v[144:159], v[176:179], v[112:119], v184\ns_mov_b64 exec, %[vm]\nds_read_b128 v[176:179], v188 offset:20736\n" \
    "s_mov_b64 exec, -1\nv_exp_f32 v124, v124\nv_exp_f32 v125, v125\nv_exp_f32 v126, v126\nv_exp_f32 v127, v127\ns_waitcnt lgkmcnt(2)\n" \
    "v_readfirstlane_b32 s94, v185\ns_cmp_ge_u32 s94, 6544000\ns_cbranch_scc1 4f\n3:\ns_sleep 1\nds_read_b32 v185, v191\ns_waitcnt lgkmcnt(0)\n" \
    "v_readfirstlane_b32 s94, v185\ns_cmp_lt_u32 s94, 6544000\ns_cbranch_scc1 3b\n4:\ns_waitcnt vmcnt(0)\n"
#define ATT_O5 \
    "ds_read_b64_tr_b16 v[44:45], v248 offset:0\nds_read_b64_tr_b16 v[46:47], v248 offset:144\nds_read_b64_tr_b16 v[48:49], v248 offset:32\n" \
    "ds_read_b64_tr_b16 v[50:51], v248 offset:176\nds_read_b64_tr_b16 v[52:53], v248 offset:64\nds_read_b64_tr_b16 v[54:55], v248 offset:208\n" \
    "ds_read_b64_tr_b16 v[56:57], v248 offset:96\nds_read_b64_tr_b16 v[58:59], v248 offset:240\nds_read_b64_tr_b16 v[60:61], v249 offset:0\n" \
    "ds_read_b64_tr_b16 v[62:63], v249 offset:144\ns_setprio 3\nv_exp_f32 v64, v64\nv_exp_f32 v65, v65\n" \
    "v_smfmac_f32_32x32x32_bf16 v[144:159], v[180:183], v[120:127], v184\ns_mov_b64 exec, %[vm]\nds_read_b128 v[180:183], v189 offset:20736\n" \
    "s_mov_b64 exec, -1\nv_exp_f32 v66, v66\nv_exp_f32 v67, v67\nv_exp_f32 v68, v68\nv_mfma_f32_32x32x4_2b_bf16 v[96:127], v[174:175], v[168:169], 0 cbsz:1\n" \
    "ds_read_b32 v174, v187 offset:36864\nv_exp_f32 v69, v69\ns_add_u32 m0, %[m2], 0\nv_exp_f32 v70, v70\nglobal_load_lds_dwordx4 v190, %[sc] sc1\n" \
    "v_exp_f32 v71, v71\nv_exp_f32 v72, v72\nv_exp_f32 v73, v73\nv_exp_f32 v74, v74\nv_exp_f32 v75, v75\n" \
    "global_load_lds_dwordx4 v190, %[sc] offset:1024 sc1\nv_exp_f32 v76, v76\ns_waitcnt lgkmcnt(12)\n" \
    "v_smfmac_f32_32x32x32_bf16 v[128:143], v[176:179], v[64:71], v184\nv_exp_f32 v77, v77\nv_exp_f32 v78, v78\nv_exp_f32 v79, v79\ns_waitcnt lgkmcnt(10)\n" \
    "v_mfma_f32_16x16x32_bf16 v[228:231], v[44:47], v[192:195], 0\nds_read_b64_tr_b16 v[44:45], v248 offset:4608\n" \
    "ds_read_b64_tr_b16 v[46:47], v248 offset:4752\nv_exp_f32 v80, v80\ns_waitcnt lgkmcnt(10)\nv_mfma_f32_16x16x32_bf16 v[232:235], v[48:51], v[192:195], 0\n" \
    "ds_read_b64_tr_b16 v[48:49], v248 offset:4640\nds_read_b64_tr_b16 v[50:51], v248 offset:4784\nv_exp_f32 v81, v81\ns_waitcnt lgkmcnt(5)\n" \
    "v_smfmac_f32_32x32x32_bf16 v[128:143], v[180:183], v[72:79], v184\nv_exp_f32 v82, v82\nv_exp_f32 v83, v83\n" \
    "global_load_lds_dwordx4 v190, %[sc] offset:2048 sc1\nv_exp_f32 v84, v84\nv_mfma_f32_32x32x16_bf16 v[96:111], v[170:173], v[160:163], v[96:111]\n" \
    "v_exp_f32 v85, v85\nv_exp_f32 v86, v86\nv_exp_f32 v87, v87\nv_mfma_f32_16x16x32_bf16 v[236:239], v[52:55], v[192:195], 0\n" \
    "ds_read_b64_tr_b16 v[52:53], v248 offset:4672\nds_read_b64_tr_b16 v[54:55], v248 offset:4816\nv_exp_f32 v88, v88\n" \
    "v_mfma_f32_32x32x16_bf16 v[112:127], v[170:173], v[164:167], v[112:127]\nds_read_b128 v[170:173], v186 offset:36864\nv_exp_f32 v89, v89\n" \
    "v_exp_f32 v90, v90\nv_exp_f32 v91, v91\nv_smfmac_f32_32x32x32_bf16 v[144:159], v[176:179], v[80:87], v184\ns_mov_b64 exec, %[vm]\n" \
    "ds_read_b128 v[176:179], v188 offset:21888\ns_mov_b64 exec, -1\nv_exp_f32 v92, v92\nv_exp_f32 v93, v93\nglobal_load_dword v255, %[to], %[tw]\n" \
    "v_exp_f32 v94, v94\nv_mfma_f32_16x16x32_bf16 v[240:243], v[56:59], v[192:195], 0\nds_read_b64_tr_b16 v[56:57], v248 offset:4704\n" \
    "ds_read_b64_tr_b16 v[58:59], v248 offset:4848\nv_exp_f32 v95, v95\ns_setprio 2\nv_mfma_f32_16x16x32_bf16 v[244:247], v[60:63], v[192:195], 0\n" \
    "ds_read_b64_tr_b16 v[60:61], v249 offset:4608\nds_read_b64_tr_b16 v[62:63], v249 offset:4752\nv_exp_f32 v96, v96\ns_waitcnt lgkmcnt(10)\n" \
    "v_mfma_f32_16x16x32_bf16 v[228:231], v[44:47], v[196:199], v[228:231]\nds_read_b64_tr_b16 v[44:45], v248 offset:9216\n" \
    "ds_read_b64_tr_b16 v[46:47], v248 offset:9360\nv_exp_f32 v97, v97\nv_smfmac_f32_32x32x32_bf16 v[144:159], v[180:183], v[88:95], v184\n" \
    "s_mov_b64 exec, %[vm]\nds_read_b128 v[180:183], v189 offset:21888\ns_mov_b64 exec, -1\nv_exp_f32 v98, v98\nv_exp_f32 v99, v99\nv_exp_f32 v100, v100\n" \
    "v_mfma_f32_32x32x4_2b_bf16 v[64:95], v[174:175], v[168:169], 0 cbsz:1\nds_read_b32 v174, v187 offset:36992\nv_exp_f32 v101, v101\nv_exp_f32 v102, v102\n" \
    "v_exp_f32 v103, v103\nv_exp_f32 v104, v104\nv_exp_f32 v105, v105\nv_exp_f32 v106, v106\nv_exp_f32 v107, v107\nv_exp_f32 v108, v108\n" \
    "s_waitcnt lgkmcnt(8)\nv_smfmac_f32_32x32x32_bf16 v[128:143], v[176:179], v[96:103], v184\nv_exp_f32 v109, v109\nv_exp_f32 v110, v110\n" \
    "v_exp_f32 v111, v111\nv_mfma_f32_16x16x32_bf16 v[232:235], v[48:51], v[196:199], v[232:235]\nds_read_b64_tr_b16 v[48:49], v248 offset:9248\n" \
    "ds_read_b64_tr_b16 v[50:51], v248 offset:9392\nv_exp_f32 v112, v112\nv_mfma_f32_16x16x32_bf16 v[236:239], v[52:55], v[196:199], v[236:239]\n" \
    "ds_read_b64_tr_b16 v[52:53], v248 offset:9280\nds_read_b64_tr_b16 v[54:55], v248 offset:9424\nv_exp_f32 v113, v113\ns_waitcnt lgkmcnt(5)\n" \
    "v_smfmac_f32_32x32x32_bf16 v[128:143], v[180:183], v[104:111], v184\nv_exp_f32 v114, v114\nv_exp_f32 v115, v115\nv_exp_f32 v116, v116\n" \
    "v_mfma_f32_32x32x16_bf16 v[64:79], v[170:173], v[160:163], v[64:79]\nv_exp_f32 v117, v117\nv_exp_f32 v118, v118\nv_exp_f32 v119, v119\n" \
    "v_mfma_f32_16x16x32_bf16 v[240:243], v[56:59], v[196:199], v[240:243]\nds_read_b64_tr_b16 v[56:57], v248 offset:9312\n" \
    "ds_read_b64_tr_b16 v[58:59], v248 offset:9456\nv_exp_f32 v120, v120\nv_mfma_f32_32x32x16_bf16 v[80:95], v[170:173], v[164:167], v[80:95]\n" \
    "ds_read_b128 v[170:173], v186 offset:37888\nv_exp_f32 v121, v121\nv_exp_f32 v122, v122\nv_exp_f32 v123, v123\n" \
    "v_smfmac_f32_32x32x32_bf16 v[144:159], v[176:179], v[112:119], v184\ns_mov_b64 exec, %[vm]\nds_read_b128 v[176:179], v188 offset:36864\n" \
    "s_mov_b64 exec, -1\nv_exp_f32 v124, v124\nv_exp_f32 v125, v125\nv_exp_f32 v126, v126\n" \
    "v_mfma_f32_16x16x32_bf16 v[244:247], v[60:63], v[196:199], v[244:247]\nds_read_b64_tr_b16 v[60:61], v249 offset:9216\n" \
    "ds_read_b64_tr_b16 v[62:63], v249 offset:9360\nv_exp_f32 v127, v127\ns_setprio 1\n" \
    "v_mfma_f32_16x16x32_bf16 v[228:231], v[44:47], v[200:203], v[228:231]\nds_read_b64_tr_b16 v[44:45], v248 offset:13824\n" \
    "ds_read_b64_tr_b16 v[46:47], v248 offset:13968\nv_exp_f32 v64, v64\ns_waitcnt lgkmcnt(10)\n" \
    "v_mfma_f32_16x16x32_bf16 v[232:235], v[48:51], v[200:203], v[232:235]\nds_read_b64_tr_b16 v[48:49], v248 offset:13856\n" \
    "ds_read_b64_tr_b16 v[50:51], v248 offset:14000\nv_exp_f32 v65, v65\nv_smfmac_f32_32x32x32_bf16 v[144:159], v[180:183], v[120:127], v184\n" \
    "s_mov_b64 exec, %[vm]\nds_read_b128 v[180:183], v189 offset:36864\ns_mov_b64 exec, -1\nv_exp_f32 v66, v66\nv_exp_f32 v67, v67\nv_exp_f32 v68, v68\n" \
    "v_mfma_f32_32x32x4_2b_bf16 v[96:127], v[174:175], v[168:169], 0 cbsz:1\nds_read_b32 v174, v187 offset:37120\nv_exp_f32 v69, v69\nv_exp_f32 v70, v70\n" \
    "v_exp_f32 v71, v71\nv_exp_f32 v72, v72\nv_exp_f32 v73, v73\nv_exp_f32 v74, v74\nv_exp_f32 v75, v75\nv_exp_f32 v76, v76\ns_waitcnt lgkmcnt(8)\n" \
    "v_smfmac_f32_32x32x32_bf16 v[128:143], v[176:179], v[64:71], v184\nv_exp_f32 v77, v77\nv_exp_f32 v78, v78\nv_exp_f32 v79, v79\n" \
    "v_mfma_f32_16x16x32_bf16 v[236:239], v[52:55], v[200:203], v[236:239]\nds_read_b64_tr_b16 v[52:53], v248 offset:13888\n" \
    "ds_read_b64_tr_b16 v[54:55], v248 offset:14032\nv_exp_f32 v80, v80\nv_mfma_f32_16x16x32_bf16 v[240:243], v[56:59], v[200:203], v[240:243]\n" \
    "ds_read_b64_tr_b16 v[56:57], v248 offset:13920\nds_read_b64_tr_b16 v[58:59], v248 offset:14064\nv_exp_f32 v81, v81\ns_waitcnt lgkmcnt(5)\n" \
    "v_smfmac_f32_32x32x32_bf16 v[128:143], v[180:183], v[72:79], v184\nv_exp_f32 v82, v82\nv_exp_f32 v83, v83\nv_exp_f32 v84, v84\n" \
    "v_mfma_f32_32x32x16_bf16 v[96:111], v[170:173], v[160:163], v[96:111]\nv_exp_f32 v85, v85\nv_exp_f32 v86, v86\nv_exp_f32 v87, v87\n" \
    "v_mfma_f32_16x16x32_bf16 v[244:247], v[60:63], v[200:203], v[244:247]\nds_read_b64_tr_b16 v[60:61], v249 offset:13824\n" \
    "ds_read_b64_tr_b16 v[62:63], v249 offset:13968\nv_exp_f32 v88, v88\nv_mfma_f32_32x32x16_bf16 v[112:127], v[170:173], v[164:167], v[112:127]\n" \
    "ds_read_b128 v[170:173], v186 offset:38912\nv_exp_f32 v89, v89\nv_exp_f32 v90, v90\nv_exp_f32 v91, v91\n" \
    "v_smfmac_f32_32x32x32_bf16 v[144:159], v[176:179], v[80:87], v184\ns_mov_b64 exec, %[vm]\nds_read_b128 v[176:179], v188 offset:38016\n" \
    "s_mov_b64 exec, -1\nv_exp_f32 v92, v92\nv_exp_f32 v93, v93\nv_exp_f32 v94, v94\nv_mfma_f32_16x16x32_bf16 v[228:231], v[44:47], v[204:207], v[228:231]\n" \
    "ds_read_b64_tr_b16 v[44:45], v250 offset:0\nds_read_b64_tr_b16 v[46:47], v250 offset:144\nv_exp_f32 v95, v95\ns_waitcnt vmcnt(1)\ns_mov_b64 exec, 1\n" \
    "ds_add_u32 v191, v191\ns_mov_b64 exec, -1\ns_setprio 0\nv_mfma_f32_16x16x32_bf16 v[232:235], v[48:51], v[204:207], v[232:235]\n" \
    "ds_read_b64_tr_b16 v[48:49], v250 offset:32\nds_read_b64_tr_b16 v[50:51], v250 offset:176\nv_exp_f32 v96, v96\ns_waitcnt lgkmcnt(11)\n" \
    "v_mfma_f32_16x16x32_bf16 v[236:239], v[52:55], v[204:207], v[236:239]\nds_read_b64_tr_b16 v[52:53], v250 offset:64\n" \
    "ds_read_b64_tr_b16 v[54:55], v250 offset:208\nv_exp_f32 v97, v97\nv_smfmac_f32_32x32x32_bf16 v[144:159], v[180:183], v[88:95], v184\n" \
    "s_mov_b64 exec, %[vm]\nds_read_b128 v[180:183], v189 offset:38016\ns_mov_b64 exec, -1\nv_exp_f32 v98, v98\nv_exp_f32 v99, v99\nv_exp_f32 v100, v100\n" \
    "v_mfma_f32_32x32x4_2b_bf16 v[64:95], v[174:175], v[168:169], 0 cbsz:1\nds_read_b32 v174, v187 offset:37248\nv_exp_f32 v101, v101\nv_exp_f32 v102, v102\n" \
    "v_exp_f32 v103, v103\nv_exp_f32 v104, v104\nv_exp_f32 v105, v105\nv_exp_f32 v106, v106\nv_exp_f32 v107, v107\nv_exp_f32 v108, v108\n" \
    "s_waitcnt lgkmcnt(9)\nv_smfmac_f32_32x32x32_bf16 v[128:143], v[176:179], v[96:103], v184\nv_exp_f32 v109, v109\nv_exp_f32 v110, v110\n" \
    "v_exp_f32 v111, v111\nv_mfma_f32_16x16x32_bf16 v[240:243], v[56:59], v[204:207], v[240:243]\nds_read_b64_tr_b16 v[56:57], v250 offset:96\n" \
    "ds_read_b64_tr_b16 v[58:59], v250 offset:240\nv_exp_f32 v112, v112\nv_mfma_f32_16x16x32_bf16 v[244:247], v[60:63], v[204:207], v[244:247]\n" \
    "ds_read_b64_tr_b16 v[60:61], v251 offset:0\nds_read_b64_tr_b16 v[62:63], v251 offset:144\nv_exp_f32 v113, v113\ns_waitcnt lgkmcnt(5)\n" \
    "v_smfmac_f32_32x32x32_bf16 v[128:143], v[180:183], v[104:111], v184\nv_exp_f32 v114, v114\nv_exp_f32 v115, v115\nv_exp_f32 v116, v116\n" \
    "v_mfma_f32_32x32x16_bf16 v[64:79], v[170:173], v[160:163], v[64:79]\nds_read_b32 v185, v191\nv_exp_f32 v117, v117\nv_exp_f32 v118, v118\n" \
    "v_exp_f32 v119, v119\nv_mfma_f32_16x16x32_bf16 v[228:231], v[44:47], v[224:227], v[228:231]\nv_exp_f32 v120, v120\n" \
    "v_mfma_f32_32x32x16_bf16 v[80:95], v[170:173], v[164:167], v[80:95]\nds_read_b128 v[170:173], v186 offset:39936\nv_exp_f32 v121, v121\n" \
    "v_exp_f32 v122, v122\nv_exp_f32 v123, v123\nv_smfmac_f32_32x32x32_bf16 v[144:159], v[176:179], v[112:119], v184\ns_mov_b64 exec, %[vm]\n" \
    "ds_read_b128 v[176:179], v188 offset:39168\ns_mov_b64 exec, -1\nv_exp_f32 v124, v124\nv_exp_f32 v125, v125\nv_exp_f32 v126, v126\n" \
    "v_mfma_f32_16x16x32_bf16 v[232:235], v[48:51], v[224:227], v[232:235]\nv_exp_f32 v127, v127\ns_waitcnt lgkmcnt(2)\nv_readfirstlane_b32 s94, v185\n" \
    "s_cmp_ge_u32 s94, 7852800\ns_cbranch_scc1 4f\n3:\ns_sleep 1\nds_read_b32 v185, v191\ns_waitcnt lgkmcnt(0)\nv_readfirstlane_b32 s94, v185\n" \
    "s_cmp_lt_u32 s94, 7852800\ns_cbranch_scc1 3b\n4:\n"
#define ATT_O6 \
    "s_setprio 3\nv_mfma_f32_16x16x32_bf16 v[236:239], v[52:55], v[224:227], v[236:239]\nv_exp_f32 v64, v64\n" \
    "v_mfma_f32_16x16x32_bf16 v[240:243], v[56:59], v[224:227], v[240:243]\nv_exp_f32 v65, v65\n" \
    "v_smfmac_f32_32x32x32_bf16 v[144:159], v[180:183], v[120:127], v184\ns_mov_b64 exec, %[vm]\nds_read_b128 v[180:183], v189 offset:39168\n" \
    "s_mov_b64 exec, -1\nv_exp_f32 v66, v66\nv_exp_f32 v67, v67\nv_exp_f32 v68, v68\nv_mfma_f32_32x32x4_2b_bf16 v[96:127], v[174:175], v[168:169], 0 cbsz:1\n" \
    "ds_read_b32 v174, v187 offset:0\nv_exp_f32 v69, v69\ns_add_u32 m0, %[m2], 18432\nv_exp_f32 v70, v70\nglobal_load_lds_dwordx4 v190, %[sc] sc1\n" \
    "v_exp_f32 v71, v71\nv_exp_f32 v72, v72\nv_exp_f32 v73, v73\nv_exp_f32 v74, v74\nv_exp_f32 v75, v75\n" \
    "global_load_lds_dwordx4 v190, %[sc] offset:1024 sc1\nv_exp_f32 v76, v76\ns_waitcnt lgkmcnt(2)\n" \
    "v_smfmac_f32_32x32x32_bf16 v[128:143], v[176:179], v[64:71], v184\nv_exp_f32 v77, v77\nv_exp_f32 v78, v78\nv_exp_f32 v79, v79\n" \
    "v_mfma_f32_16x16x32_bf16 v[244:247], v[60:63], v[224:227], v[244:247]\nv_exp_f32 v80, v80\nv_exp_f32 v81, v81\ns_waitcnt lgkmcnt(1)\n" \
    "v_smfmac_f32_32x32x32_bf16 v[128:143], v[180:183], v[72:79], v184\nv_exp_f32 v82, v82\nv_exp_f32 v83, v83\n" \
    "global_load_lds_dwordx4 v190, %[sc] offset:2048 sc1\nv_exp_f32 v84, v84\nv_mfma_f32_32x32x16_bf16 v[96:111], v[170:173], v[160:163], v[96:111]\n" \
    "v_exp_f32 v85, v85\nv_exp_f32 v86, v86\nv_exp_f32 v87, v87\nv_exp_f32 v88, v88\n" \
    "v_mfma_f32_32x32x16_bf16 v[112:127], v[170:173], v[164:167], v[112:127]\nds_read_b128 v[170:173], v186 offset:0\nv_exp_f32 v89, v89\n" \
    "v_exp_f32 v90, v90\nv_exp_f32 v91, v91\nv_smfmac_f32_32x32x32_bf16 v[144:159], v[176:179], v[80:87], v184\ns_mov_b64 exec, %[vm]\n" \
    "ds_read_b128 v[176:179], v188 offset:40320\ns_mov_b64 exec, -1\nv_exp_f32 v92, v92\nv_exp_f32 v93, v93\nv_exp_f32 v94, v94\nv_exp_f32 v95, v95\n" \
    "s_setprio 2\nv_exp_f32 v96, v96\nv_exp_f32 v97, v97\nv_smfmac_f32_32x32x32_bf16 v[144:159], v[180:183], v[88:95], v184\ns_mov_b64 exec, %[vm]\n" \
    "ds_read_b128 v[180:183], v189 offset:40320\ns_mov_b64 exec, -1\nv_exp_f32 v98, v98\nv_exp_f32 v99, v99\nv_exp_f32 v100, v100\ns_waitcnt lgkmcnt(3)\n" \
    "v_mfma_f32_32x32x4_2b_bf16 v[64:95], v[174:175], v[168:169], 0 cbsz:1\nds_read_b32 v174, v187 offset:128\nv_exp_f32 v101, v101\ns_add_u32 s98, s96, 0\n" \
    "s_addc_u32 s99, s97, 0\ns_add_u32 m0, %[m1], 55296\nv_exp_f32 v102, v102\nglobal_load_lds_dwordx4 %[w0], s[98:99]\nv_exp_f32 v103, v103\n" \
    "v_exp_f32 v104, v104\nv_exp_f32 v105, v105\nv_exp_f32 v106, v106\ns_add_u32 s98, s96, 82944\ns_addc_u32 s99, s97, 0\ns_add_u32 m0, %[m1], 64512\n" \
    "v_exp_f32 v107, v107\nglobal_load_lds_dwordx4 %[w0], s[98:99]\nv_exp_f32 v108, v108\ns_waitcnt lgkmcnt(2)\n" \
    "v_smfmac_f32_32x32x32_bf16 v[128:143], v[176:179], v[96:103], v184\nv_exp_f32 v109, v109\nv_exp_f32 v110, v110\nv_exp_f32 v111, v111\n" \
    "v_exp_f32 v112, v112\nv_exp_f32 v113, v113\ns_waitcnt lgkmcnt(1)\nv_smfmac_f32_32x32x32_bf16 v[128:143], v[180:183], v[104:111], v184\n" \
    "v_exp_f32 v114, v114\ns_add_u32 s98, s96, 331776\ns_addc_u32 s99, s97, 0\ns_add_u32 m0, %[m1], 92160\nv_exp_f32 v115, v115\n" \
    "global_load_lds_dwordx4 %[w0], s[98:99]\nv_exp_f32 v116, v116\nv_mfma_f32_32x32x16_bf16 v[64:79], v[170:173], v[160:163], v[64:79]\n" \
    "v_exp_f32 v117, v117\nv_exp_f32 v118, v118\nv_exp_f32 v119, v119\nv_exp_f32 v120, v120\n" \
    "v_mfma_f32_32x32x16_bf16 v[80:95], v[170:173], v[164:167], v[80:95]\nds_read_b128 v[170:173], v186 offset:1024\nv_exp_f32 v121, v121\n" \
    "v_exp_f32 v122, v122\nv_exp_f32 v123, v123\nv_smfmac_f32_32x32x32_bf16 v[144:159], v[176:179], v[112:119], v184\ns_mov_b64 exec, %[vm]\n" \
    "ds_read_b128 v[176:179], v188 offset:0\ns_mov_b64 exec, -1\nv_exp_f32 v124, v124\ns_add_u32 s98, s96, 414720\ns_addc_u32 s99, s97, 0\n" \
    "s_add_u32 m0, %[m1], 101376\nv_exp_f32 v125, v125\nglobal_load_lds_dwordx4 %[w0], s[98:99]\nv_exp_f32 v126, v126\nv_exp_f32 v127, v127\ns_setprio 1\n" \
    "v_exp_f32 v64, v64\nv_exp_f32 v65, v65\nv_smfmac_f32_32x32x32_bf16 v[144:159], v[180:183], v[120:127], v184\ns_mov_b64 exec, %[vm]\n" \
    "ds_read_b128 v[180:183], v189 offset:0\ns_mov_b64 exec, -1\nv_exp_f32 v66, v66\nv_exp_f32 v67, v67\nv_exp_f32 v68, v68\ns_waitcnt lgkmcnt(3)\n" \
    "v_mfma_f32_32x32x4_2b_bf16 v[96:127], v[174:175], v[168:169], 0 cbsz:1\nds_read_b32 v174, v187 offset:256\nv_exp_f32 v69, v69\nv_exp_f32 v70, v70\n" \
    "v_exp_f32 v71, v71\nv_exp_f32 v72, v72\nv_exp_f32 v73, v73\nv_exp_f32 v74, v74\nv_exp_f32 v75, v75\nv_exp_f32 v76, v76\ns_waitcnt lgkmcnt(2)\n" \
    "v_smfmac_f32_32x32x32_bf16 v[128:143], v[176:179], v[64:71], v184\nv_exp_f32 v77, v77\nv_exp_f32 v78, v78\nv_exp_f32 v79, v79\nv_exp_f32 v80, v80\n" \
    "v_exp_f32 v81, v81\ns_waitcnt lgkmcnt(1)\nv_smfmac_f32_32x32x32_bf16 v[128:143], v[180:183], v[72:79], v184\nv_exp_f32 v82, v82\nv_exp_f32 v83, v83\n" \
    "v_exp_f32 v84, v84\nv_mfma_f32_32x32x16_bf16 v[96:111], v[170:173], v[160:163], v[96:111]\nv_exp_f32 v85, v85\nv_exp_f32 v86, v86\nv_exp_f32 v87, v87\n" \
    "v_exp_f32 v88, v88\nv_mfma_f32_32x32x16_bf16 v[112:127], v[170:173], v[164:167], v[112:127]\nds_read_b128 v[170:173], v186 offset:2048\n" \
    "v_exp_f32 v89, v89\nv_exp_f32 v90, v90\nv_exp_f32 v91, v91\nv_smfmac_f32_32x32x32_bf16 v[144:159], v[176:179], v[80:87], v184\ns_mov_b64 exec, %[vm]\n" \
    "ds_read_b128 v[176:179], v188 offset:1152\ns_mov_b64 exec, -1\nv_exp_f32 v92, v92\nv_exp_f32 v93, v93\nv_exp_f32 v94, v94\nv_exp_f32 v95, v95\n" \
    "s_waitcnt vmcnt(4)\ns_mov_b64 exec, 1\nds_add_u32 v191, v191\ns_mov_b64 exec, -1\ns_setprio 0\nv_exp_f32 v96, v96\nv_exp_f32 v97, v97\n" \
    "v_smfmac_f32_32x32x32_bf16 v[144:159], v[180:183], v[88:95], v184\ns_mov_b64 exec, %[vm]\nds_read_b128 v[180:183], v189 offset:1152\n" \
    "s_mov_b64 exec, -1\nv_exp_f32 v98, v98\nv_exp_f32 v99, v99\nv_exp_f32 v100, v100\ns_waitcnt lgkmcnt(4)\n" \
    "v_mfma_f32_32x32x4_2b_bf16 v[64:95], v[174:175], v[168:169], 0 cbsz:1\nds_read_b32 v174, v187 offset:384\nv_exp_f32 v101, v101\n" \
    "s_add_u32 s98, s96, 165888\ns_addc_u32 s99, s97, 0\ns_add_u32 m0, %[m1], 138240\nv_exp_f32 v102, v102\nglobal_load_lds_dwordx4 %[w0], s[98:99]\n" \
    "v_exp_f32 v103, v103\nv_exp_f32 v104, v104\nv_exp_f32 v105, v105\nv_exp_f32 v106, v106\nv_exp_f32 v107, v107\nv_exp_f32 v108, v108\n" \
    "s_waitcnt lgkmcnt(3)\nv_smfmac_f32_32x32x32_bf16 v[128:143], v[176:179], v[96:103], v184\nv_exp_f32 v109, v109\nv_exp_f32 v110, v110\n" \
    "v_exp_f32 v111, v111\nv_exp_f32 v112, v112\nv_exp_f32 v113, v113\ns_waitcnt lgkmcnt(1)\n" \
    "v_smfmac_f32_32x32x32_bf16 v[128:143], v[180:183], v[104:111], v184\nv_exp_f32 v114, v114\nv_exp_f32 v115, v115\nv_exp_f32 v116, v116\n" \
    "v_mfma_f32_32x32x16_bf16 v[64:79], v[170:173], v[160:163], v[64:79]\nds_read_b32 v185, v191\nv_exp_f32 v117, v117\nv_exp_f32 v118, v118\n" \
    "v_exp_f32 v119, v119\nv_exp_f32 v120, v120\nv_mfma_f32_32x32x16_bf16 v[80:95], v[170:173], v[164:167], v[80:95]\n" \
    "ds_read_b128 v[170:173], v186 offset:3072\nv_exp_f32 v121, v121\nv_exp_f32 v122, v122\nv_exp_f32 v123, v123\n" \
    "v_smfmac_f32_32x32x32_bf16 v[144:159], v[176:179], v[112:119], v184\ns_mov_b64 exec, %[vm]\nds_read_b128 v[176:179], v188 offset:2304\n" \
    "s_mov_b64 exec, -1\nv_exp_f32 v124, v124\nv_exp_f32 v125, v125\nv_exp_f32 v126, v126\nv_exp_f32 v127, v127\ns_waitcnt lgkmcnt(2)\n" \
    "v_readfirstlane_b32 s94, v185\ns_cmp_ge_u32 s94, 9161600\ns_cbranch_scc1 4f\n3:\ns_sleep 1\nds_read_b32 v185, v191\ns_waitcnt lgkmcnt(0)\n" \
    "v_readfirstlane_b32 s94, v185\ns_cmp_lt_u32 s94, 9161600\ns_cbranch_scc1 3b\n4:\ns_setprio 3\n"
#define ATT_O7 \
    "s_setprio 3\nv_exp_f32 v64, v64\nv_exp_f32 v65, v65\nv_smfmac_f32_32x32x32_bf16 v[144:159], v[180:183], v[120:127], v184\ns_mov_b64 exec, %[vm]\n" \
    "ds_read_b128 v[180:183], v189 offset:2304\ns_mov_b64 exec, -1\nv_exp_f32 v66, v66\nv_exp_f32 v67, v67\nv_exp_f32 v68, v68\n" \
    "v_mfma_f32_32x32x4_2b_bf16 v[96:127], v[174:175], v[168:169], 0 cbsz:1\nds_read_b32 v174, v187 offset:18432\nv_exp_f32 v69, v69\ns_mov_b32 m0, %[mr]\n" \
    "v_exp_f32 v70, v70\nglobal_load_lds_dwordx4 %[w2], s[96:97]\nv_exp_f32 v71, v71\nv_exp_f32 v72, v72\nv_exp_f32 v73, v73\nv_exp_f32 v74, v74\n" \
    "v_exp_f32 v75, v75\nv_exp_f32 v76, v76\ns_waitcnt lgkmcnt(2)\nv_smfmac_f32_32x32x32_bf16 v[128:143], v[176:179], v[64:71], v184\nv_exp_f32 v77, v77\n" \
    "v_exp_f32 v78, v78\nv_exp_f32 v79, v79\nv_exp_f32 v80, v80\nv_exp_f32 v81, v81\ns_waitcnt lgkmcnt(1)\n" \
    "v_smfmac_f32_32x32x32_bf16 v[128:143], v[180:183], v[72:79], v184\nv_exp_f32 v82, v82\ns_add_u32 s98, s96, 248832\ns_addc_u32 s99, s97, 0\n" \
    "s_add_u32 m0, %[m1], 147456\nv_exp_f32 v83, v83\nglobal_load_lds_dwordx4 %[w0], s[98:99]\nv_exp_f32 v84, v84\n" \
    "v_mfma_f32_32x32x16_bf16 v[96:111], v[170:173], v[160:163], v[96:111]\nv_exp_f32 v85, v85\nv_exp_f32 v86, v86\nv_exp_f32 v87, v87\nv_exp_f32 v88, v88\n" \
    "v_mfma_f32_32x32x16_bf16 v[112:127], v[170:173], v[164:167], v[112:127]\nds_read_b128 v[170:173], v186 offset:18432\nv_exp_f32 v89, v89\n" \
    "v_exp_f32 v90, v90\nv_exp_f32 v91, v91\nv_smfmac_f32_32x32x32_bf16 v[144:159], v[176:179], v[80:87], v184\ns_mov_b64 exec, %[vm]\n" \
    "ds_read_b128 v[176:179], v188 offset:3456\ns_mov_b64 exec, -1\nv_exp_f32 v92, v92\nv_exp_f32 v93, v93\nv_exp_f32 v94, v94\nv_exp_f32 v95, v95\n" \
    "s_setprio 2\nv_exp_f32 v96, v96\nv_exp_f32 v97, v97\nv_smfmac_f32_32x32x32_bf16 v[144:159], v[180:183], v[88:95], v184\ns_mov_b64 exec, %[vm]\n" \
    "ds_read_b128 v[180:183], v189 offset:3456\ns_mov_b64 exec, -1\nv_exp_f32 v98, v98\nv_exp_f32 v99, v99\nv_exp_f32 v100, v100\ns_waitcnt lgkmcnt(3)\n" \
    "v_mfma_f32_32x32x4_2b_bf16 v[64:95], v[174:175], v[168:169], 0 cbsz:1\nds_read_b32 v174, v187 offset:18560\nv_exp_f32 v101, v101\nv_exp_f32 v102, v102\n" \
    "v_exp_f32 v103, v103\nv_exp_f32 v104, v104\nv_exp_f32 v105, v105\nv_exp_f32 v106, v106\nv_exp_f32 v107, v107\nv_exp_f32 v108, v108\n" \
    "s_waitcnt lgkmcnt(2)\nv_smfmac_f32_32x32x32_bf16 v[128:143], v[176:179], v[96:103], v184\nv_exp_f32 v109, v109\nv_exp_f32 v110, v110\n" \
    "v_exp_f32 v111, v111\nv_exp_f32 v112, v112\nv_exp_f32 v113, v113\ns_waitcnt lgkmcnt(1)\n" \
    "v_smfmac_f32_32x32x32_bf16 v[128:143], v[180:183], v[104:111], v184\nv_exp_f32 v114, v114\nv_exp_f32 v115, v115\nv_exp_f32 v116, v116\n" \
    "v_mfma_f32_32x32x16_bf16 v[64:79], v[170:173], v[160:163], v[64:79]\nv_exp_f32 v117, v117\nv_exp_f32 v118, v118\nv_exp_f32 v119, v119\n" \
    "v_exp_f32 v120, v120\nv_mfma_f32_32x32x16_bf16 v[80:95], v[170:173], v[164:167], v[80:95]\nds_read_b128 v[170:173], v186 offset:19456\n" \
    "v_exp_f32 v121, v121\nv_exp_f32 v122, v122\nv_exp_f32 v123, v123\nv_smfmac_f32_32x32x32_bf16 v[144:159], v[176:179], v[112:119], v184\n" \
    "s_mov_b64 exec, %[vm]\nds_read_b128 v[176:179], v188 offset:18432\ns_mov_b64 exec, -1\nv_exp_f32 v124, v124\nv_exp_f32 v125, v125\n" \
    "v_exp_f32 v126, v126\nv_exp_f32 v127, v127\ns_setprio 1\nv_exp_f32 v64, v64\nv_exp_f32 v65, v65\n" \
    "v_smfmac_f32_32x32x32_bf16 v[144:159], v[180:183], v[120:127], v184\ns_mov_b64 exec, %[vm]\nds_read_b128 v[180:183], v189 offset:18432\n" \
    "s_mov_b64 exec, -1\nv_exp_f32 v66, v66\nv_exp_f32 v67, v67\nv_exp_f32 v68, v68\ns_waitcnt lgkmcnt(3)\n" \
    "v_mfma_f32_32x32x4_2b_bf16 v[96:127], v[174:175], v[168:169], 0 cbsz:1\nds_read_b32 v174, v187 offset:18688\nv_exp_f32 v69, v69\n" \
    "s_add_u32 s98, s96, 497664\ns_addc_u32 s99, s97, 0\ns_add_u32 m0, %[m1], 36864\nv_exp_f32 v70, v70\nglobal_load_lds_dwordx4 %[w0], s[98:99]\n" \
    "v_exp_f32 v71, v71\nv_exp_f32 v72, v72\nv_exp_f32 v73, v73\nv_exp_f32 v74, v74\nv_exp_f32 v75, v75\nv_exp_f32 v76, v76\ns_waitcnt lgkmcnt(2)\n" \
    "v_smfmac_f32_32x32x32_bf16 v[128:143], v[176:179], v[64:71], v184\nv_exp_f32 v77, v77\nv_exp_f32 v78, v78\nv_exp_f32 v79, v79\nv_exp_f32 v80, v80\n" \
    "v_exp_f32 v81, v81\ns_waitcnt lgkmcnt(1)\nv_smfmac_f32_32x32x32_bf16 v[128:143], v[180:183], v[72:79], v184\nv_exp_f32 v82, v82\n" \
    "s_add_u32 s98, s96, 580608\ns_addc_u32 s99, s97, 0\ns_add_u32 m0, %[m1], 46080\nv_exp_f32 v83, v83\nglobal_load_lds_dwordx4 %[w0], s[98:99]\n" \
    "v_exp_f32 v84, v84\nv_mfma_f32_32x32x16_bf16 v[96:111], v[170:173], v[160:163], v[96:111]\nv_exp_f32 v85, v85\nv_exp_f32 v86, v86\nv_exp_f32 v87, v87\n" \
    "v_exp_f32 v88, v88\nv_mfma_f32_32x32x16_bf16 v[112:127], v[170:173], v[164:167], v[112:127]\nds_read_b128 v[170:173], v186 offset:20480\n" \
    "v_exp_f32 v89, v89\nv_exp_f32 v90, v90\nv_exp_f32 v91, v91\nv_smfmac_f32_32x32x32_bf16 v[144:159], v[176:179], v[80:87], v184\ns_mov_b64 exec, %[vm]\n" \
    "ds_read_b128 v[176:179], v188 offset:19584\ns_mov_b64 exec, -1\nv_exp_f32 v92, v92\ns_add_u32 m0, %[m1], 129024\nv_exp_f32 v93, v93\n" \
    "global_load_lds_dwordx4 %[w1], s[96:97]\nv_exp_f32 v94, v94\nv_exp_f32 v95, v95\ns_mov_b64 exec, 1\nds_add_u32 v191, v191\ns_mov_b64 exec, -1\n" \
    "s_setprio 0\nv_exp_f32 v96, v96\nv_exp_f32 v97, v97\nv_smfmac_f32_32x32x32_bf16 v[144:159], v[180:183], v[88:95], v184\ns_mov_b64 exec, %[vm]\n" \
    "ds_read_b128 v[180:183], v189 offset:19584\ns_mov_b64 exec, -1\nv_exp_f32 v98, v98\nv_exp_f32 v99, v99\nv_exp_f32 v100, v100\ns_waitcnt lgkmcnt(4)\n" \
    "v_mfma_f32_32x32x4_2b_bf16 v[64:95], v[174:175], v[168:169], 0 cbsz:1\nds_read_b32 v174, v187 offset:18816\nv_exp_f32 v101, v101\nv_exp_f32 v102, v102\n" \
    "v_exp_f32 v103, v103\nv_exp_f32 v104, v104\nv_exp_f32 v105, v105\nv_exp_f32 v106, v106\nv_exp_f32 v107, v107\nv_exp_f32 v108, v108\n" \
    "s_waitcnt lgkmcnt(3)\nv_smfmac_f32_32x32x32_bf16 v[128:143], v[176:179], v[96:103], v184\nv_exp_f32 v109, v109\nv_exp_f32 v110, v110\n" \
    "v_exp_f32 v111, v111\nv_exp_f32 v112, v112\nv_exp_f32 v113, v113\ns_waitcnt lgkmcnt(1)\n" \
    "v_smfmac_f32_32x32x32_bf16 v[128:143], v[180:183], v[104:111], v184\nv_exp_f32 v114, v114\nv_exp_f32 v115, v115\nv_exp_f32 v116, v116\n" \
    "v_mfma_f32_32x32x16_bf16 v[64:79], v[170:173], v[160:163], v[64:79]\nv_exp_f32 v117, v117\nv_exp_f32 v118, v118\nv_exp_f32 v119, v119\n" \
    "v_exp_f32 v120, v120\nv_mfma_f32_32x32x16_bf16 v[80:95], v[170:173], v[164:167], v[80:95]\nds_read_b128 v[170:173], v186 offset:21504\n" \
    "v_exp_f32 v121, v121\nv_exp_f32 v122, v122\nv_exp_f32 v123, v123\nv_smfmac_f32_32x32x32_bf16 v[144:159], v[176:179], v[112:119], v184\n" \
    "s_mov_b64 exec, %[vm]\nds_read_b128 v[176:179], v188 offset:20736\ns_mov_b64 exec, -1\nv_exp_f32 v124, v124\nv_exp_f32 v125, v125\n" \
    "v_exp_f32 v126, v126\nv_exp_f32 v127, v127\n"
#define ATT_O8 \
    "s_setprio 3\nv_exp_f32 v64, v64\nv_exp_f32 v65, v65\nv_smfmac_f32_32x32x32_bf16 v[144:159], v[180:183], v[120:127], v184\ns_mov_b64 exec, %[vm]\n" \
    "ds_read_b128 v[180:183], v189 offset:20736\ns_mov_b64 exec, -1\nv_exp_f32 v66, v66\nv_exp_f32 v67, v67\nv_exp_f32 v68, v68\ns_waitcnt lgkmcnt(3)\n" \
    "v_mfma_f32_32x32x4_2b_bf16 v[96:127], v[174:175], v[168:169], 0 cbsz:1\nv_exp_f32 v69, v69\nv_exp_f32 v70, v70\nv_exp_f32 v71, v71\nv_exp_f32 v72, v72\n" \
    "v_exp_f32 v73, v73\nv_exp_f32 v74, v74\nv_exp_f32 v75, v75\nv_exp_f32 v76, v76\ns_waitcnt lgkmcnt(1)\n" \
    "v_smfmac_f32_32x32x32_bf16 v[128:143], v[176:179], v[64:71], v184\nv_exp_f32 v77, v77\nv_exp_f32 v78, v78\nv_exp_f32 v79, v79\nv_exp_f32 v80, v80\n" \
    "v_exp_f32 v81, v81\ns_waitcnt lgkmcnt(0)\nv_smfmac_f32_32x32x32_bf16 v[128:143], v[180:183], v[72:79], v184\nv_exp_f32 v82, v82\nv_exp_f32 v83, v83\n" \
    "v_exp_f32 v84, v84\nv_mfma_f32_32x32x16_bf16 v[96:111], v[170:173], v[160:163], v[96:111]\nds_read_b32 v185, v191\nv_exp_f32 v85, v85\n" \
    "v_exp_f32 v86, v86\nv_exp_f32 v87, v87\nv_exp_f32 v88, v88\nv_mfma_f32_32x32x16_bf16 v[112:127], v[170:173], v[164:167], v[112:127]\n" \
    "v_exp_f32 v89, v89\nv_exp_f32 v90, v90\nv_exp_f32 v91, v91\nv_smfmac_f32_32x32x32_bf16 v[144:159], v[176:179], v[80:87], v184\ns_mov_b64 exec, %[vm]\n" \
    "ds_read_b128 v[176:179], v188 offset:21888\ns_mov_b64 exec, -1\nv_exp_f32 v92, v92\nv_exp_f32 v93, v93\nv_exp_f32 v94, v94\nv_exp_f32 v95, v95\n" \
    "s_waitcnt lgkmcnt(1)\nv_readfirstlane_b32 s94, v185\ns_cmp_ge_u32 s94, 10470400\ns_cbranch_scc1 4f\n3:\ns_sleep 1\nds_read_b32 v185, v191\n" \
    "s_waitcnt lgkmcnt(0)\nv_readfirstlane_b32 s94, v185\ns_cmp_lt_u32 s94, 10470400\ns_cbranch_scc1 3b\n4:\ns_setprio 2\nv_exp_f32 v96, v96\n" \
    "v_exp_f32 v97, v97\nv_smfmac_f32_32x32x32_bf16 v[144:159], v[180:183], v[88:95], v184\ns_mov_b64 exec, %[vm]\n" \
    "ds_read_b128 v[180:183], v189 offset:21888\ns_mov_b64 exec, -1\nv_exp_f32 v98, v98\nv_exp_f32 v99, v99\nv_exp_f32 v100, v100\nv_exp_f32 v101, v101\n" \
    "v_exp_f32 v102, v102\nv_exp_f32 v103, v103\nv_exp_f32 v104, v104\nv_exp_f32 v105, v105\nv_exp_f32 v106, v106\nv_exp_f32 v107, v107\n" \
    "v_exp_f32 v108, v108\ns_waitcnt lgkmcnt(1)\nv_smfmac_f32_32x32x32_bf16 v[128:143], v[176:179], v[96:103], v184\nv_exp_f32 v109, v109\n" \
    "v_exp_f32 v110, v110\nv_exp_f32 v111, v111\nv_exp_f32 v112, v112\nv_exp_f32 v113, v113\ns_waitcnt lgkmcnt(0)\n" \
    "v_smfmac_f32_32x32x32_bf16 v[128:143], v[180:183], v[104:111], v184\nv_exp_f32 v114, v114\nv_exp_f32 v115, v115\nv_exp_f32 v116, v116\n" \
    "v_exp_f32 v117, v117\nv_exp_f32 v118, v118\nv_exp_f32 v119, v119\nv_exp_f32 v120, v120\nv_exp_f32 v121, v121\nv_exp_f32 v122, v122\n" \
    "v_exp_f32 v123, v123\nv_smfmac_f32_32x32x32_bf16 v[144:159], v[176:179], v[112:119], v184\nv_exp_f32 v124, v124\nv_exp_f32 v125, v125\n" \
    "v_exp_f32 v126, v126\nv_exp_f32 v127, v127\ns_nop 1\nv_smfmac_f32_32x32x32_bf16 v[144:159], v[180:183], v[120:127], v184\ns_nop 11\n"
#define ATT_Y1 \
    "s_setprio 3\nv_exp_f32 v64, v64\nv_exp_f32 v65, v65\nv_smfmac_f32_32x32x32_bf16 v[144:159], v[180:183], v[120:127], v184\ns_mov_b64 exec, %[vm]\n" \
    "ds_read_b128 v[180:183], v189 offset:2304\ns_mov_b64 exec, -1\nv_exp_f32 v66, v66\nv_exp_f32 v67, v67\nv_exp_f32 v68, v68\n" \
    "v_mfma_f32_32x32x4_2b_bf16 v[96:127], v[174:175], v[168:169], 0 cbsz:1\nds_read_b32 v174, v187 offset:18432\nv_exp_f32 v69, v69\n" \
    "s_add_u32 m0, %[m2], 36864\nv_exp_f32 v70, v70\nglobal_load_lds_dwordx4 v190, %[sc] sc1\nv_exp_f32 v71, v71\nv_exp_f32 v72, v72\nv_exp_f32 v73, v73\n" \
    "v_exp_f32 v74, v74\nv_exp_f32 v75, v75\nglobal_load_lds_dwordx4 v190, %[sc] offset:1024 sc1\nv_exp_f32 v76, v76\ns_waitcnt lgkmcnt(2)\n" \
    "v_smfmac_f32_32x32x32_bf16 v[128:143], v[176:179], v[64:71], v184\nv_exp_f32 v77, v77\nv_exp_f32 v78, v78\nv_exp_f32 v79, v79\nv_exp_f32 v80, v80\n" \
    "v_exp_f32 v81, v81\ns_waitcnt lgkmcnt(1)\nv_smfmac_f32_32x32x32_bf16 v[128:143], v[180:183], v[72:79], v184\nv_exp_f32 v82, v82\nv_exp_f32 v83, v83\n" \
    "global_load_lds_dwordx4 v190, %[sc] offset:2048 sc1\nv_exp_f32 v84, v84\nv_mfma_f32_32x32x16_bf16 v[96:111], v[170:173], v[160:163], v[96:111]\n" \
    "v_exp_f32 v85, v85\nv_exp_f32 v86, v86\nv_exp_f32 v87, v87\nv_exp_f32 v88, v88\n" \
    "v_mfma_f32_32x32x16_bf16 v[112:127], v[170:173], v[164:167], v[112:127]\nds_read_b128 v[170:173], v186 offset:18432\nv_exp_f32 v89, v89\n" \
    "v_exp_f32 v90, v90\nv_exp_f32 v91, v91\nv_smfmac_f32_32x32x32_bf16 v[144:159], v[176:179], v[80:87], v184\ns_mov_b64 exec, %[vm]\n" \
    "ds_read_b128 v[176:179], v188 offset:3456\ns_mov_b64 exec, -1\nv_exp_f32 v92, v92\nv_exp_f32 v93, v93\nv_exp_f32 v94, v94\nv_exp_f32 v95, v95\n" \
    "s_setprio 2\nv_exp_f32 v96, v96\nv_exp_f32 v97, v97\nv_smfmac_f32_32x32x32_bf16 v[144:159], v[180:183], v[88:95], v184\ns_mov_b64 exec, %[vm]\n" \
    "ds_read_b128 v[180:183], v189 offset:3456\ns_mov_b64 exec, -1\nv_exp_f32 v98, v98\nv_exp_f32 v99, v99\nv_exp_f32 v100, v100\ns_waitcnt lgkmcnt(3)\n" \
    "v_mfma_f32_32x32x4_2b_bf16 v[64:95], v[174:175], v[168:169], 0 cbsz:1\nds_read_b32 v174, v187 offset:18560\nv_exp_f32 v101, v101\nv_exp_f32 v102, v102\n" \
    "buffer_load_dwordx4 v[192:195], v224, %[rs], 0 offen sc1\nbuffer_load_dwordx4 v[196:199], v224, %[rs], 0 offen offset:1024 sc1\nv_exp_f32 v103, v103\n" \
    "v_exp_f32 v104, v104\nv_exp_f32 v105, v105\nv_exp_f32 v106, v106\nv_mov_b32 v232, -1\nv_exp_f32 v107, v107\nv_mov_b32 v233, -1\nv_exp_f32 v108, v108\n" \
    "s_waitcnt lgkmcnt(2)\nv_smfmac_f32_32x32x32_bf16 v[128:143], v[176:179], v[96:103], v184\nv_exp_f32 v109, v109\nv_exp_f32 v110, v110\n" \
    "v_exp_f32 v111, v111\nv_exp_f32 v112, v112\nv_exp_f32 v113, v113\ns_waitcnt lgkmcnt(1)\n" \
    "v_smfmac_f32_32x32x32_bf16 v[128:143], v[180:183], v[104:111], v184\nv_exp_f32 v114, v114\nv_exp_f32 v115, v115\n" \
    "buffer_load_dword v200, v225, %[rs], 0 offen sc1\nv_exp_f32 v116, v116\nv_mfma_f32_32x32x16_bf16 v[64:79], v[170:173], v[160:163], v[64:79]\n" \
    "v_exp_f32 v117, v117\nv_exp_f32 v118, v118\nv_exp_f32 v119, v119\nv_exp_f32 v120, v120\n" \
    "v_mfma_f32_32x32x16_bf16 v[80:95], v[170:173], v[164:167], v[80:95]\nds_read_b128 v[170:173], v186 offset:19456\nv_exp_f32 v121, v121\n" \
    "v_exp_f32 v122, v122\nv_exp_f32 v123, v123\nv_smfmac_f32_32x32x32_bf16 v[144:159], v[176:179], v[112:119], v184\ns_mov_b64 exec, %[vm]\n" \
    "ds_read_b128 v[176:179], v188 offset:18432\ns_mov_b64 exec, -1\nv_exp_f32 v124, v124\nv_mov_b32 v234, -1\nv_exp_f32 v125, v125\nv_mov_b32 v235, -1\n" \
    "v_exp_f32 v126, v126\nv_exp_f32 v127, v127\ns_setprio 1\nv_exp_f32 v64, v64\nv_exp_f32 v65, v65\n" \
    "v_smfmac_f32_32x32x32_bf16 v[144:159], v[180:183], v[120:127], v184\ns_mov_b64 exec, %[vm]\nds_read_b128 v[180:183], v189 offset:18432\n" \
    "s_mov_b64 exec, -1\nv_exp_f32 v66, v66\nv_exp_f32 v67, v67\nv_exp_f32 v68, v68\ns_waitcnt lgkmcnt(3)\n" \
    "v_mfma_f32_32x32x4_2b_bf16 v[96:127], v[174:175], v[168:169], 0 cbsz:1\nds_read_b32 v174, v187 offset:18688\nv_exp_f32 v69, v69\nv_exp_f32 v70, v70\n" \
    "buffer_store_dwordx4 v[232:235], v226, %[rs], 0 offen sc1\nbuffer_store_dword v232, v227, %[rs], 0 offen sc1\nv_exp_f32 v71, v71\nv_exp_f32 v72, v72\n" \
    "v_exp_f32 v73, v73\nv_exp_f32 v74, v74\nv_exp_f32 v75, v75\nbuffer_store_dwordx4 v[232:235], v228, %[rs], 0 offen sc1\n" \
    "buffer_store_dword v232, v229, %[rs], 0 offen sc1\nv_exp_f32 v76, v76\ns_waitcnt lgkmcnt(2)\n" \
    "v_smfmac_f32_32x32x32_bf16 v[128:143], v[176:179], v[64:71], v184\nv_exp_f32 v77, v77\nv_exp_f32 v78, v78\nv_exp_f32 v79, v79\nv_exp_f32 v80, v80\n" \
    "v_exp_f32 v81, v81\ns_waitcnt lgkmcnt(1)\nv_smfmac_f32_32x32x32_bf16 v[128:143], v[180:183], v[72:79], v184\nv_exp_f32 v82, v82\nv_exp_f32 v83, v83\n" \
    "v_exp_f32 v84, v84\nv_mfma_f32_32x32x16_bf16 v[96:111], v[170:173], v[160:163], v[96:111]\nv_exp_f32 v85, v85\nv_exp_f32 v86, v86\nv_exp_f32 v87, v87\n" \
    "v_exp_f32 v88, v88\nv_mfma_f32_32x32x16_bf16 v[112:127], v[170:173], v[164:167], v[112:127]\nds_read_b128 v[170:173], v186 offset:20480\n" \
    "v_exp_f32 v89, v89\nv_exp_f32 v90, v90\nv_exp_f32 v91, v91\nv_smfmac_f32_32x32x32_bf16 v[144:159], v[176:179], v[80:87], v184\ns_mov_b64 exec, %[vm]\n" \
    "ds_read_b128 v[176:179], v188 offset:19584\ns_mov_b64 exec, -1\nv_exp_f32 v92, v92\nv_exp_f32 v93, v93\nv_exp_f32 v94, v94\nv_exp_f32 v95, v95\n" \
    "s_waitcnt vmcnt(7)\ns_mov_b64 exec, 1\nds_add_u32 v191, v191\ns_mov_b64 exec, -1\ns_setprio 0\nv_exp_f32 v96, v96\nv_exp_f32 v97, v97\n" \
    "v_smfmac_f32_32x32x32_bf16 v[144:159], v[180:183], v[88:95], v184\ns_mov_b64 exec, %[vm]\nds_read_b128 v[180:183], v189 offset:19584\n" \
    "s_mov_b64 exec, -1\nv_exp_f32 v98, v98\nv_exp_f32 v99, v99\nv_exp_f32 v100, v100\ns_waitcnt lgkmcnt(4)\n" \
    "v_mfma_f32_32x32x4_2b_bf16 v[64:95], v[174:175], v[168:169], 0 cbsz:1\nds_read_b32 v174, v187 offset:18816\nv_exp_f32 v101, v101\nv_exp_f32 v102, v102\n" \
    "v_exp_f32 v103, v103\nv_exp_f32 v104, v104\nv_exp_f32 v105, v105\nv_exp_f32 v106, v106\nv_exp_f32 v107, v107\nv_exp_f32 v108, v108\n" \
    "s_waitcnt lgkmcnt(3)\nv_smfmac_f32_32x32x32_bf16 v[128:143], v[176:179], v[96:103], v184\nv_exp_f32 v109, v109\nv_exp_f32 v110, v110\n" \
    "v_exp_f32 v111, v111\nv_exp_f32 v112, v112\nv_exp_f32 v113, v113\ns_waitcnt lgkmcnt(1)\n" \
    "v_smfmac_f32_32x32x32_bf16 v[128:143], v[180:183], v[104:111], v184\nv_exp_f32 v114, v114\nv_exp_f32 v115, v115\nv_exp_f32 v116, v116\n" \
    "v_mfma_f32_32x32x16_bf16 v[64:79], v[170:173], v[160:163], v[64:79]\nds_read_b32 v185, v191\nv_exp_f32 v117, v117\nv_exp_f32 v118, v118\n" \
    "v_exp_f32 v119, v119\nv_exp_f32 v120, v120\nv_mfma_f32_32x32x16_bf16 v[80:95], v[170:173], v[164:167], v[80:95]\n" \
    "ds_read_b128 v[170:173], v186 offset:21504\nv_exp_f32 v121, v121\nv_exp_f32 v122, v122\nv_exp_f32 v123, v123\n" \
    "v_smfmac_f32_32x32x32_bf16 v[144:159], v[176:179], v[112:119], v184\ns_mov_b64 exec, %[vm]\nds_read_b128 v[176:179], v188 offset:20736\n" \
    "s_mov_b64 exec, -1\nv_exp_f32 v124, v124\nv_exp_f32 v125, v125\nv_exp_f32 v126, v126\nv_exp_f32 v127, v127\ns_waitcnt lgkmcnt(2)\n" \
    "v_readfirstlane_b32 s94, v185\ns_cmp_ge_u32 s94, 2617600\ns_cbranch_scc1 4f\n3:\ns_sleep 1\nds_read_b32 v185, v191\ns_waitcnt lgkmcnt(0)\n" \
    "v_readfirstlane_b32 s94, v185\ns_cmp_lt_u32 s94, 2617600\ns_cbranch_scc1 3b\n4:\ns_waitcnt vmcnt(4)\n"
#define ATT_Y2 \
    "s_setprio 3\nv_exp_f32 v64, v64\nv_exp_f32 v65, v65\nv_smfmac_f32_32x32x32_bf16 v[144:159], v[180:183], v[120:127], v184\ns_mov_b64 exec, %[vm]\n" \
    "ds_read_b128 v[180:183], v189 offset:20736\ns_mov_b64 exec, -1\nv_exp_f32 v66, v66\nds_read_b32 v206, v241\nv_exp_f32 v67, v67\n" \
    "ds_read_b128 v[202:205], v240\nv_exp_f32 v68, v68\nv_mfma_f32_32x32x4_2b_bf16 v[96:127], v[174:175], v[168:169], 0 cbsz:1\n" \
    "ds_read_b32 v174, v187 offset:36864\nv_exp_f32 v69, v69\ns_add_u32 m0, %[m2], 0\nv_exp_f32 v70, v70\nglobal_load_lds_dwordx4 v190, %[sc] sc1\n" \
    "v_exp_f32 v71, v71\nv_mov_b32 v201, 0\nv_exp_f32 v72, v72\nv_mov_b32 v207, 0\nv_exp_f32 v73, v73\nv_exp_f32 v74, v74\nv_exp_f32 v75, v75\n" \
    "global_load_lds_dwordx4 v190, %[sc] offset:1024 sc1\nv_exp_f32 v76, v76\ns_waitcnt lgkmcnt(4)\n" \
    "v_smfmac_f32_32x32x32_bf16 v[128:143], v[176:179], v[64:71], v184\nv_exp_f32 v77, v77\nv_exp_f32 v78, v78\nv_exp_f32 v79, v79\ns_waitcnt lgkmcnt(1)\n" \
    "v_mfma_f32_32x32x4_2b_bf16 v[208:239], v[206:207], v[200:201], 0 cbsz:1\nv_exp_f32 v80, v80\nv_exp_f32 v81, v81\n" \
    "v_smfmac_f32_32x32x32_bf16 v[128:143], v[180:183], v[72:79], v184\nv_exp_f32 v82, v82\nv_exp_f32 v83, v83\n" \
    "global_load_lds_dwordx4 v190, %[sc] offset:2048 sc1\nv_exp_f32 v84, v84\nv_mfma_f32_32x32x16_bf16 v[96:111], v[170:173], v[160:163], v[96:111]\n" \
    "v_exp_f32 v85, v85\nv_exp_f32 v86, v86\nv_exp_f32 v87, v87\nv_exp_f32 v88, v88\n" \
    "v_mfma_f32_32x32x16_bf16 v[112:127], v[170:173], v[164:167], v[112:127]\nds_read_b128 v[170:173], v186 offset:36864\nv_exp_f32 v89, v89\n" \
    "v_exp_f32 v90, v90\nv_exp_f32 v91, v91\nv_smfmac_f32_32x32x32_bf16 v[144:159], v[176:179], v[80:87], v184\ns_mov_b64 exec, %[vm]\n" \
    "ds_read_b128 v[176:179], v188 offset:21888\ns_mov_b64 exec, -1\nv_exp_f32 v92, v92\nv_exp_f32 v93, v93\nv_exp_f32 v94, v94\n" \
    "v_mfma_f32_32x32x16_bf16 v[208:223], v[202:205], v[192:195], v[208:223]\nv_exp_f32 v95, v95\ns_setprio 2\nv_exp_f32 v96, v96\nv_exp_f32 v97, v97\n" \
    "v_smfmac_f32_32x32x32_bf16 v[144:159], v[180:183], v[88:95], v184\ns_mov_b64 exec, %[vm]\nds_read_b128 v[180:183], v189 offset:21888\n" \
    "s_mov_b64 exec, -1\nv_exp_f32 v98, v98\nv_exp_f32 v99, v99\nv_exp_f32 v100, v100\ns_waitcnt lgkmcnt(3)\n" \
    "v_mfma_f32_32x32x4_2b_bf16 v[64:95], v[174:175], v[168:169], 0 cbsz:1\nds_read_b32 v174, v187 offset:36992\nv_exp_f32 v101, v101\nv_exp_f32 v102, v102\n" \
    "v_exp_f32 v103, v103\nv_exp_f32 v104, v104\nv_exp_f32 v105, v105\nv_mfma_f32_32x32x16_bf16 v[224:239], v[202:205], v[196:199], v[224:239]\n" \
    "v_exp_f32 v106, v106\nv_exp_f32 v107, v107\nv_exp_f32 v108, v108\ns_waitcnt lgkmcnt(2)\n" \
    "v_smfmac_f32_32x32x32_bf16 v[128:143], v[176:179], v[96:103], v184\nv_exp_f32 v109, v109\nv_mov_b32 v219, v218\nv_exp_f32 v110, v110\n" \
    "v_exp_f32 v111, v111\nv_permlane32_swap_b32 v218, v219\nv_exp_f32 v112, v112\nv_add_f32 v218, 0x358637bd, v218\nv_exp_f32 v113, v113\n" \
    "s_waitcnt lgkmcnt(1)\nv_smfmac_f32_32x32x32_bf16 v[128:143], v[180:183], v[104:111], v184\nv_rcp_f32 v218, v218\nv_exp_f32 v114, v114\n" \
    "v_pk_mul_f32 v[208:209], v[208:209], v[218:219] op_sel_hi:[1,0]\nv_exp_f32 v115, v115\nv_pk_mul_f32 v[210:211], v[210:211], v[218:219] op_sel_hi:[1,0]\n" \
    "v_exp_f32 v116, v116\nv_mfma_f32_32x32x16_bf16 v[64:79], v[170:173], v[160:163], v[64:79]\n" \
    "v_pk_mul_f32 v[212:213], v[212:213], v[218:219] op_sel_hi:[1,0]\nv_exp_f32 v117, v117\nv_pk_mul_f32 v[214:215], v[214:215], v[218:219] op_sel_hi:[1,0]\n" \
    "v_exp_f32 v118, v118\nv_pk_mul_f32 v[216:217], v[216:217], v[218:219] op_sel_hi:[1,0]\nv_exp_f32 v119, v119\nv_cvt_pk_bf16_f32 v246, v208, v209\n" \
    "v_exp_f32 v120, v120\nv_mfma_f32_32x32x16_bf16 v[80:95], v[170:173], v[164:167], v[80:95]\nds_read_b128 v[170:173], v186 offset:37888\n" \
    "v_cvt_pk_bf16_f32 v247, v210, v211\nv_exp_f32 v121, v121\nv_cvt_pk_bf16_f32 v248, v212, v213\nv_exp_f32 v122, v122\nv_cvt_pk_bf16_f32 v249, v214, v215\n" \
    "v_exp_f32 v123, v123\nv_smfmac_f32_32x32x32_bf16 v[144:159], v[176:179], v[112:119], v184\ns_mov_b64 exec, %[vm]\n" \
    "ds_read_b128 v[176:179], v188 offset:36864\ns_mov_b64 exec, -1\nv_cvt_pk_bf16_f32 v250, v216, v217\nv_exp_f32 v124, v124\n" \
    "buffer_store_dwordx4 v[246:249], v242, %[rs], 0 offen sc1\nbuffer_store_dword v250, v243, %[rs], 0 offen sc1\nv_exp_f32 v125, v125\n" \
    "v_exp_f32 v126, v126\nv_exp_f32 v127, v127\ns_setprio 1\nv_exp_f32 v64, v64\nv_mov_b32 v235, v234\nv_exp_f32 v65, v65\n" \
    "v_smfmac_f32_32x32x32_bf16 v[144:159], v[180:183], v[120:127], v184\ns_mov_b64 exec, %[vm]\nds_read_b128 v[180:183], v189 offset:36864\n" \
    "s_mov_b64 exec, -1\nv_exp_f32 v66, v66\nv_permlane32_swap_b32 v234, v235\nv_exp_f32 v67, v67\nv_add_f32 v234, 0x358637bd, v234\nv_exp_f32 v68, v68\n" \
    "s_waitcnt lgkmcnt(3)\nv_mfma_f32_32x32x4_2b_bf16 v[96:127], v[174:175], v[168:169], 0 cbsz:1\nds_read_b32 v174, v187 offset:37120\n" \
    "v_rcp_f32 v234, v234\nv_exp_f32 v69, v69\nv_pk_mul_f32 v[224:225], v[224:225], v[234:235] op_sel_hi:[1,0]\nv_exp_f32 v70, v70\n" \
    "v_pk_mul_f32 v[226:227], v[226:227], v[234:235] op_sel_hi:[1,0]\nv_exp_f32 v71, v71\nv_pk_mul_f32 v[228:229], v[228:229], v[234:235] op_sel_hi:[1,0]\n" \
    "v_exp_f32 v72, v72\nv_pk_mul_f32 v[230:231], v[230:231], v[234:235] op_sel_hi:[1,0]\nv_exp_f32 v73, v73\n" \
    "v_pk_mul_f32 v[232:233], v[232:233], v[234:235] op_sel_hi:[1,0]\nv_exp_f32 v74, v74\nv_cvt_pk_bf16_f32 v192, v224, v225\nv_exp_f32 v75, v75\n" \
    "v_cvt_pk_bf16_f32 v193, v226, v227\nv_exp_f32 v76, v76\ns_waitcnt lgkmcnt(2)\nv_smfmac_f32_32x32x32_bf16 v[128:143], v[176:179], v[64:71], v184\n" \
    "v_cvt_pk_bf16_f32 v194, v228, v229\nv_exp_f32 v77, v77\nv_cvt_pk_bf16_f32 v195, v230, v231\nv_exp_f32 v78, v78\nv_cvt_pk_bf16_f32 v196, v232, v233\n" \
    "v_exp_f32 v79, v79\nbuffer_store_dwordx4 v[192:195], v244, %[rs], 0 offen sc1\nbuffer_store_dword v196, v245, %[rs], 0 offen sc1\nv_exp_f32 v80, v80\n" \
    "v_exp_f32 v81, v81\ns_waitcnt lgkmcnt(1)\nv_smfmac_f32_32x32x32_bf16 v[128:143], v[180:183], v[72:79], v184\nv_exp_f32 v82, v82\nv_exp_f32 v83, v83\n" \
    "v_exp_f32 v84, v84\nv_mfma_f32_32x32x16_bf16 v[96:111], v[170:173], v[160:163], v[96:111]\nv_exp_f32 v85, v85\nv_exp_f32 v86, v86\nv_exp_f32 v87, v87\n" \
    "v_exp_f32 v88, v88\nv_mfma_f32_32x32x16_bf16 v[112:127], v[170:173], v[164:167], v[112:127]\nds_read_b128 v[170:173], v186 offset:38912\n" \
    "v_exp_f32 v89, v89\nv_exp_f32 v90, v90\nv_exp_f32 v91, v91\nv_smfmac_f32_32x32x32_bf16 v[144:159], v[176:179], v[80:87], v184\ns_mov_b64 exec, %[vm]\n" \
    "ds_read_b128 v[176:179], v188 offset:38016\ns_mov_b64 exec, -1\nv_exp_f32 v92, v92\ns_add_u32 s98, s96, 0\ns_addc_u32 s99, s97, 0\n" \
    "s_add_u32 m0, %[m1], 55296\nv_exp_f32 v93, v93\nglobal_load_lds_dwordx4 %[w0], s[98:99]\nv_exp_f32 v94, v94\nv_exp_f32 v95, v95\ns_waitcnt vmcnt(5)\n" \
    "s_mov_b64 exec, 1\nds_add_u32 v191, v191\ns_mov_b64 exec, -1\ns_setprio 0\nv_exp_f32 v96, v96\nv_exp_f32 v97, v97\n" \
    "v_smfmac_f32_32x32x32_bf16 v[144:159], v[180:183], v[88:95], v184\ns_mov_b64 exec, %[vm]\nds_read_b128 v[180:183], v189 offset:38016\n" \
    "s_mov_b64 exec, -1\nv_exp_f32 v98, v98\nv_exp_f32 v99, v99\nv_exp_f32 v100, v100\ns_waitcnt lgkmcnt(4)\n" \
    "v_mfma_f32_32x32x4_2b_bf16 v[64:95], v[174:175], v[168:169], 0 cbsz:1\nds_read_b32 v174, v187 offset:37248\nv_exp_f32 v101, v101\n" \
    "s_add_u32 s98, s96, 82944\ns_addc_u32 s99, s97, 0\ns_add_u32 m0, %[m1], 64512\nv_exp_f32 v102, v102\nglobal_load_lds_dwordx4 %[w0], s[98:99]\n" \
    "v_exp_f32 v103, v103\nv_exp_f32 v104, v104\nv_exp_f32 v105, v105\nv_exp_f32 v106, v106\ns_add_u32 s98, s96, 165888\ns_addc_u32 s99, s97, 0\n" \
    "s_add_u32 m0, %[m1], 73728\nv_exp_f32 v107, v107\nglobal_load_lds_dwordx4 %[w0], s[98:99]\nv_exp_f32 v108, v108\ns_waitcnt lgkmcnt(3)\n" \
    "v_smfmac_f32_32x32x32_bf16 v[128:143], v[176:179], v[96:103], v184\nv_exp_f32 v109, v109\nv_exp_f32 v110, v110\nv_exp_f32 v111, v111\n" \
    "v_exp_f32 v112, v112\nv_exp_f32 v113, v113\ns_waitcnt lgkmcnt(1)\nv_smfmac_f32_32x32x32_bf16 v[128:143], v[180:183], v[104:111], v184\n" \
    "v_exp_f32 v114, v114\ns_add_u32 s98, s96, 248832\ns_addc_u32 s99, s97, 0\ns_add_u32 m0, %[m1], 82944\nv_exp_f32 v115, v115\n" \
    "global_load_lds_dwordx4 %[w0], s[98:99]\nv_exp_f32 v116, v116\nv_mfma_f32_32x32x16_bf16 v[64:79], v[170:173], v[160:163], v[64:79]\n" \
    "ds_read_b32 v185, v191\nv_exp_f32 v117, v117\nv_exp_f32 v118, v118\nv_exp_f32 v119, v119\nv_exp_f32 v120, v120\n" \
    "v_mfma_f32_32x32x16_bf16 v[80:95], v[170:173], v[164:167], v[80:95]\nds_read_b128 v[170:173], v186 offset:39936\nv_exp_f32 v121, v121\n" \
    "v_exp_f32 v122, v122\nv_exp_f32 v123, v123\nv_smfmac_f32_32x32x32_bf16 v[144:159], v[176:179], v[112:119], v184\ns_mov_b64 exec, %[vm]\n" \
    "ds_read_b128 v[176:179], v188 offset:39168\ns_mov_b64 exec, -1\nv_exp_f32 v124, v124\nv_exp_f32 v125, v125\ns_waitcnt vmcnt(4)\ns_mov_b64 exec, 3\n" \
    "global_store_dwordx2 %[fa], %[fv], %[fs] sc1\ns_mov_b64 exec, -1\nv_exp_f32 v126, v126\nv_exp_f32 v127, v127\ns_waitcnt lgkmcnt(2)\n" \
    "v_readfirstlane_b32 s94, v185\ns_cmp_ge_u32 s94, 3926400\ns_cbranch_scc1 4f\n3:\ns_sleep 1\nds_read_b32 v185, v191\ns_waitcnt lgkmcnt(0)\n" \
    "v_readfirstlane_b32 s94, v185\ns_cmp_lt_u32 s94, 3926400\ns_cbranch_scc1 3b\n4:\n"
#define ATT_Y3 \
    "s_setprio 3\nv_exp_f32 v64, v64\nv_exp_f32 v65, v65\nv_smfmac_f32_32x32x32_bf16 v[144:159], v[180:183], v[120:127], v184\ns_mov_b64 exec, %[vm]\n" \
    "ds_read_b128 v[180:183], v189 offset:39168\ns_mov_b64 exec, -1\nv_exp_f32 v66, v66\nv_exp_f32 v67, v67\nv_exp_f32 v68, v68\n" \
    "v_mfma_f32_32x32x4_2b_bf16 v[96:127], v[174:175], v[168:169], 0 cbsz:1\nds_read_b32 v174, v187 offset:0\nv_exp_f32 v69, v69\n" \
    "s_add_u32 m0, %[m2], 18432\nv_exp_f32 v70, v70\nglobal_load_lds_dwordx4 v190, %[sc] sc1\nv_exp_f32 v71, v71\nv_exp_f32 v72, v72\nv_exp_f32 v73, v73\n" \
    "v_exp_f32 v74, v74\nv_exp_f32 v75, v75\nglobal_load_lds_dwordx4 v190, %[sc] offset:1024 sc1\nv_exp_f32 v76, v76\ns_waitcnt lgkmcnt(2)\n" \
    "v_smfmac_f32_32x32x32_bf16 v[128:143], v[176:179], v[64:71], v184\nv_exp_f32 v77, v77\nv_exp_f32 v78, v78\nv_exp_f32 v79, v79\nv_exp_f32 v80, v80\n" \
    "v_exp_f32 v81, v81\ns_waitcnt lgkmcnt(1)\nv_smfmac_f32_32x32x32_bf16 v[128:143], v[180:183], v[72:79], v184\nv_exp_f32 v82, v82\nv_exp_f32 v83, v83\n" \
    "global_load_lds_dwordx4 v190, %[sc] offset:2048 sc1\nv_exp_f32 v84, v84\nv_mfma_f32_32x32x16_bf16 v[96:111], v[170:173], v[160:163], v[96:111]\n" \
    "v_exp_f32 v85, v85\nv_exp_f32 v86, v86\nv_exp_f32 v87, v87\nv_exp_f32 v88, v88\n" \
    "v_mfma_f32_32x32x16_bf16 v[112:127], v[170:173], v[164:167], v[112:127]\nds_read_b128 v[170:173], v186 offset:0\nv_exp_f32 v89, v89\n" \
    "v_exp_f32 v90, v90\nv_exp_f32 v91, v91\nv_smfmac_f32_32x32x32_bf16 v[144:159], v[176:179], v[80:87], v184\ns_mov_b64 exec, %[vm]\n" \
    "ds_read_b128 v[176:179], v188 offset:40320\ns_mov_b64 exec, -1\nv_exp_f32 v92, v92\ns_add_u32 s98, s96, 331776\ns_addc_u32 s99, s97, 0\n" \
    "s_add_u32 m0, %[m1], 92160\nv_exp_f32 v93, v93\nglobal_load_lds_dwordx4 %[w0], s[98:99]\nv_exp_f32 v94, v94\nv_exp_f32 v95, v95\ns_setprio 2\n" \
    "v_exp_f32 v96, v96\nv_exp_f32 v97, v97\nv_smfmac_f32_32x32x32_bf16 v[144:159], v[180:183], v[88:95], v184\ns_mov_b64 exec, %[vm]\n" \
    "ds_read_b128 v[180:183], v189 offset:40320\ns_mov_b64 exec, -1\nv_exp_f32 v98, v98\nv_exp_f32 v99, v99\nv_exp_f32 v100, v100\ns_waitcnt lgkmcnt(3)\n" \
    "v_mfma_f32_32x32x4_2b_bf16 v[64:95], v[174:175], v[168:169], 0 cbsz:1\nds_read_b32 v174, v187 offset:128\nv_exp_f32 v101, v101\nv_exp_f32 v102, v102\n" \
    "v_exp_f32 v103, v103\nv_exp_f32 v104, v104\nv_exp_f32 v105, v105\nv_exp_f32 v106, v106\nv_exp_f32 v107, v107\nv_exp_f32 v108, v108\n" \
    "s_waitcnt lgkmcnt(2)\nv_smfmac_f32_32x32x32_bf16 v[128:143], v[176:179], v[96:103], v184\nv_exp_f32 v109, v109\nv_exp_f32 v110, v110\n" \
    "v_exp_f32 v111, v111\nv_exp_f32 v112, v112\nv_exp_f32 v113, v113\ns_waitcnt lgkmcnt(1)\n" \
    "v_smfmac_f32_32x32x32_bf16 v[128:143], v[180:183], v[104:111], v184\nv_exp_f32 v114, v114\nv_exp_f32 v115, v115\nv_exp_f32 v116, v116\n" \
    "v_mfma_f32_32x32x16_bf16 v[64:79], v[170:173], v[160:163], v[64:79]\nv_exp_f32 v117, v117\nv_exp_f32 v118, v118\nv_exp_f32 v119, v119\n" \
    "v_exp_f32 v120, v120\nv_mfma_f32_32x32x16_bf16 v[80:95], v[170:173], v[164:167], v[80:95]\nds_read_b128 v[170:173], v186 offset:1024\n" \
    "v_exp_f32 v121, v121\nv_exp_f32 v122, v122\nv_exp_f32 v123, v123\nv_smfmac_f32_32x32x32_bf16 v[144:159], v[176:179], v[112:119], v184\n" \
    "s_mov_b64 exec, %[vm]\nds_read_b128 v[176:179], v188 offset:0\ns_mov_b64 exec, -1\nv_exp_f32 v124, v124\nv_exp_f32 v125, v125\nv_exp_f32 v126, v126\n" \
    "v_exp_f32 v127, v127\ns_setprio 1\nv_exp_f32 v64, v64\nv_exp_f32 v65, v65\nv_smfmac_f32_32x32x32_bf16 v[144:159], v[180:183], v[120:127], v184\n" \
    "s_mov_b64 exec, %[vm]\nds_read_b128 v[180:183], v189 offset:0\ns_mov_b64 exec, -1\nv_exp_f32 v66, v66\nv_exp_f32 v67, v67\nv_exp_f32 v68, v68\n" \
    "s_waitcnt lgkmcnt(3)\nv_mfma_f32_32x32x4_2b_bf16 v[96:127], v[174:175], v[168:169], 0 cbsz:1\nds_read_b32 v174, v187 offset:256\nv_exp_f32 v69, v69\n" \
    "s_mov_b32 m0, %[mr]\nv_exp_f32 v70, v70\nglobal_load_lds_dwordx4 %[w2], s[96:97]\nv_exp_f32 v71, v71\nv_exp_f32 v72, v72\nv_exp_f32 v73, v73\n" \
    "v_exp_f32 v74, v74\ns_add_u32 s98, s96, 414720\ns_addc_u32 s99, s97, 0\ns_add_u32 m0, %[m1], 101376\nv_exp_f32 v75, v75\n" \
    "global_load_lds_dwordx4 %[w0], s[98:99]\nv_exp_f32 v76, v76\ns_waitcnt lgkmcnt(2)\nv_smfmac_f32_32x32x32_bf16 v[128:143], v[176:179], v[64:71], v184\n" \
    "v_exp_f32 v77, v77\nv_exp_f32 v78, v78\nv_exp_f32 v79, v79\nv_exp_f32 v80, v80\nv_exp_f32 v81, v81\ns_waitcnt lgkmcnt(1)\n" \
    "v_smfmac_f32_32x32x32_bf16 v[128:143], v[180:183], v[72:79], v184\nv_exp_f32 v82, v82\nv_exp_f32 v83, v83\n" \
    "global_load_dwordx2 v[192:193], %[pa], %[pb] sc1\nv_exp_f32 v84, v84\nv_mfma_f32_32x32x16_bf16 v[96:111], v[170:173], v[160:163], v[96:111]\n" \
    "v_exp_f32 v85, v85\nv_exp_f32 v86, v86\nv_exp_f32 v87, v87\nv_exp_f32 v88, v88\n" \
    "v_mfma_f32_32x32x16_bf16 v[112:127], v[170:173], v[164:167], v[112:127]\nds_read_b128 v[170:173], v186 offset:2048\nv_exp_f32 v89, v89\n" \
    "v_exp_f32 v90, v90\nv_exp_f32 v91, v91\nv_smfmac_f32_32x32x32_bf16 v[144:159], v[176:179], v[80:87], v184\ns_mov_b64 exec, %[vm]\n" \
    "ds_read_b128 v[176:179], v188 offset:1152\ns_mov_b64 exec, -1\nv_exp_f32 v92, v92\ns_add_u32 s98, s96, 497664\ns_addc_u32 s99, s97, 0\n" \
    "s_add_u32 m0, %[m1], 110592\nv_exp_f32 v93, v93\nglobal_load_lds_dwordx4 %[w0], s[98:99]\nv_exp_f32 v94, v94\nv_exp_f32 v95, v95\ns_waitcnt vmcnt(5)\n" \
    "s_mov_b64 exec, 1\nds_add_u32 v191, v191\ns_mov_b64 exec, -1\ns_setprio 0\nv_exp_f32 v96, v96\nv_exp_f32 v97, v97\n" \
    "v_smfmac_f32_32x32x32_bf16 v[144:159], v[180:183], v[88:95], v184\ns_mov_b64 exec, %[vm]\nds_read_b128 v[180:183], v189 offset:1152\n" \
    "s_mov_b64 exec, -1\nv_exp_f32 v98, v98\nv_exp_f32 v99, v99\nv_exp_f32 v100, v100\ns_waitcnt lgkmcnt(4)\n" \
    "v_mfma_f32_32x32x4_2b_bf16 v[64:95], v[174:175], v[168:169], 0 cbsz:1\nds_read_b32 v174, v187 offset:384\nv_exp_f32 v101, v101\n" \
    "s_add_u32 s98, s96, 580608\ns_addc_u32 s99, s97, 0\ns_add_u32 m0, %[m1], 119808\nv_exp_f32 v102, v102\nglobal_load_lds_dwordx4 %[w0], s[98:99]\n" \
    "v_exp_f32 v103, v103\nv_exp_f32 v104, v104\nv_exp_f32 v105, v105\nv_exp_f32 v106, v106\ns_add_u32 m0, %[m1], 129024\nv_exp_f32 v107, v107\n" \
    "global_load_lds_dwordx4 %[w1], s[96:97]\nv_exp_f32 v108, v108\ns_waitcnt lgkmcnt(3)\n" \
    "v_smfmac_f32_32x32x32_bf16 v[128:143], v[176:179], v[96:103], v184\nv_exp_f32 v109, v109\nv_exp_f32 v110, v110\nv_exp_f32 v111, v111\n" \
    "v_exp_f32 v112, v112\nv_exp_f32 v113, v113\ns_waitcnt lgkmcnt(1)\nv_smfmac_f32_32x32x32_bf16 v[128:143], v[180:183], v[104:111], v184\n" \
    "v_exp_f32 v114, v114\nv_exp_f32 v115, v115\nv_exp_f32 v116, v116\nv_mfma_f32_32x32x16_bf16 v[64:79], v[170:173], v[160:163], v[64:79]\n" \
    "ds_read_b32 v185, v191\nv_exp_f32 v117, v117\nv_exp_f32 v118, v118\nv_exp_f32 v119, v119\nv_exp_f32 v120, v120\n" \
    "v_mfma_f32_32x32x16_bf16 v[80:95], v[170:173], v[164:167], v[80:95]\nds_read_b128 v[170:173], v186 offset:3072\nv_exp_f32 v121, v121\n" \
    "v_exp_f32 v122, v122\nv_exp_f32 v123, v123\nv_smfmac_f32_32x32x32_bf16 v[144:159], v[176:179], v[112:119], v184\ns_mov_b64 exec, %[vm]\n" \
    "ds_read_b128 v[176:179], v188 offset:2304\ns_mov_b64 exec, -1\nv_exp_f32 v124, v124\nv_exp_f32 v125, v125\nv_exp_f32 v126, v126\nv_exp_f32 v127, v127\n" \
    "s_waitcnt lgkmcnt(2)\nv_readfirstlane_b32 s94, v185\ns_cmp_ge_u32 s94, 5235200\ns_cbranch_scc1 4f\n3:\ns_sleep 1\nds_read_b32 v185, v191\n" \
    "s_waitcnt lgkmcnt(0)\nv_readfirstlane_b32 s94, v185\ns_cmp_lt_u32 s94, 5235200\ns_cbranch_scc1 3b\n4:\ns_waitcnt vmcnt(3)\ns_setprio 3\n"
#define ATT_Y4 \
    "s_setprio 3\nv_exp_f32 v64, v64\nv_exp_f32 v65, v65\nv_smfmac_f32_32x32x32_bf16 v[144:159], v[180:183], v[120:127], v184\ns_mov_b64 exec, %[vm]\n" \
    "ds_read_b128 v[180:183], v189 offset:2304\ns_mov_b64 exec, -1\nv_exp_f32 v66, v66\nv_exp_f32 v67, v67\nv_exp_f32 v68, v68\n" \
    "v_mfma_f32_32x32x4_2b_bf16 v[96:127], v[174:175], v[168:169], 0 cbsz:1\nds_read_b32 v174, v187 offset:18432\nv_exp_f32 v69, v69\n" \
    "s_add_u32 m0, %[m2], 36864\nv_exp_f32 v70, v70\nglobal_load_lds_dwordx4 v190, %[sc] sc1\nv_exp_f32 v71, v71\nv_exp_f32 v72, v72\nv_exp_f32 v73, v73\n" \
    "v_exp_f32 v74, v74\nv_exp_f32 v75, v75\nglobal_load_lds_dwordx4 v190, %[sc] offset:1024 sc1\nv_exp_f32 v76, v76\ns_waitcnt lgkmcnt(2)\n" \
    "v_smfmac_f32_32x32x32_bf16 v[128:143], v[176:179], v[64:71], v184\nv_exp_f32 v77, v77\nv_exp_f32 v78, v78\nv_exp_f32 v79, v79\nv_exp_f32 v80, v80\n" \
    "v_exp_f32 v81, v81\ns_waitcnt lgkmcnt(1)\nv_smfmac_f32_32x32x32_bf16 v[128:143], v[180:183], v[72:79], v184\nv_exp_f32 v82, v82\nv_exp_f32 v83, v83\n" \
    "global_load_lds_dwordx4 v190, %[sc] offset:2048 sc1\nv_exp_f32 v84, v84\nv_mfma_f32_32x32x16_bf16 v[96:111], v[170:173], v[160:163], v[96:111]\n" \
    "v_exp_f32 v85, v85\nv_exp_f32 v86, v86\nv_exp_f32 v87, v87\nv_exp_f32 v88, v88\n" \
    "v_mfma_f32_32x32x16_bf16 v[112:127], v[170:173], v[164:167], v[112:127]\nds_read_b128 v[170:173], v186 offset:18432\nv_exp_f32 v89, v89\n" \
    "v_exp_f32 v90, v90\nv_exp_f32 v91, v91\nv_smfmac_f32_32x32x32_bf16 v[144:159], v[176:179], v[80:87], v184\ns_mov_b64 exec, %[vm]\n" \
    "ds_read_b128 v[176:179], v188 offset:3456\ns_mov_b64 exec, -1\nv_exp_f32 v92, v92\nv_exp_f32 v93, v93\n" \
    "buffer_load_dword v224, %[ar], %[rs], 0 offen offset:0 sc1\nbuffer_load_dword v225, %[ar], %[rs], 0 offen offset:128 sc1\nv_exp_f32 v94, v94\n" \
    "v_exp_f32 v95, v95\ns_setprio 2\nv_exp_f32 v96, v96\nv_exp_f32 v97, v97\nv_smfmac_f32_32x32x32_bf16 v[144:159], v[180:183], v[88:95], v184\n" \
    "s_mov_b64 exec, %[vm]\nds_read_b128 v[180:183], v189 offset:3456\ns_mov_b64 exec, -1\nv_exp_f32 v98, v98\nv_exp_f32 v99, v99\nv_exp_f32 v100, v100\n" \
    "s_waitcnt lgkmcnt(3)\nv_mfma_f32_32x32x4_2b_bf16 v[64:95], v[174:175], v[168:169], 0 cbsz:1\nds_read_b32 v174, v187 offset:18560\nv_exp_f32 v101, v101\n" \
    "v_exp_f32 v102, v102\nbuffer_load_dword v226, %[ar], %[rs], 0 offen offset:256 sc1\nbuffer_load_dword v227, %[ar], %[rs], 0 offen offset:384 sc1\n" \
    "v_exp_f32 v103, v103\nv_exp_f32 v104, v104\nv_exp_f32 v105, v105\nv_exp_f32 v106, v106\nv_exp_f32 v107, v107\n" \
    "buffer_load_dword v254, %[ag], %[rs], 0 offen sc1\nv_exp_f32 v108, v108\ns_waitcnt lgkmcnt(2)\n" \
    "v_smfmac_f32_32x32x32_bf16 v[128:143], v[176:179], v[96:103], v184\nv_exp_f32 v109, v109\nv_exp_f32 v110, v110\nv_exp_f32 v111, v111\n" \
    "v_exp_f32 v112, v112\nv_exp_f32 v113, v113\ns_waitcnt lgkmcnt(1)\nv_smfmac_f32_32x32x32_bf16 v[128:143], v[180:183], v[104:111], v184\n" \
    "v_exp_f32 v114, v114\nv_exp_f32 v115, v115\nbuffer_load_dwordx4 v[192:195], %[a0], %[rs], 0 offen offset:0 sc1\n" \
    "buffer_load_dwordx4 v[196:199], %[a0], %[rs], 0 offen offset:1024 sc1\nv_exp_f32 v116, v116\n" \
    "v_mfma_f32_32x32x16_bf16 v[64:79], v[170:173], v[160:163], v[64:79]\nv_exp_f32 v117, v117\nv_exp_f32 v118, v118\nv_exp_f32 v119, v119\n" \
    "v_exp_f32 v120, v120\nv_mfma_f32_32x32x16_bf16 v[80:95], v[170:173], v[164:167], v[80:95]\nds_read_b128 v[170:173], v186 offset:19456\n" \
    "v_exp_f32 v121, v121\nv_exp_f32 v122, v122\nv_exp_f32 v123, v123\nv_smfmac_f32_32x32x32_bf16 v[144:159], v[176:179], v[112:119], v184\n" \
    "s_mov_b64 exec, %[vm]\nds_read_b128 v[176:179], v188 offset:18432\ns_mov_b64 exec, -1\nv_exp_f32 v124, v124\nv_exp_f32 v125, v125\n" \
    "buffer_load_dwordx4 v[200:203], %[a0], %[rs], 0 offen offset:2048 sc1\nbuffer_load_dwordx4 v[204:207], %[a0], %[rs], 0 offen offset:3072 sc1\n" \
    "v_exp_f32 v126, v126\nv_exp_f32 v127, v127\ns_setprio 1\nv_exp_f32 v64, v64\nv_exp_f32 v65, v65\n" \
    "v_smfmac_f32_32x32x32_bf16 v[144:159], v[180:183], v[120:127], v184\ns_mov_b64 exec, %[vm]\nds_read_b128 v[180:183], v189 offset:18432\n" \
    "s_mov_b64 exec, -1\nv_exp_f32 v66, v66\nv_exp_f32 v67, v67\nv_exp_f32 v68, v68\ns_waitcnt lgkmcnt(3)\n" \
    "v_mfma_f32_32x32x4_2b_bf16 v[96:127], v[174:175], v[168:169], 0 cbsz:1\nds_read_b32 v174, v187 offset:18688\nv_exp_f32 v69, v69\nv_exp_f32 v70, v70\n" \
    "buffer_load_dwordx4 v[208:211], %[a1], %[rs], 0 offen offset:0 sc1\nbuffer_load_dwordx4 v[212:215], %[a1], %[rs], 0 offen offset:1024 sc1\n" \
    "v_exp_f32 v71, v71\nv_exp_f32 v72, v72\nv_exp_f32 v73, v73\nv_exp_f32 v74, v74\nv_exp_f32 v75, v75\n" \
    "buffer_load_dwordx4 v[216:219], %[a1], %[rs], 0 offen offset:2048 sc1\nbuffer_load_dwordx4 v[220:223], %[a1], %[rs], 0 offen offset:3072 sc1\n" \
    "v_exp_f32 v76, v76\ns_waitcnt lgkmcnt(2)\nv_smfmac_f32_32x32x32_bf16 v[128:143], v[176:179], v[64:71], v184\nv_exp_f32 v77, v77\nv_exp_f32 v78, v78\n" \
    "v_exp_f32 v79, v79\nv_exp_f32 v80, v80\nv_exp_f32 v81, v81\ns_waitcnt lgkmcnt(1)\nv_smfmac_f32_32x32x32_bf16 v[128:143], v[180:183], v[72:79], v184\n" \
    "v_exp_f32 v82, v82\nv_exp_f32 v83, v83\nv_exp_f32 v84, v84\nv_mfma_f32_32x32x16_bf16 v[96:111], v[170:173], v[160:163], v[96:111]\nv_exp_f32 v85, v85\n" \
    "v_exp_f32 v86, v86\nv_exp_f32 v87, v87\nv_exp_f32 v88, v88\nv_mfma_f32_32x32x16_bf16 v[112:127], v[170:173], v[164:167], v[112:127]\n" \
    "ds_read_b128 v[170:173], v186 offset:20480\nv_exp_f32 v89, v89\nv_exp_f32 v90, v90\nv_exp_f32 v91, v91\n" \
    "v_smfmac_f32_32x32x32_bf16 v[144:159], v[176:179], v[80:87], v184\ns_mov_b64 exec, %[vm]\nds_read_b128 v[176:179], v188 offset:19584\n" \
    "s_mov_b64 exec, -1\nv_exp_f32 v92, v92\nv_exp_f32 v93, v93\nv_exp_f32 v94, v94\nv_exp_f32 v95, v95\ns_waitcnt vmcnt(13)\ns_mov_b64 exec, 1\n" \
    "ds_add_u32 v191, v191\ns_mov_b64 exec, -1\ns_setprio 0\nv_exp_f32 v96, v96\nv_exp_f32 v97, v97\n" \
    "v_smfmac_f32_32x32x32_bf16 v[144:159], v[180:183], v[88:95], v184\ns_mov_b64 exec, %[vm]\nds_read_b128 v[180:183], v189 offset:19584\n" \
    "s_mov_b64 exec, -1\nv_exp_f32 v98, v98\nv_exp_f32 v99, v99\nv_exp_f32 v100, v100\ns_waitcnt lgkmcnt(4)\n" \
    "v_mfma_f32_32x32x4_2b_bf16 v[64:95], v[174:175], v[168:169], 0 cbsz:1\nds_read_b32 v174, v187 offset:18816\nv_exp_f32 v101, v101\nv_exp_f32 v102, v102\n" \
    "v_exp_f32 v103, v103\nv_exp_f32 v104, v104\nv_exp_f32 v105, v105\nv_exp_f32 v106, v106\nv_exp_f32 v107, v107\nv_exp_f32 v108, v108\n" \
    "s_waitcnt lgkmcnt(3)\nv_smfmac_f32_32x32x32_bf16 v[128:143], v[176:179], v[96:103], v184\nv_exp_f32 v109, v109\nv_exp_f32 v110, v110\n" \
    "v_exp_f32 v111, v111\nv_exp_f32 v112, v112\nv_exp_f32 v113, v113\ns_waitcnt lgkmcnt(1)\n" \
    "v_smfmac_f32_32x32x32_bf16 v[128:143], v[180:183], v[104:111], v184\nv_exp_f32 v114, v114\nv_exp_f32 v115, v115\nv_exp_f32 v116, v116\n" \
    "v_mfma_f32_32x32x16_bf16 v[64:79], v[170:173], v[160:163], v[64:79]\nds_read_b32 v185, v191\nv_exp_f32 v117, v117\nv_exp_f32 v118, v118\n" \
    "v_exp_f32 v119, v119\nv_exp_f32 v120, v120\nv_mfma_f32_32x32x16_bf16 v[80:95], v[170:173], v[164:167], v[80:95]\n" \
    "ds_read_b128 v[170:173], v186 offset:21504\nv_exp_f32 v121, v121\nv_exp_f32 v122, v122\nv_exp_f32 v123, v123\n" \
    "v_smfmac_f32_32x32x32_bf16 v[144:159], v[176:179], v[112:119], v184\ns_mov_b64 exec, %[vm]\nds_read_b128 v[176:179], v188 offset:20736\n" \
    "s_mov_b64 exec, -1\nv_exp_f32 v124, v124\nv_exp_f32 v125, v125\nv_exp_f32 v126, v126\nv_exp_f32 v127, v127\ns_waitcnt lgkmcnt(2)\n" \
    "v_readfirstlane_b32 s94, v185\ns_cmp_ge_u32 s94, 6544000\ns_cbranch_scc1 4f\n3:\ns_sleep 1\nds_read_b32 v185, v191\ns_waitcnt lgkmcnt(0)\n" \
    "v_readfirstlane_b32 s94, v185\ns_cmp_lt_u32 s94, 6544000\ns_cbranch_scc1 3b\n4:\ns_waitcnt vmcnt(0)\n"
#define ATT_Y6 \
    "s_setprio 3\nv_mfma_f32_16x16x32_bf16 v[236:239], v[52:55], v[224:227], v[236:239]\nv_exp_f32 v64, v64\n" \
    "v_mfma_f32_16x16x32_bf16 v[240:243], v[56:59], v[224:227], v[240:243]\nv_exp_f32 v65, v65\n" \
    "v_smfmac_f32_32x32x32_bf16 v[144:159], v[180:183], v[120:127], v184\ns_mov_b64 exec, %[vm]\nds_read_b128 v[180:183], v189 offset:39168\n" \
    "s_mov_b64 exec, -1\nv_exp_f32 v66, v66\nv_exp_f32 v67, v67\nv_exp_f32 v68, v68\nv_mfma_f32_32x32x4_2b_bf16 v[96:127], v[174:175], v[168:169], 0 cbsz:1\n" \
    "ds_read_b32 v174, v187 offset:0\nv_exp_f32 v69, v69\ns_add_u32 m0, %[m2], 18432\nv_exp_f32 v70, v70\nglobal_load_lds_dwordx4 v190, %[sc] sc1\n" \
    "v_exp_f32 v71, v71\nv_exp_f32 v72, v72\nv_exp_f32 v73, v73\nv_exp_f32 v74, v74\nv_exp_f32 v75, v75\n" \
    "global_load_lds_dwordx4 v190, %[sc] offset:1024 sc1\nv_exp_f32 v76, v76\ns_waitcnt lgkmcnt(2)\n" \
    "v_smfmac_f32_32x32x32_bf16 v[128:143], v[176:179], v[64:71], v184\nv_exp_f32 v77, v77\nv_exp_f32 v78, v78\nv_exp_f32 v79, v79\n" \
    "v_mfma_f32_16x16x32_bf16 v[244:247], v[60:63], v[224:227], v[244:247]\nv_exp_f32 v80, v80\nv_exp_f32 v81, v81\ns_waitcnt lgkmcnt(1)\n" \
    "v_smfmac_f32_32x32x32_bf16 v[128:143], v[180:183], v[72:79], v184\nv_exp_f32 v82, v82\nv_exp_f32 v83, v83\n" \
    "global_load_lds_dwordx4 v190, %[sc] offset:2048 sc1\nv_exp_f32 v84, v84\nv_mfma_f32_32x32x16_bf16 v[96:111], v[170:173], v[160:163], v[96:111]\n" \
    "v_exp_f32 v85, v85\nv_exp_f32 v86, v86\nv_exp_f32 v87, v87\nv_exp_f32 v88, v88\n" \
    "v_mfma_f32_32x32x16_bf16 v[112:127], v[170:173], v[164:167], v[112:127]\nds_read_b128 v[170:173], v186 offset:0\nv_exp_f32 v89, v89\n" \
    "v_exp_f32 v90, v90\nv_exp_f32 v91, v91\nv_smfmac_f32_32x32x32_bf16 v[144:159], v[176:179], v[80:87], v184\ns_mov_b64 exec, %[vm]\n" \
    "ds_read_b128 v[176:179], v188 offset:40320\ns_mov_b64 exec, -1\nv_exp_f32 v92, v92\nv_exp_f32 v93, v93\nv_exp_f32 v94, v94\nv_exp_f32 v95, v95\n" \
    "s_setprio 2\nv_exp_f32 v96, v96\nv_exp_f32 v97, v97\nv_smfmac_f32_32x32x32_bf16 v[144:159], v[180:183], v[88:95], v184\ns_mov_b64 exec, %[vm]\n" \
    "ds_read_b128 v[180:183], v189 offset:40320\ns_mov_b64 exec, -1\nv_exp_f32 v98, v98\nv_exp_f32 v99, v99\nv_exp_f32 v100, v100\ns_waitcnt lgkmcnt(3)\n" \
    "v_mfma_f32_32x32x4_2b_bf16 v[64:95], v[174:175], v[168:169], 0 cbsz:1\nds_read_b32 v174, v187 offset:128\nv_exp_f32 v101, v101\nv_exp_f32 v102, v102\n" \
    "v_exp_f32 v103, v103\nv_exp_f32 v104, v104\nv_exp_f32 v105, v105\nv_exp_f32 v106, v106\nv_exp_f32 v107, v107\nv_exp_f32 v108, v108\n" \
    "s_waitcnt lgkmcnt(2)\nv_smfmac_f32_32x32x32_bf16 v[128:143], v[176:179], v[96:103], v184\nv_exp_f32 v109, v109\nv_exp_f32 v110, v110\n" \
    "v_exp_f32 v111, v111\nv_exp_f32 v112, v112\nv_exp_f32 v113, v113\ns_waitcnt lgkmcnt(1)\n" \
    "v_smfmac_f32_32x32x32_bf16 v[128:143], v[180:183], v[104:111], v184\nv_exp_f32 v114, v114\nv_exp_f32 v115, v115\nv_exp_f32 v116, v116\n" \
    "v_mfma_f32_32x32x16_bf16 v[64:79], v[170:173], v[160:163], v[64:79]\nv_exp_f32 v117, v117\nv_exp_f32 v118, v118\nv_exp_f32 v119, v119\n" \
    "v_exp_f32 v120, v120\nv_mfma_f32_32x32x16_bf16 v[80:95], v[170:173], v[164:167], v[80:95]\nds_read_b128 v[170:173], v186 offset:1024\n" \
    "v_exp_f32 v121, v121\nv_exp_f32 v122, v122\nv_exp_f32 v123, v123\nv_smfmac_f32_32x32x32_bf16 v[144:159], v[176:179], v[112:119], v184\n" \
    "s_mov_b64 exec, %[vm]\nds_read_b128 v[176:179], v188 offset:0\ns_mov_b64 exec, -1\nv_exp_f32 v124, v124\nv_exp_f32 v125, v125\nv_exp_f32 v126, v126\n" \
    "v_exp_f32 v127, v127\ns_setprio 1\nv_exp_f32 v64, v64\nv_exp_f32 v65, v65\nv_smfmac_f32_32x32x32_bf16 v[144:159], v[180:183], v[120:127], v184\n" \
    "s_mov_b64 exec, %[vm]\nds_read_b128 v[180:183], v189 offset:0\ns_mov_b64 exec, -1\nv_exp_f32 v66, v66\nv_exp_f32 v67, v67\nv_exp_f32 v68, v68\n" \
    "s_waitcnt lgkmcnt(3)\nv_mfma_f32_32x32x4_2b_bf16 v[96:127], v[174:175], v[168:169], 0 cbsz:1\nds_read_b32 v174, v187 offset:256\nv_exp_f32 v69, v69\n" \
    "s_add_u32 s98, s96, 0\ns_addc_u32 s99, s97, 0\ns_add_u32 m0, %[m1], 55296\nv_exp_f32 v70, v70\nglobal_load_lds_dwordx4 %[w0], s[98:99]\n" \
    "v_exp_f32 v71, v71\nv_exp_f32 v72, v72\nv_exp_f32 v73, v73\nv_exp_f32 v74, v74\ns_add_u32 s98, s96, 82944\ns_addc_u32 s99, s97, 0\n" \
    "s_add_u32 m0, %[m1], 64512\nv_exp_f32 v75, v75\nglobal_load_lds_dwordx4 %[w0], s[98:99]\nv_exp_f32 v76, v76\ns_waitcnt lgkmcnt(2)\n" \
    "v_smfmac_f32_32x32x32_bf16 v[128:143], v[176:179], v[64:71], v184\nv_exp_f32 v77, v77\nv_exp_f32 v78, v78\nv_exp_f32 v79, v79\nv_exp_f32 v80, v80\n" \
    "v_exp_f32 v81, v81\ns_waitcnt lgkmcnt(1)\nv_smfmac_f32_32x32x32_bf16 v[128:143], v[180:183], v[72:79], v184\nv_exp_f32 v82, v82\n" \
    "s_add_u32 s98, s96, 331776\ns_addc_u32 s99, s97, 0\ns_add_u32 m0, %[m1], 92160\nv_exp_f32 v83, v83\nglobal_load_lds_dwordx4 %[w0], s[98:99]\n" \
    "v_exp_f32 v84, v84\nv_mfma_f32_32x32x16_bf16 v[96:111], v[170:173], v[160:163], v[96:111]\nv_exp_f32 v85, v85\nv_exp_f32 v86, v86\nv_exp_f32 v87, v87\n" \
    "v_exp_f32 v88, v88\nv_mfma_f32_32x32x16_bf16 v[112:127], v[170:173], v[164:167], v[112:127]\nds_read_b128 v[170:173], v186 offset:2048\n" \
    "v_exp_f32 v89, v89\nv_exp_f32 v90, v90\nv_exp_f32 v91, v91\nv_smfmac_f32_32x32x32_bf16 v[144:159], v[176:179], v[80:87], v184\ns_mov_b64 exec, %[vm]\n" \
    "ds_read_b128 v[176:179], v188 offset:1152\ns_mov_b64 exec, -1\nv_exp_f32 v92, v92\ns_add_u32 s98, s96, 414720\ns_addc_u32 s99, s97, 0\n" \
    "s_add_u32 m0, %[m1], 101376\nv_exp_f32 v93, v93\nglobal_load_lds_dwordx4 %[w0], s[98:99]\nv_exp_f32 v94, v94\nv_exp_f32 v95, v95\ns_waitcnt vmcnt(4)\n" \
    "s_mov_b64 exec, 1\nds_add_u32 v191, v191\ns_mov_b64 exec, -1\ns_setprio 0\nv_exp_f32 v96, v96\nv_exp_f32 v97, v97\n" \
    "v_smfmac_f32_32x32x32_bf16 v[144:159], v[180:183], v[88:95], v184\ns_mov_b64 exec, %[vm]\nds_read_b128 v[180:183], v189 offset:1152\n" \
    "s_mov_b64 exec, -1\nv_exp_f32 v98, v98\nv_exp_f32 v99, v99\nv_exp_f32 v100, v100\ns_waitcnt lgkmcnt(4)\n" \
    "v_mfma_f32_32x32x4_2b_bf16 v[64:95], v[174:175], v[168:169], 0 cbsz:1\nds_read_b32 v174, v187 offset:384\nv_exp_f32 v101, v101\nv_exp_f32 v102, v102\n" \
    "v_exp_f32 v103, v103\nv_exp_f32 v104, v104\nv_exp_f32 v105, v105\nv_exp_f32 v106, v106\nv_exp_f32 v107, v107\nv_exp_f32 v108, v108\n" \
    "s_waitcnt lgkmcnt(3)\nv_smfmac_f32_32x32x32_bf16 v[128:143], v[176:179], v[96:103], v184\nv_exp_f32 v109, v109\nv_exp_f32 v110, v110\n" \
    "v_exp_f32 v111, v111\nv_exp_f32 v112, v112\nv_exp_f32 v113, v113\ns_waitcnt lgkmcnt(1)\n" \
    "v_smfmac_f32_32x32x32_bf16 v[128:143], v[180:183], v[104:111], v184\nv_exp_f32 v114, v114\ns_add_u32 s98, s96, 165888\ns_addc_u32 s99, s97, 0\n" \
    "s_add_u32 m0, %[m1], 138240\nv_exp_f32 v115, v115\nglobal_load_lds_dwordx4 %[w0], s[98:99]\nv_exp_f32 v116, v116\n" \
    "v_mfma_f32_32x32x16_bf16 v[64:79], v[170:173], v[160:163], v[64:79]\nds_read_b32 v185, v191\nv_exp_f32 v117, v117\nv_exp_f32 v118, v118\n" \
    "v_exp_f32 v119, v119\nv_exp_f32 v120, v120\nv_mfma_f32_32x32x16_bf16 v[80:95], v[170:173], v[164:167], v[80:95]\n" \
    "ds_read_b128 v[170:173], v186 offset:3072\nv_exp_f32 v121, v121\nv_exp_f32 v122, v122\nv_exp_f32 v123, v123\n" \
    "v_smfmac_f32_32x32x32_bf16 v[144:159], v[176:179], v[112:119], v184\ns_mov_b64 exec, %[vm]\nds_read_b128 v[176:179], v188 offset:2304\n" \
    "s_mov_b64 exec, -1\nv_exp_f32 v124, v124\nv_exp_f32 v125, v125\nv_exp_f32 v126, v126\nv_exp_f32 v127, v127\ns_waitcnt lgkmcnt(2)\n" \
    "v_readfirstlane_b32 s94, v185\ns_cmp_ge_u32 s94, 9161600\ns_cbranch_scc1 4f\n3:\ns_sleep 1\nds_read_b32 v185, v191\ns_waitcnt lgkmcnt(0)\n" \
    "v_readfirstlane_b32 s94, v185\ns_cmp_lt_u32 s94, 9161600\ns_cbranch_scc1 3b\n4:\ns_setprio 3\n"
#define ATT_Y7 \
    "s_setprio 3\nv_exp_f32 v64, v64\nv_exp_f32 v65, v65\nv_smfmac_f32_32x32x32_bf16 v[144:159], v[180:183], v[120:127], v184\ns_mov_b64 exec, %[vm]\n" \
    "ds_read_b128 v[180:183], v189 offset:2304\ns_mov_b64 exec, -1\nv_exp_f32 v66, v66\nv_exp_f32 v67, v67\nv_exp_f32 v68, v68\n" \
    "v_mfma_f32_32x32x4_2b_bf16 v[96:127], v[174:175], v[168:169], 0 cbsz:1\nds_read_b32 v174, v187 offset:18432\nv_exp_f32 v69, v69\nv_exp_f32 v70, v70\n" \
    "v_exp_f32 v71, v71\nv_exp_f32 v72, v72\nv_exp_f32 v73, v73\nv_exp_f32 v74, v74\nv_exp_f32 v75, v75\nv_exp_f32 v76, v76\ns_waitcnt lgkmcnt(2)\n" \
    "v_smfmac_f32_32x32x32_bf16 v[128:143], v[176:179], v[64:71], v184\nv_exp_f32 v77, v77\nv_exp_f32 v78, v78\nv_exp_f32 v79, v79\nv_exp_f32 v80, v80\n" \
    "v_exp_f32 v81, v81\ns_waitcnt lgkmcnt(1)\nv_smfmac_f32_32x32x32_bf16 v[128:143], v[180:183], v[72:79], v184\nv_exp_f32 v82, v82\nv_exp_f32 v83, v83\n" \
    "v_exp_f32 v84, v84\nv_mfma_f32_32x32x16_bf16 v[96:111], v[170:173], v[160:163], v[96:111]\nv_exp_f32 v85, v85\nv_exp_f32 v86, v86\nv_exp_f32 v87, v87\n" \
    "v_exp_f32 v88, v88\nv_mfma_f32_32x32x16_bf16 v[112:127], v[170:173], v[164:167], v[112:127]\nds_read_b128 v[170:173], v186 offset:18432\n" \
    "v_exp_f32 v89, v89\nv_exp_f32 v90, v90\nv_exp_f32 v91, v91\nv_smfmac_f32_32x32x32_bf16 v[144:159], v[176:179], v[80:87], v184\ns_mov_b64 exec, %[vm]\n" \
    "ds_read_b128 v[176:179], v188 offset:3456\ns_mov_b64 exec, -1\nv_exp_f32 v92, v92\nv_exp_f32 v93, v93\nv_exp_f32 v94, v94\nv_exp_f32 v95, v95\n" \
    "s_setprio 2\nv_exp_f32 v96, v96\nv_exp_f32 v97, v97\nv_smfmac_f32_32x32x32_bf16 v[144:159], v[180:183], v[88:95], v184\ns_mov_b64 exec, %[vm]\n" \
    "ds_read_b128 v[180:183], v189 offset:3456\ns_mov_b64 exec, -1\nv_exp_f32 v98, v98\nv_exp_f32 v99, v99\nv_exp_f32 v100, v100\ns_waitcnt lgkmcnt(3)\n" \
    "v_mfma_f32_32x32x4_2b_bf16 v[64:95], v[174:175], v[168:169], 0 cbsz:1\nds_read_b32 v174, v187 offset:18560\nv_exp_f32 v101, v101\ns_mov_b32 m0, %[mr]\n" \
    "v_exp_f32 v102, v102\nglobal_load_lds_dwordx4 %[w2], s[96:97]\nv_exp_f32 v103, v103\nv_exp_f32 v104, v104\nv_exp_f32 v105, v105\nv_exp_f32 v106, v106\n" \
    "v_exp_f32 v107, v107\nv_exp_f32 v108, v108\ns_waitcnt lgkmcnt(2)\nv_smfmac_f32_32x32x32_bf16 v[128:143], v[176:179], v[96:103], v184\n" \
    "v_exp_f32 v109, v109\nv_exp_f32 v110, v110\nv_exp_f32 v111, v111\nv_exp_f32 v112, v112\nv_exp_f32 v113, v113\ns_waitcnt lgkmcnt(1)\n" \
    "v_smfmac_f32_32x32x32_bf16 v[128:143], v[180:183], v[104:111], v184\nv_exp_f32 v114, v114\ns_add_u32 s98, s96, 248832\ns_addc_u32 s99, s97, 0\n" \
    "s_add_u32 m0, %[m1], 147456\nv_exp_f32 v115, v115\nglobal_load_lds_dwordx4 %[w0], s[98:99]\nv_exp_f32 v116, v116\n" \
    "v_mfma_f32_32x32x16_bf16 v[64:79], v[170:173], v[160:163], v[64:79]\nv_exp_f32 v117, v117\nv_exp_f32 v118, v118\nv_exp_f32 v119, v119\n" \
    "v_exp_f32 v120, v120\nv_mfma_f32_32x32x16_bf16 v[80:95], v[170:173], v[164:167], v[80:95]\nds_read_b128 v[170:173], v186 offset:19456\n" \
    "v_exp_f32 v121, v121\nv_exp_f32 v122, v122\nv_exp_f32 v123, v123\nv_smfmac_f32_32x32x32_bf16 v[144:159], v[176:179], v[112:119], v184\n" \
    "s_mov_b64 exec, %[vm]\nds_read_b128 v[176:179], v188 offset:18432\ns_mov_b64 exec, -1\nv_exp_f32 v124, v124\nv_exp_f32 v125, v125\n" \
    "v_exp_f32 v126, v126\nv_exp_f32 v127, v127\ns_setprio 1\nv_exp_f32 v64, v64\nv_exp_f32 v65, v65\n" \
    "v_smfmac_f32_32x32x32_bf16 v[144:159], v[180:183], v[120:127], v184\ns_mov_b64 exec, %[vm]\nds_read_b128 v[180:183], v189 offset:18432\n" \
    "s_mov_b64 exec, -1\nv_exp_f32 v66, v66\nv_exp_f32 v67, v67\nv_exp_f32 v68, v68\ns_waitcnt lgkmcnt(3)\n" \
    "v_mfma_f32_32x32x4_2b_bf16 v[96:127], v[174:175], v[168:169], 0 cbsz:1\nds_read_b32 v174, v187 offset:18688\nv_exp_f32 v69, v69\nv_exp_f32 v70, v70\n" \
    "v_exp_f32 v71, v71\nv_exp_f32 v72, v72\nv_exp_f32 v73, v73\nv_exp_f32 v74, v74\nv_exp_f32 v75, v75\nv_exp_f32 v76, v76\ns_waitcnt lgkmcnt(2)\n" \
    "v_smfmac_f32_32x32x32_bf16 v[128:143], v[176:179], v[64:71], v184\nv_exp_f32 v77, v77\nv_exp_f32 v78, v78\nv_exp_f32 v79, v79\nv_exp_f32 v80, v80\n" \
    "v_exp_f32 v81, v81\ns_waitcnt lgkmcnt(1)\nv_smfmac_f32_32x32x32_bf16 v[128:143], v[180:183], v[72:79], v184\nv_exp_f32 v82, v82\nv_exp_f32 v83, v83\n" \
    "v_exp_f32 v84, v84\nv_mfma_f32_32x32x16_bf16 v[96:111], v[170:173], v[160:163], v[96:111]\nv_exp_f32 v85, v85\nv_exp_f32 v86, v86\nv_exp_f32 v87, v87\n" \
    "v_exp_f32 v88, v88\nv_mfma_f32_32x32x16_bf16 v[112:127], v[170:173], v[164:167], v[112:127]\nds_read_b128 v[170:173], v186 offset:20480\n" \
    "v_exp_f32 v89, v89\nv_exp_f32 v90, v90\nv_exp_f32 v91, v91\nv_smfmac_f32_32x32x32_bf16 v[144:159], v[176:179], v[80:87], v184\ns_mov_b64 exec, %[vm]\n" \
    "ds_read_b128 v[176:179], v188 offset:19584\ns_mov_b64 exec, -1\nv_exp_f32 v92, v92\nv_exp_f32 v93, v93\nv_exp_f32 v94, v94\nv_exp_f32 v95, v95\n" \
    "s_mov_b64 exec, 1\nds_add_u32 v191, v191\ns_mov_b64 exec, -1\ns_setprio 0\nv_exp_f32 v96, v96\nv_exp_f32 v97, v97\n" \
    "v_smfmac_f32_32x32x32_bf16 v[144:159], v[180:183], v[88:95], v184\ns_mov_b64 exec, %[vm]\nds_read_b128 v[180:183], v189 offset:19584\n" \
    "s_mov_b64 exec, -1\nv_exp_f32 v98, v98\nv_exp_f32 v99, v99\nv_exp_f32 v100, v100\ns_waitcnt lgkmcnt(4)\n" \
    "v_mfma_f32_32x32x4_2b_bf16 v[64:95], v[174:175], v[168:169], 0 cbsz:1\nds_read_b32 v174, v187 offset:18816\nv_exp_f32 v101, v101\n" \
    "s_add_u32 s98, s96, 497664\ns_addc_u32 s99, s97, 0\ns_add_u32 m0, %[m1], 36864\nv_exp_f32 v102, v102\nglobal_load_lds_dwordx4 %[w0], s[98:99]\n" \
    "v_exp_f32 v103, v103\nv_exp_f32 v104, v104\nv_exp_f32 v105, v105\nv_exp_f32 v106, v106\nv_exp_f32 v107, v107\nv_exp_f32 v108, v108\n" \
    "s_waitcnt lgkmcnt(3)\nv_smfmac_f32_32x32x32_bf16 v[128:143], v[176:179], v[96:103], v184\nv_exp_f32 v109, v109\nv_exp_f32 v110, v110\n" \
    "v_exp_f32 v111, v111\nv_exp_f32 v112, v112\nv_exp_f32 v113, v113\ns_waitcnt lgkmcnt(1)\n" \
    "v_smfmac_f32_32x32x32_bf16 v[128:143], v[180:183], v[104:111], v184\nv_exp_f32 v114, v114\ns_add_u32 s98, s96, 580608\ns_addc_u32 s99, s97, 0\n" \
    "s_add_u32 m0, %[m1], 46080\nv_exp_f32 v115, v115\nglobal_load_lds_dwordx4 %[w0], s[98:99]\nv_exp_f32 v116, v116\n" \
    "v_mfma_f32_32x32x16_bf16 v[64:79], v[170:173], v[160:163], v[64:79]\nv_exp_f32 v117, v117\nv_exp_f32 v118, v118\nv_exp_f32 v119, v119\n" \
    "v_exp_f32 v120, v120\nv_mfma_f32_32x32x16_bf16 v[80:95], v[170:173], v[164:167], v[80:95]\nds_read_b128 v[170:173], v186 offset:21504\n" \
    "v_exp_f32 v121, v121\nv_exp_f32 v122, v122\nv_exp_f32 v123, v123\nv_smfmac_f32_32x32x32_bf16 v[144:159], v[176:179], v[112:119], v184\n" \
    "s_mov_b64 exec, %[vm]\nds_read_b128 v[176:179], v188 offset:20736\ns_mov_b64 exec, -1\nv_exp_f32 v124, v124\ns_add_u32 m0, %[m1], 129024\n" \
    "v_exp_f32 v125, v125\nglobal_load_lds_dwordx4 %[w1], s[96:97]\nv_exp_f32 v126, v126\nv_exp_f32 v127, v127\n"

typedef __attribute__((ext_vector_type(32))) unsigned u32x32;
typedef __attribute__((ext_vector_type(16))) unsigned u32x16;
struct Att {
    f32x32 S0, S1, O; u32x16 F; u32x8 q1; u32x2 q2; u32x4 ad; u32x2 vc;
    const char* kvsrc; unsigned m1, m2, mr;
};
#define ATT_OUT "+{v[64:95]}"(A.S0), "+{v[96:127]}"(A.S1), "+{v[128:159]}"(A.O), "+{v[170:185]}"(A.F)
#define ATT_IN(J) "{v[160:167]}"(A.q1), "{v[168:169]}"(A.q2), "{v[186:189]}"(A.ad), "{v[190:191]}"(A.vc), [vm] "s"(0x0003ffff0003ffffull), \
                  [sc] "s"(A.kvsrc + (J + 1) * CHUNK), [m2] "s"(A.m2)
#define ATT_TXT1(J) "s_mov_b32 s95, m0\n" ATT_O##J "s_mov_b32 m0, s95\n"
#define ATT_TXT(J) "s_mov_b32 s95, m0\n s_cmp_lg_u32 %[yg], 0\n s_cbranch_scc1 1f\n" ATT_O##J "s_branch 2f\n 1:\n" ATT_Y##J "2:\n s_mov_b32 m0, s95\n"
#define ATT_PLAIN(J) asm volatile(ATT_TXT1(J) : ATT_OUT : ATT_IN(J) : "memory", "scc", "s94", "s95")
#define ATT_W3(J, Wo) asm volatile(ATT_TXT(J) : ATT_OUT : ATT_IN(J), [yg] "s"(kh), [m1] "s"(A.m1), [mr] "s"(A.mr), \
                  "{s[96:97]}"(Wo), [w0] "v"(ws3.om), [w1] "v"(ws3.orm), [w2] "v"(ws3.ob) : "memory", "scc", "s94", "s95", "s98", "s99")
#define ATT_FIRST(J) asm volatile(ATT_TXT1(J) : ATT_OUT, "+{v[224:255]}"(hx), "=&{v[192:193]}"(seen) : ATT_IN(J), [to] "v"(tofs), [tw] "s"(WoB), \
                  [pa] "v"(rho * 8), [pb] "s"(ws + OFF_F1 + (h >> 1) * 256) : "memory", "scc", "s94", "s95")
#define ATT_KV(J) asm volatile(ATT_TXT(J) : ATT_OUT, "=&{v[192:223]}"(kvp), "+{v[224:255]}"(hx) : ATT_IN(J), [yg] "s"(kh), [rs] "s"(rs), \
                  [km] "s"(0x003fffff003fffffull) : "memory", "scc", "s94", "s95")
#define ATT_W3FB(J, Wo) asm volatile(ATT_TXT(J) : ATT_OUT, "+{v[192:223]}"(kvp), "+{v[224:255]}"(hx) : ATT_IN(J), [yg] "s"(kh), [m1] "s"(A.m1), \
                  [mr] "s"(A.mr), "{s[96:97]}"(Wo), [w0] "v"(ws3.om), [w2] "v"(ws3.ob), [rs] "s"(rs), \
                  [fa] "v"(fba), [fv] "v"(tagv), [fs] "s"(ws + OFF_FB + (qt0 * 32 + h) * 8) : "memory", "scc", "s94", "s95", "s98", "s99")
#define ATT_W3PK(J, Wo) asm volatile(ATT_TXT(J) : ATT_OUT, "=&{v[192:193]}"(seen) : ATT_IN(J), [yg] "s"(kh), [m1] "s"(A.m1), [mr] "s"(A.mr), \
                  "{s[96:97]}"(Wo), [w0] "v"(ws3.om), [w1] "v"(ws3.orm), [w2] "v"(ws3.ob), [pa] "v"(rho * 8), [pb] "s"(ws + OFF_FB + qt3 * 256) \
                  : "memory", "scc", "s94", "s95", "s98", "s99")
#define ATT_AF(J) asm volatile(ATT_TXT(J) : ATT_OUT, "=&{v[192:223]}"(ga0), "+{v[224:255]}"(gx) : ATT_IN(J), [yg] "s"(kh), \
                  [rs] "s"(rs), [a0] "v"(afo), [a1] "v"(afo + 4096), [ar] "v"(aro), [ag] "v"(ago) : "memory", "scc", "s94", "s95")
#define ATT_GEMM(J) asm volatile(ATT_TXT1(J) : ATT_OUT, "+{v[224:255]}"(gx), "+{v[44:59]}"(gw0), "+{v[60:63]}"(gw1) \
                  : ATT_IN(J), "{v[192:223]}"(ga0), [to] "v"(tofs), [tw] "s"(WoA) : "memory", "scc", "s94", "s95")
#define ATT_GEMMW(J, Wo) asm volatile(ATT_TXT(J) : ATT_OUT, "+{v[224:255]}"(gx), "+{v[44:59]}"(gw0), "+{v[60:63]}"(gw1) \
                  : ATT_IN(J), "{v[192:223]}"(ga0), [yg] "s"(kh), [m1] "s"(A.m1), "{s[96:97]}"(Wo), [w0] "v"(ws3.om) : "memory", "scc", "s94", "s95", "s98", "s99")

DI u32x2 o_addr(int br, int h, int qt, int lane) {
    const int hh = lane >> 5, rho = lane & 31;
    const int tok = (((rho >> 2) & 1) << 4) + ((rho >> 3) << 2) + (rho & 3);
    const unsigned base = OFF_O + (br * 128 + 2 * qt + (tok >> 4)) * 16384 + (tok & 15) * 16;
    return (u32x2){base + (h >> 1) * 1024 + (2 * (h & 1) + hh) * 256, hh ? 2 * WS_USED : OFF_OR + ((br * 64 + qt) * 32 + h) * 128 + tok * 4};
}
DI void store_o(rsrc_t rs, const f32x16& o, u32x2 oa) {
    auto dn = __builtin_amdgcn_permlane32_swap(__float_as_uint(o[10]), __float_as_uint(o[10]), false, false);
    const float sc = __builtin_amdgcn_rcpf(__uint_as_float(dn[0]));
    u32x4 v = {pk(o[0] * sc, o[1] * sc), pk(o[2] * sc, o[3] * sc), pk(o[4] * sc, o[5] * sc), pk(o[6] * sc, o[7] * sc)};
    unsigned v8 = pk(o[8] * sc, o[9] * sc);
#pragma unroll
    for (int i = 0; i < 4; i++) v[i] = v[i] < UNSET ? v[i] : UNSET - 1;
    v8 = v8 < UNSET ? v8 : UNSET - 1;
    st128(rs, oa[0], v);
    st32(rs, oa[1], v8);
}

DI unsigned w3off(int res, bool rem, int cb, int lane) {
    const unsigned u = res * 64 + lane, lr = (u * 7282u) >> 16, pc = u - 9 * lr;
    const unsigned k = (lr & ~7u) | ((lr & 1u) << 2) | ((lr >> 1) & 3u);
    const unsigned hd = rem ? k >> 1 : k >> 4;
    const unsigned d = rem ? ((hd & 1) ? 0 : 16) + (k & 1) : 2 * (hd & 1) + (k & 3) + 2 * (k & 4) + ((k >> 1) & 4);
    return (18 * hd + d) * 1152 + cb * 144 + pc * 16;
}
struct W3Src { unsigned om, orm, ob; };
DI W3Src w3src(int cb, int lane, int w) { return W3Src{w3off(w, false, cb, lane), w3off(w, true, cb, lane), w3off(8, false, cb, lane) + 72 * 1152 * w}; }
DI void load_chunk(const char* kvsrc, int c, int slot, int lane, int lw) {
#pragma unroll
    for (int i = 0; i < 4; i++) glds16_dev(lane * 16, kvsrc + c * CHUNK + (lw + 4 * i) * 1024, slot * CHUNK + (lw + 4 * i) * 1024);
    if (lw < 2) glds16_dev(lane * 16, kvsrc + c * CHUNK + 16384 + lw * 1024, slot * CHUNK + 16384 + lw * 1024);
}
DI bf16x8 w3frag(const u32x4& A, int i, int t) {
    const unsigned a = A[(t == 4) + 2 * (i == 8)] + (i < 8 ? i * 4608 : 0) + (t < 4 ? 32 * t : 0);
    return trfrag(a, a + 144);
}
DI u32x4 w3addr(int kh, int lane) {
    const int g = lane >> 4, m = lane & 15;
    const unsigned base = L_W3 + (g * 8 + (m >> 2) * 2) * 144, a0 = base + 8 * (m & 3), a1 = base + 128 + 8 * (m & 1);
    u32x4 A = {a0 + kh * 36864, a1 + kh * 36864, a0 + (16 + kh) * 4608, a1 + (16 + kh) * 4608};
    return A;
}
DI void load_af(u32x4 (&af)[9], rsrc_t rs, int br, int rtile, int kh, int lane) {
    const unsigned a = OFF_O + (br * 128 + rtile) * 16384 + kh * 8192 + lane * 16;
    const unsigned o = OFF_OR + ((br * 64 + (rtile >> 1)) * 32 + 16 * kh + 4 * (lane >> 4)) * 128 + (16 * (rtile & 1) + (lane & 15)) * 4;
#pragma unroll
    for (int i = 0; i < 8; i++) af[i] = ld128(rs, a + i * 1024);
#pragma unroll
    for (int j = 0; j < 4; j++) af[8][j] = ld32(rs, o + j * 128);
}
__global__ void __launch_bounds__(512, 2) fused(const char* X, const char* WqA, const char* WqB, const char* WoA, const char* WoB,
                                                 const unsigned* gw, char* ws, char* out) {
    const int tid = threadIdx.x, lane = tid & 63, w = __builtin_amdgcn_readfirstlane(tid >> 6);
    const int b = blockIdx.x, xcd = b & 7, idx = b >> 3;
    const rsrc_t rs = __builtin_amdgcn_make_buffer_rsrc(ws, 0, WS_USED, 0x00020000);
    u64 pc0; asm volatile("s_getpc_b64 %0" : "=s"(pc0));
    const int h = 8 * ((xcd + 1) & 3) + (xcd & 4) + (idx & 3), qb = idx >> 2;
    const int rb = xcd * 4 + (idx >> 3), cb = idx & 7;
    const int qg = w & 3, kh = w >> 2, hh = lane >> 5, rho = lane & 31, g = lane >> 4, m = lane & 15;
    const bool young = kh;
    const char* kvsrc = ws + OFF_KV + (size_t)h * KV_HEAD;
    const int qt0 = 8 * qb + 2 * qg;
    const int br = idx & 1, quad = (xcd & 3) * 2 + ((idx >> 1) & 1), r = (xcd >> 2) * 8 + (idx >> 2);
    const int rt = w & 3, qt3 = 2 * rb + (rt >> 1);
    unsigned su[25];
    auto setup = [&]() {
        const unsigned bV = 9216 + kh * 4608 + (rho < 16 ? hh * 256 + rho * 16 : 768 + hh * 32 + rho * 16);
        su[2] = bV; su[3] = bV + (rho < 8 ? 640 : rho < 16 ? 384 : 64);
        const W3Src w3 = w3src(cb, lane, w);
        su[4] = w3.om; su[5] = w3.orm; su[6] = w3.ob;
        const unsigned tl = (idx * 8 + w) * 21 + (lane < 21 ? lane : 0);
        su[7] = (tl < 5184 ? tl : 0) * 128;
        const u32x2 ad = o_addr(0, h, qt0, lane);
        su[15] = OFF_O + (128 + rb * 4 + rt) * 16384 + kh * 8192 + lane * 16;
        su[16] = OFF_OR + ((64 + qt3) * 32 + 16 * kh + 4 * g) * 128 + (16 * (rt & 1) + m) * 4; su[17] = OFF_GATE + (rb * 64 + rt * 16 + m) * 4;
        const u32x4 wa = w3addr(kh, lane);
        su[18] = wa[0]; su[19] = wa[1];
        su[22] = ad[0] + kh * 32768; su[23] = ad[1] + kh * 4096; su[24] = w3off(8, true, cb, lane);
        if (young) {
            const unsigned e = rho < 19 ? rho : 18;
            su[8] = OFF_Q1 + (32 + h) * 65536 + qt0 * 1024 + lane * 16; su[9] = OFF_Q2 + (32 + h) * 8192 + qt0 * 128 + lane * 4;
            su[10] = ad[0]; su[11] = ad[1]; su[12] = ad[0] + 32768; su[13] = L_KVT + e * 32 + hh * 16; su[14] = L_KVT + 640 + e * 4;
        } else {
            const unsigned o = rho < 22 ? OFF_KVP + (h * 16 + hh) * 1408 + (qg * 22 + rho) * 16 : WS_USED, u = qg * 22 + rho;
            su[8] = o; su[9] = o + 5632; su[10] = o + 11264; su[11] = o + 16896; su[12] = L_KVT + u * 8 + hh * 4 + (u < 76 ? 0 : 32); su[13] = 0; su[14] = 0;
        }
#pragma unroll
        for (int i = 2; i < 25; i++) if (i != 20 && i != 21 && (young || (i != 13 && i != 14))) OPAQUE(su[i]);
    };
    u64 tag;
    Att A;
    unsigned pf = 0;
    if (__builtin_expect(young, 1)) {
        const unsigned c2 = 2016 + quad * 144, c0 = br ? quad * 144 : (quad & 1) * 1152 + (quad >> 1) * 288, c1 = br ? c0 + 1008 : c0;
        const int kg = (idx >> 2) + (idx & 3);
        const u64 kord = (kg & 1) ? 0x785634120ull : 0x876543210ull;
        p1_loader(ws, X, br ? WqB : WqA, c0, c1, c2, r, kord, (idx >> 2) == 7 ? 3 : idx >> 2, idx & 3, lane, qg);
        unsigned pg = 0;
        {
            const unsigned o = br ? (lane < 3 ? OFF_KVP + ((4 * quad + qg) * 16 + r) * 1408 + 1024 + lane * 128 : WS_USED)
                                  : (lane == 0 && (b >> 4) * 32 + (b & 7) * 4 + qg < OFF_KVP / 128 ? ((b >> 4) * 32 + (b & 7) * 4 + qg) * 128 : WS_USED);
            asm volatile("buffer_load_dword %0, %1, %2, 0 offen sc1" : "+v"(pg) : "v"(o), "s"(rs) : "memory");
        }
        tag = make_tag(read_epoch());
        {
            const int row = 256 * qb + 8 * h + 2 * qg + hh;
            const unsigned* xr = (const unsigned*)(X + (size_t)row * 1152) + rho;
            float s = 0.f;
#pragma unroll
            for (int i = 0; i < 9; i++) s = dot2(xr[32 * i], gw[rho + 32 * i], s);
            s = row_sum(s); s = swap16_add(s, s);
            asm volatile("" :: "v"(pg), "v"(s));
            if (rho == 0) st32(rs, OFF_GATE + row * 4, __float_as_uint(__builtin_amdgcn_rcpf(1.f + __expf(-s))));
        }
        setup();
    } else {
        const unsigned cofs = ((tid & 127) * 4 + (idx & 3)) * 128;
        if (w < 2) asm volatile("global_load_dword %0, %1, %2" : "+v"(pf) : "v"(cofs < CODE_BYTES ? cofs : 0), "s"(pc0) : "memory");
        if (tid < 8) lds<unsigned>(L_FLAG)[tid] = 0;
        setup();
        const unsigned pay = br ? p1_consumer<2>(rs, quad, r, lane, w)
                                : (quad & 1) ? p1_consumer<1>(rs, quad, r, lane, w) : p1_consumer<0>(rs, quad, r, lane, w);
        tag = make_tag(read_epoch());
        if (!br) {
            WAITV();
            if (lane < 2) __hip_atomic_store((gu64*)(size_t)(ws + OFF_FA) + (4 * (quad >> 1) + (w >> 1) + 2 * lane) * 64 + (quad & 1) * 32 + 2 * r + (w & 1), tag | pay, RLX_AGENT);
        }
    }
    {
        const unsigned vinit = rho == 18 ? ONES : 0u;
        asm volatile(".set r_, 120\n .rept 20\n v_mov_b64 v[r_:r_+1], 0\n .set r_, r_+2\n .endr\n"
                     ".set r_, 176\n .rept 8\n v_mov_b32 v[r_], %4\n .set r_, r_+1\n .endr\n"
                     "v_mov_b32 v175, 0x3f803f80\n v_mov_b32 v184, 0xdddddddd\n"
                     : "={v[64:95]}"(A.S0), "={v[96:127]}"(A.S1), "={v[128:159]}"(A.O), "=&{v[170:185]}"(A.F) : "{v192}"(vinit));
    }
    A.ad = (u32x4){kh * 4096u + lane * 16u, 8192u + kh * 512u + rho * 4u, su[2], su[3]};
    A.kvsrc = kvsrc; A.m1 = w * 1024; A.m2 = (w < 6 ? w : w - 2) * 3072; A.mr = L_W3 + w * 9216 + 8192;
    A.vc = (u32x2){lane * 16 + A.m2, L_FLAG + 16};
    asm volatile("" : "+{v[186:189]}"(A.ad), "+{v[190:191]}"(A.vc));
    u32x8 keep = {su[4], su[5], su[6], su[7], su[22], su[23], su[24], su[15]};
    asm volatile("" : "+{v[36:43]}"(keep));
    W3Src ws3 = {keep[0], keep[1], keep[2]};
    const unsigned tofs = keep[3];
    u32x32 hx;
    asm volatile("v_mov_b32 v224, %1\n\tv_mov_b32 v225, %2\n\tv_mov_b32 v226, %3\n\tv_mov_b32 v227, %4\n\tv_mov_b32 v228, %5\n\tv_add_u32 v229, 4096, %4\n\t"
                 "v_mov_b32 v240, %6\n\tv_mov_b32 v241, %7\n\tv_add_u32 v242, 0x200000, %3\n\tv_add_u32 v243, 0x40000, %4\n\t"
                 "v_add_u32 v244, 0x208000, %3\n\tv_add_u32 v245, 0x41000, %4"
                 : "=&{v[224:255]}"(hx) : "v"(su[8]), "v"(su[9]), "v"(su[10]), "v"(su[11]), "v"(su[12]), "v"(su[13]), "v"(su[14]));
    unsigned fba = lane * 256; u64 tagv = tag;
    OPAQUE(fba); OPAQUE(tagv);
    if (young) {
        volatile LAS unsigned* go = lds<unsigned>(L_FLAG + 12);
        unsigned kmax;
        if (qg == 0) {
            const u64 f = wait_flags<63, 4>(ws + OFF_FA + (h >> 1) * 512, tag, ~tag, lane);
            kmax = swap32_umax(swap16_umax(row_umax((((unsigned)f >> (12 * (h & 1))) & 0xfffu) << 19))) | 1u;
            *go = kmax;
        } else {
            while ((kmax = *go) == 0) __builtin_amdgcn_s_sleep(1);
            asm volatile("" ::: "memory");
        }
        const u32x4 q1b = ld128(rs, OFF_Q1 + h * 65536 + qt0 * 1024 + lane * 16), q1a = ld128(rs, OFF_Q1 + h * 65536 + qt0 * 1024 + 1024 + lane * 16);
        const unsigned q2r = ld32(rs, OFF_Q2 + h * 8192 + qt0 * 128 + (lane ^ 32) * 4);
        load_chunk(kvsrc, 0, 0, lane, qg);
        const float n = dot2(q2r, q2r, swap32_add(nrm8(q1a), nrm8(q1b)));
        const float mneg = -1.01f * __builtin_amdgcn_sqrtf(n * __uint_as_float(kmax));
        A.q2 = (u32x2){q2r, pk(mneg, 0.f)};
        A.q1 = __builtin_shufflevector(q1a, q1b, 0, 1, 2, 3, 4, 5, 6, 7);
        lds<u32x4>(L_EX)[qg * 128 + lane] = q1b; lds<u32x4>(L_EX)[qg * 128 + 64 + lane] = q1a; lds<u32x2>(L_EX + 8192)[qg * 64 + (lane ^ 32)] = A.q2;
    } else if (br) post_flag(ws + OFF_F1 + ((quad * 2 + (w >> 1)) * 32 + 2 * r + (w & 1)) * 8, tag, lane);
    asm volatile("" :: "v"(pf));
    WAITV_BAR(0);
    u64 seen;
    if (!young) {
        const u32x4 q1a = lds<u32x4>(L_EX)[qg * 128 + lane], q1b = lds<u32x4>(L_EX)[qg * 128 + 64 + lane];
        A.q1 = __builtin_shufflevector(q1a, q1b, 0, 1, 2, 3, 4, 5, 6, 7); A.q2 = lds<u32x2>(L_EX + 8192)[qg * 64 + lane];
    }
    ATT_FIRST(0);
    wait_flags<31, 4>(ws + OFF_F1 + (h >> 1) * 256, tag, seen, lane);
    if (w == 7) glds16(keep[6], WoB, L_W3 + 80 * 1024);
    u32x32 kvp;
    ATT_KV(1);
    ATT_W3FB(2, WoB);
    ATT_W3PK(3, WoB);
    wait_flags<31, 4>(ws + OFF_FB + qt3 * 256, tag, seen, lane);
    if (b == 0 && w == 0) __hip_atomic_store((gu64*)(size_t)(ws + OFF_EPOCH), read_epoch() + 1, RLX_AGENT);
    const unsigned afo = keep[7], aro = su[16], ago = su[17];
    f32x32 gx;
    {
        asm volatile("v_mov_b32 v248, %1\n\tv_mov_b32 v249, %2\n\tv_add_u32 v250, %3, %1\n\tv_add_u32 v251, %3, %2"
                     : "=&{v[224:255]}"(gx) : "v"(su[18]), "v"(su[19]), "s"(73728 - kh * 32256));
    }
    u32x32 ga0; u32x16 gw0; u32x4 gw1;
    ATT_AF(4);
    asm volatile("" : "={v[44:59]}"(gw0), "={v[60:63]}"(gw1));
    ATT_GEMM(5);
    ATT_GEMMW(6, WoA);
    const float gate = gx[30];
    if (w == 4) glds16(keep[6], WoA, L_W3 + 80 * 1024);
    const u32x2 oad = {keep[4], keep[5]};
    unsigned exw = A.vc[0] + (w * 2048 - A.m2), exr = exw ^ 8192, ex3 = (A.ad[1] & 127) * 4 + w * 512;
    OPAQUE(exw); OPAQUE(exr); OPAQUE(ex3);
    A.mr = ((w & 2) ? ((w & 4) ? 36864 : L_EX) + (w & 1) * 9216 : L_W3 + w * 9216) + 8192;
    ATT_W3(7, WoA);
    ATT_PLAIN(8);
    {
    f32x16 Oa = __builtin_shufflevector(A.O, A.O, 0, 1, 2, 3, 4, 5, 6, 7, 8, 9, 10, 11, 12, 13, 14, 15);
    f32x16 Ob = __builtin_shufflevector(A.O, A.O, 16, 17, 18, 19, 20, 21, 22, 23, 24, 25, 26, 27, 28, 29, 30, 31);
    f32x4 xs[3];
    {
        auto give = [&]() {
            *lds<f32x4>(exw) = sub4(Ob, 0); *lds<f32x4>(exw + 1024) = sub4(Ob, 1);
            if ((exw & 512) == 0) *lds<f32x4>(ex3 + L_EX3) = sub4(Ob, 2);
        };
        auto take = [&]() { xs[2] = *lds<f32x4>((ex3 ^ 2048) + L_EX3); xs[0] = *lds<f32x4>(exr); xs[1] = *lds<f32x4>(exr + 1024); };
        give();
        const bool ok = Oa[10] >= 9.094947e-13f && Oa[10] <= 1.1529215e18f && Ob[10] >= 9.094947e-13f && Ob[10] <= 1.1529215e18f;
        if ((unsigned)__builtin_amdgcn_ballot_w64(ok) != 0xffffffffu) *(volatile LAS unsigned*)lds<unsigned>(L_FLAG) = 1;
        LBAR();
        const unsigned redo = *(volatile LAS unsigned*)lds<unsigned>(L_FLAG);
        take();
        if (__builtin_expect(redo != 0, 0)) {
            const int lane = fresh_lane(), hh = lane >> 5, rho = lane & 31;
            const unsigned bK = A.ad[0], bK2 = A.ad[1];
            const u32x2 b2 = {A.q2[0], 0u};
            const u32x4 q1a = __builtin_shufflevector(A.q1, A.q1, 0, 1, 2, 3), q1b = __builtin_shufflevector(A.q1, A.q1, 4, 5, 6, 7);
            float xa = -INFINITY, xb = -INFINITY;
            for (int pass = 0; pass < 2; pass++) {
                for (int c = 0; c < 8; c++) {
                    LBAR();
                    if (young) load_chunk(kvsrc, c, 0, lane, qg);
                    WAITV_BAR(0);
                    for (int t = 0; t < 4; t++) {
                        const bf16x8 kf = ldsr128(bK + t * 1024);
                        const u32x2 k2 = {*lds<unsigned>(bK2 + t * 128), 0u};
                        f32x32 s = {};
                        s = __builtin_amdgcn_mfma_f32_32x32x4bf16_1k(__builtin_bit_cast(s16x4, k2), __builtin_bit_cast(s16x4, b2), s, 1, 0, 0);
                        f32x16 sa = __builtin_shufflevector(s, s, 0, 1, 2, 3, 4, 5, 6, 7, 8, 9, 10, 11, 12, 13, 14, 15);
                        f32x16 sb = __builtin_shufflevector(s, s, 16, 17, 18, 19, 20, 21, 22, 23, 24, 25, 26, 27, 28, 29, 30, 31);
                        sa = __builtin_amdgcn_mfma_f32_32x32x16_bf16(kf, __builtin_bit_cast(bf16x8, q1a), sa, 0, 0, 0);
                        sb = __builtin_amdgcn_mfma_f32_32x32x16_bf16(kf, __builtin_bit_cast(bf16x8, q1b), sb, 0, 0, 0);
                        if (pass == 0) {
#pragma unroll
                            for (int i = 0; i < 16; i++) { xa = fmaxf(xa, sa[i]); xb = fmaxf(xb, sb[i]); }
                        } else {
#pragma unroll
                            for (int i = 0; i < 16; i++) { sa[i] = __builtin_amdgcn_exp2f(sa[i] - xa); sb[i] = __builtin_amdgcn_exp2f(sb[i] - xb); }
#pragma unroll
                            for (int st = 0; st < 2; st++) {
                                u32x4 vf = {ONES, ONES, ONES, ONES};
                                if (rho < 18) vf = __builtin_bit_cast(u32x4, ldsr128(A.ad[2 + st] + t * 1152));
                                f32x8 pa, pb;
#pragma unroll
                                for (int i = 0; i < 8; i++) { pa[i] = sa[8 * st + i]; pb[i] = sb[8 * st + i]; }
                                Oa = __builtin_amdgcn_smfmac_f32_32x32x32_bf16(__builtin_bit_cast(bf16x8, vf), __builtin_bit_cast(bf16x16, pa), Oa, (int)0xdddddddd, 0, 0);
                                Ob = __builtin_amdgcn_smfmac_f32_32x32x32_bf16(__builtin_bit_cast(bf16x8, vf), __builtin_bit_cast(bf16x16, pb), Ob, (int)0xdddddddd, 0, 0);
                            }
                        }
                    }
                }
                if (pass == 0) {
                    xa = swap32_max(xa); xb = swap32_max(xb);
                    LAS float* mx = lds<float>(L_EX3);
                    mx[w * 64 + lane] = hh ? xb : xa;
                    LBAR();
                    xa = fmaxf(xa, mx[(w ^ 4) * 64 + 32 + rho]); xb = fmaxf(xb, mx[(w ^ 4) * 64 + rho]);
#pragma unroll
                    for (int i = 0; i < 16; i++) { Oa[i] = 0.f; Ob[i] = 0.f; }
                }
            }
            LBAR();
            give();
            LBAR();
            take();
        }
    }
#pragma unroll
    for (int i = 0; i < 11; i++) Oa[i] += xs[i >> 2][i & 3];
    store_o(rs, Oa, oad);
    const int lane = fresh_lane(), g = lane >> 4, m = lane & 15;
    const u32x4 WA = __builtin_bit_cast(u32x4, (f32x4){gx[24], gx[25], gx[26], gx[27]});
    f32x4 pb[5];
    {
        bf16x8 wb[4][5];
#pragma unroll
        for (int i = 0; i < 4; i++)
#pragma unroll
            for (int t = 0; t < 5; t++) wb[i][t] = w3frag(WA, 4 + i, t);
        __builtin_amdgcn_sched_barrier(0);
#pragma unroll
        for (int t = 0; t < 5; t++) pb[t] = (f32x4){gx[4 + 4 * t], gx[5 + 4 * t], gx[6 + 4 * t], gx[7 + 4 * t]};
#pragma unroll
        for (int i = 0; i < 4; i++)
#pragma unroll
            for (int t = 0; t < 5; t++)
                pb[t] = mfma16(wb[i][t], __builtin_bit_cast(bf16x8, (u32x4){ga0[16 + 4 * i], ga0[17 + 4 * i], ga0[18 + 4 * i], ga0[19 + 4 * i]}), pb[t]);
    }
#pragma unroll
    for (int t = 0; t < 5; t++) { pb[t] *= 1.f - gate; OPAQUE(pb[t]); }
    asm volatile("s_waitcnt vmcnt(2)\n\ts_barrier" ::: "memory");
    u32x4 af[9];
    load_af(af, rs, 0, rb * 4 + rt, kh, lane);
    __builtin_amdgcn_sched_barrier(0);
    u32x4 WA2 = WA + (kh ? 36864u - (L_W3 + 55296u) : L_EX - (L_W3 + 18432u));
    OPAQUE(WA2);
    bf16x8 wf[7][5];
#pragma unroll
    for (int i = 0; i < 7; i++)
#pragma unroll
        for (int t = 0; t < 5; t++) wf[i][t] = w3frag(i < 4 ? WA : WA2, i, t);
    unsigned redw = (rt * 5 + (kh ? 0 : 3)) * 1024 + lane * 16, redr = (rt * 5 + (kh ? 3 : 0)) * 1024 + lane * 16;
    unsigned oofs = (rb * 64 + rt * 16 + m) * 1152 + cb * 144 + g * 8 + kh * 96;
    OPAQUE(redw); OPAQUE(redr); OPAQUE(oofs);
    __builtin_amdgcn_sched_barrier(0);
    {
        unsigned mx = 0;
#pragma unroll
        for (int i = 0; i < 9; i++) mx = umax(umax(mx, umax(af[i][0], af[i][1])), umax(af[i][2], af[i][3]));
        if (__any(mx == UNSET)) {
            const unsigned a = OFF_O + (rb * 4 + rt) * 16384 + kh * 8192 + lane * 16;
            const unsigned o = OFF_OR + (qt3 * 32 + 16 * kh + 4 * g) * 128 + (16 * (rt & 1) + m) * 4;
            const u64 t0 = __builtin_amdgcn_s_memrealtime();
            for (bool again = true; again && __builtin_amdgcn_s_memrealtime() - t0 < 10000000ull;) {
                again = false;
#pragma unroll
                for (int i = 0; i < 9; i++)
                    if (__any(umax(umax(af[i][0], af[i][1]), umax(af[i][2], af[i][3])) == UNSET)) {
                        again = true;
                        if (i < 8) af[i] = ld128(rs, a + i * 1024);
                        else {
#pragma unroll
                            for (int j = 0; j < 4; j++) af[8][j] = ld32(rs, o + j * 128);
                        }
                    }
                asm volatile("" ::: "memory");
            }
        }
    }
    f32x4 acc[5];
#pragma unroll
    for (int i = 0; i < 9; i++) {
#pragma unroll
        for (int t = 0; t < 5; t++) acc[t] = mfma16(wf[i % 7][t], __builtin_bit_cast(bf16x8, af[i]), i ? acc[t] : (f32x4){0, 0, 0, 0});
        if (i < 2) {
#pragma unroll
            for (int t = 0; t < 5; t++) wf[i][t] = w3frag(i ? WA : WA2, 7 + i, t);
        }
        __builtin_amdgcn_sched_barrier(0);
    }
#pragma unroll
    for (int t = 0; t < 5; t++) acc[t] = acc[t] * gate + pb[t];
    if (kh) {
#pragma unroll
        for (int t = 0; t < 3; t++) *lds<f32x4>(redw + t * 1024) = acc[t];
        LBAR();
        const f32x4 v3 = acc[3] + *lds<f32x4>(redr), v4 = acc[4] + *lds<f32x4>(redr + 1024);
        *(u32x2*)(out + oofs) = (u32x2){pk(v3[0], v3[1]), pk(v3[2], v3[3])};
        if (g < 2) *(u32x2*)(out + oofs + 32) = (u32x2){pk(v4[0], v4[1]), pk(v4[2], v4[3])};
    } else {
#pragma unroll
        for (int t = 0; t < 2; t++) *lds<f32x4>(redw + t * 1024) = acc[3 + t];
        LBAR();
        f32x4 v[3];
#pragma unroll
        for (int t = 0; t < 3; t++) v[t] = acc[t] + *lds<f32x4>(redr + t * 1024);
#pragma unroll
        for (int t = 0; t < 3; t++) *(u32x2*)(out + oofs + t * 32) = (u32x2){pk(v[t][0], v[t][1]), pk(v[t][2], v[t][3])};
    }
    }
}

extern "C" void kernel_launch(void* const* d_in, const int* in_sizes, int n_in, void* d_out, int out_size, void* d_ws, size_t ws_size,
                              hipStream_t stream) {
    (void)hipFuncSetAttribute((const void*)fused, hipFuncAttributeMaxDynamicSharedMemorySize, LDS_BYTES);
    fused<<<256, 512, LDS_BYTES, stream>>>((const char*)d_in[0], (const char*)d_in[1], (const char*)d_in[4], (const char*)d_in[2],
                                          (const char*)d_in[5], (const unsigned*)d_in[6], (char*)d_ws, (char*)d_out);
}
```
